```python
import torch

def _softmax(x, axis=-1):
    return torch.softmax(x, dim=axis)
def _gelu(x):
    return torch.nn.functional.gelu(x, approximate="tanh")
def _silu(x):
    return torch.nn.functional.silu(x)
def _sigmoid(x):
    return torch.sigmoid(x)
def _layernorm(x, axis=-1, eps=1e-5):
    m = x.mean(dim=axis, keepdim=True)
    v = x.var(dim=axis, keepdim=True, unbiased=False)
    return (x - m) / torch.sqrt(v + eps)
def _scatter_add(tgt, idx, src, axis=0):
    return tgt.clone().index_add_(axis, idx, src)
def _segment_sum(src, seg, n):
    out = torch.zeros((n,) + src.shape[1:], dtype=src.dtype)
    return out.index_add_(0, seg, src)
def _pad(x, pw):
    flat = tuple(v for pair in reversed(pw) for v in pair)
    return torch.nn.functional.pad(x, flat)

kernel_name = 'gated_mix__compressed_kv_cla__dense_activation_silu__decode'

def setup_inputs():

    _w = (lambda a: (lambda t: t.to(torch.bfloat16) if t.is_floating_point() else t)(torch.as_tensor(a)))
    def _A_setup():
        B, H, Sq, Sk, D, M = 1, 32, 1, 4096, 128, 640
        Dc = 4 * D
        return {
            "x": _w((lambda *s: torch.randn(*s, dtype=torch.bfloat16) * 0.05)(B, Sq, M)),
            "c_kv": _w((lambda *s: torch.randn(*s, dtype=torch.bfloat16) * 0.2)(B, Sk, Dc)),
            "w_q": _w((lambda *s: torch.randn(*s, dtype=torch.bfloat16) * 3.2)(M, H, D) * (M ** -0.5)),
            "w_uk": _w((lambda *s: torch.randn(*s, dtype=torch.bfloat16) * 3.2)(Dc, H, D) * (Dc ** -0.5)),
            "w_uv": _w((lambda *s: torch.randn(*s, dtype=torch.bfloat16) * 0.2)(Dc, H, D) * (Dc ** -0.5)),
            "w_o": _w((lambda *s: torch.randn(*s, dtype=torch.bfloat16) * 0.2)(H, D, M) * ((H * D) ** -0.5)),
        }
    _A_in = _A_setup()

    def _B_setup():
        B, S, D, F = 1, 1, 640, 1728
        return {
            "x": _w((lambda *s: torch.randn(*s, dtype=torch.bfloat16) * 0.05)(B, S, D)),
            "w1": _w((lambda *s: torch.randn(*s, dtype=torch.bfloat16) * 0.05)(D, F) * (D ** -0.5)),
            "w2": _w((lambda *s: torch.randn(*s, dtype=torch.bfloat16) * 0.05)(F, D) * (F ** -0.5)),
        }
    _B_in = _B_setup()

    return {
        "A_x": _A_in["x"],
        "A_c_kv": _A_in["c_kv"],
        "A_w_q": _A_in["w_q"],
        "A_w_uk": _A_in["w_uk"],
        "A_w_uv": _A_in["w_uv"],
        "A_w_o": _A_in["w_o"],
        "B_x": _B_in["x"],
        "B_w1": _B_in["w1"],
        "B_w2": _B_in["w2"],
        "gate_w": _w((lambda *s: torch.randn(*s, dtype=torch.bfloat16) * 0.05)(640) * 16.0),
    }

def reference(A_x, A_c_kv, A_w_q, A_w_uk, A_w_uv, A_w_o, B_x, B_w1, B_w2, gate_w):
    def _A_ref(x, c_kv, w_q, w_uk, w_uv, w_o):
        B, Sq, M = x.shape[0], x.shape[1], x.shape[2]
        Sk = c_kv.shape[1]
        H, D = w_q.shape[1], w_q.shape[2]
        q = torch.einsum("bqm,mhd->bhqd", x, w_q)
        k = torch.einsum("bkc,chd->bhkd", c_kv, w_uk)
        v = torch.einsum("bkc,chd->bhkd", c_kv, w_uv)
        scores = torch.einsum("bhqd,bhkd->bhqk", q, k) * (D ** -0.5)
        off = Sk - Sq
        qi = torch.arange(Sq)[:, None]
        kj = torch.arange(Sk)[None, :]
        cmask = kj <= (qi + off)
        scores = torch.where(cmask[None, None], scores, -1e9)
        p = _softmax(scores, axis=-1)
        ctx = torch.einsum("bhqk,bhkd->bhqd", p, v)
        return torch.einsum("bhqd,hdm->bqm", ctx, w_o)

    def _B_ref(x, w1, w2):
        h = torch.einsum("bsd,df->bsf", x, w1)
        h = _silu(h)
        out = torch.einsum("bsf,fd->bsd", h, w2)
        return out

    x = A_x
    g = _sigmoid((x * gate_w).sum(-1, keepdims=True))
    a = _A_ref(x, A_c_kv, A_w_q, A_w_uk, A_w_uv, A_w_o)
    b = _B_ref(x, B_w1, B_w2)
    return g * a + (1 - g) * b
```

```cpp
#include <hip/hip_runtime.h>
#include <hip/hip_bf16.h>
#include <stdint.h>

typedef unsigned short u16;
static __device__ __forceinline__ float bf2f(u16 b) { return __uint_as_float(((unsigned)b) << 16); }
static __device__ __forceinline__ u16 f2bf(float f) { unsigned u = __float_as_uint(f); u += 0x7FFFu + ((u >> 16) & 1u); return (u16)(u >> 16); }

constexpr int H = 32, D = 128, SK = 4096, M = 640, DC = 512, F = 1728;

__global__ void k_q(const u16* x, const u16* wq, float* q) {
  int h = blockIdx.x, d = threadIdx.x;
  float acc = 0.f;
  for (int m = 0; m < M; ++m) acc += bf2f(x[m]) * bf2f(wq[(size_t)m * H * D + h * D + d]);
  q[h * D + d] = acc;
}
__global__ void k_qabs(const float* q, const u16* wuk, float* qa) {
  int h = blockIdx.x, c = threadIdx.x;
  float acc = 0.f;
  for (int d = 0; d < D; ++d) acc += q[h * D + d] * bf2f(wuk[(size_t)c * H * D + h * D + d]);
  qa[h * DC + c] = acc * 0.08838834764831845f;
}
__global__ void k_scores(const float* qa, const u16* ckv, float* s) {
  int h = blockIdx.x, k = blockIdx.y * 256 + threadIdx.x;
  float acc = 0.f;
  for (int c = 0; c < DC; ++c) acc += qa[h * DC + c] * bf2f(ckv[(size_t)k * DC + c]);
  s[h * SK + k] = acc;
}
__global__ void k_softmax(float* s) {
  __shared__ float red[256];
  int h = blockIdx.x, t = threadIdx.x;
  float m = -1e30f;
  for (int k = t; k < SK; k += 256) m = fmaxf(m, s[h * SK + k]);
  red[t] = m; __syncthreads();
  for (int o = 128; o > 0; o >>= 1) { if (t < o) red[t] = fmaxf(red[t], red[t + o]); __syncthreads(); }
  m = red[0]; __syncthreads();
  float l = 0.f;
  for (int k = t; k < SK; k += 256) { float e = __expf(s[h * SK + k] - m); s[h * SK + k] = e; l += e; }
  red[t] = l; __syncthreads();
  for (int o = 128; o > 0; o >>= 1) { if (t < o) red[t] += red[t + o]; __syncthreads(); }
  l = red[0];
  for (int k = t; k < SK; k += 256) s[h * SK + k] /= l;
}
__global__ void k_pv(const float* p, const u16* ckv, float* cl) {
  int h = blockIdx.x, c = threadIdx.x;
  float acc = 0.f;
  for (int k = 0; k < SK; ++k) acc += p[h * SK + k] * bf2f(ckv[(size_t)k * DC + c]);
  cl[h * DC + c] = acc;
}
__global__ void k_v(const float* cl, const u16* wuv, float* ctx) {
  int h = blockIdx.x, d = threadIdx.x;
  float acc = 0.f;
  for (int c = 0; c < DC; ++c) acc += cl[h * DC + c] * bf2f(wuv[(size_t)c * H * D + h * D + d]);
  ctx[h * D + d] = acc;
}
__global__ void k_h(const u16* x, const u16* w1, float* hh) {
  int f = blockIdx.x * 64 + threadIdx.x;
  float acc = 0.f;
  for (int m = 0; m < M; ++m) acc += bf2f(x[m]) * bf2f(w1[(size_t)m * F + f]);
  hh[f] = acc / (1.f + __expf(-acc));
}
__global__ void k_out(const u16* x, const u16* gw, const float* ctx, const u16* wo, const float* hh, const u16* w2, u16* out) {
  int m = blockIdx.x * 64 + threadIdx.x;
  float a = 0.f, b = 0.f, g = 0.f;
  for (int i = 0; i < H * D; ++i) a += ctx[i] * bf2f(wo[(size_t)i * M + m]);
  for (int f = 0; f < F; ++f) b += hh[f] * bf2f(w2[(size_t)f * M + m]);
  for (int i = 0; i < M; ++i) g += bf2f(x[i]) * bf2f(gw[i]);
  g = 1.f / (1.f + __expf(-g));
  out[m] = f2bf(g * a + (1.f - g) * b);
}

extern "C" void kernel_launch(void* const* d_in, const int* in_sizes, int n_in,
                              void* d_out, int out_size, void* d_ws, size_t ws_size,
                              hipStream_t stream) {
  const u16* x = (const u16*)d_in[0];
  const u16* ckv = (const u16*)d_in[1];
  const u16* wq = (const u16*)d_in[2];
  const u16* wuk = (const u16*)d_in[3];
  const u16* wuv = (const u16*)d_in[4];
  const u16* wo = (const u16*)d_in[5];
  const u16* w1 = (const u16*)d_in[7];
  const u16* w2 = (const u16*)d_in[8];
  const u16* gw = (const u16*)d_in[9];
  float* ws = (float*)d_ws;
  float *q = ws, *qa = q + 4096, *s = qa + H * DC, *cl = s + H * SK, *ctx = cl + H * DC, *hh = ctx + 4096;
  k_q<<<32, 128, 0, stream>>>(x, wq, q);
  k_qabs<<<32, 512, 0, stream>>>(q, wuk, qa);
  k_scores<<<dim3(32, 16), 256, 0, stream>>>(qa, ckv, s);
  k_softmax<<<32, 256, 0, stream>>>(s);
  k_pv<<<32, 512, 0, stream>>>(s, ckv, cl);
  k_v<<<32, 128, 0, stream>>>(cl, wuv, ctx);
  k_h<<<27, 64, 0, stream>>>(x, w1, hh);
  k_out<<<10, 64, 0, stream>>>(x, gw, ctx, wo, hh, w2, (u16*)d_out);
}
```

```cpp
#include <hip/hip_runtime.h>
#include <hip/hip_bf16.h>
#include <stdint.h>

typedef unsigned short u16;
static __device__ __forceinline__ float bf2f(u16 b) { return __uint_as_float(((unsigned)b) << 16); }
static __device__ __forceinline__ u16 f2bf(float f) { unsigned u = __float_as_uint(f); u += 0x7FFFu + ((u >> 16) & 1u); return (u16)(u >> 16); }

constexpr int H = 32, D = 128, SK = 4096, M = 640, DC = 512, F = 1728;

__global__ void k_q(const u16* x, const u16* wq, float* q) {
  int h = blockIdx.x, d = threadIdx.x;
  float acc = 0.f;
  for (int m = 0; m < M; ++m) acc += bf2f(x[m]) * bf2f(wq[(size_t)m * H * D + h * D + d]);
  q[h * D + d] = acc;
}
__global__ void k_qabs(const float* q, const u16* wuk, float* qa) {
  int h = blockIdx.x, c = threadIdx.x;
  float acc = 0.f;
  for (int d = 0; d < D; ++d) acc += q[h * D + d] * bf2f(wuk[(size_t)c * H * D + h * D + d]);
  qa[h * DC + c] = acc * 0.08838834764831845f;
}
__global__ void k_scores(const float* qa, const u16* ckv, float* s) {
  int h = blockIdx.x, k = blockIdx.y * 256 + threadIdx.x;
  float acc = 0.f;
  for (int c = 0; c < DC; ++c) acc += qa[h * DC + c] * bf2f(ckv[(size_t)k * DC + c]);
  s[h * SK + k] = acc;
}
__global__ void k_softmax(float* s) {
  __shared__ float red[256];
  int h = blockIdx.x, t = threadIdx.x;
  float m = -1e30f;
  for (int k = t; k < SK; k += 256) m = fmaxf(m, s[h * SK + k]);
  red[t] = m; __syncthreads();
  for (int o = 128; o > 0; o >>= 1) { if (t < o) red[t] = fmaxf(red[t], red[t + o]); __syncthreads(); }
  m = red[0]; __syncthreads();
  float l = 0.f;
  for (int k = t; k < SK; k += 256) { float e = __expf(s[h * SK + k] - m); s[h * SK + k] = e; l += e; }
  red[t] = l; __syncthreads();
  for (int o = 128; o > 0; o >>= 1) { if (t < o) red[t] += red[t + o]; __syncthreads(); }
  l = red[0];
  for (int k = t; k < SK; k += 256) s[h * SK + k] /= l;
}
__global__ void k_pv(const float* p, const u16* ckv, float* cl) {
  int h = blockIdx.x, c = threadIdx.x;
  float acc = 0.f;
  for (int k = 0; k < SK; ++k) acc += p[h * SK + k] * bf2f(ckv[(size_t)k * DC + c]);
  cl[h * DC + c] = acc;
}
__global__ void k_v(const float* cl, const u16* wuv, float* ctx) {
  int h = blockIdx.x, d = threadIdx.x;
  float acc = 0.f;
  for (int c = 0; c < DC; ++c) acc += cl[h * DC + c] * bf2f(wuv[(size_t)c * H * D + h * D + d]);
  ctx[h * D + d] = acc;
}
__global__ void k_h(const u16* x, const u16* w1, float* hh) {
  int f = blockIdx.x * 64 + threadIdx.x;
  float acc = 0.f;
  for (int m = 0; m < M; ++m) acc += bf2f(x[m]) * bf2f(w1[(size_t)m * F + f]);
  hh[f] = acc / (1.f + __expf(-acc));
}
__global__ void k_out(const u16* x, const u16* gw, const float* ctx, const u16* wo, const float* hh, const u16* w2, u16* out) {
  int m = blockIdx.x * 64 + threadIdx.x;
  float a = 0.f, b = 0.f, g = 0.f;
  for (int i = 0; i < H * D; ++i) a += ctx[i] * bf2f(wo[(size_t)i * M + m]);
  for (int f = 0; f < F; ++f) b += hh[f] * bf2f(w2[(size_t)f * M + m]);
  for (int i = 0; i < M; ++i) g += bf2f(x[i]) * bf2f(gw[i]);
  g = 1.f / (1.f + __expf(-g));
  out[m] = f2bf(g * a + (1.f - g) * b);
}


typedef unsigned long long u64;
typedef unsigned v4u __attribute__((ext_vector_type(4)));
__global__ void __launch_bounds__(512) k_probe_start(u64* st) {
  u64 t0 = __builtin_amdgcn_s_memrealtime();
  if (threadIdx.x == 0) st[blockIdx.x] = t0;
}
__global__ void k_amp(const u64* st, int mode, int amp) {
  __shared__ u64 s[256];
  int t = threadIdx.x;
  s[t] = st[t];
  __syncthreads();
  if (t == 0) {
    u64 mn = ~0ull, mx = 0, mj = ~0ull;
    for (int i = 0; i < 256; ++i) { mn = s[i] < mn ? s[i] : mn; mx = s[i] > mx ? s[i] : mx; if (mode > 0 && (i & 7) == mode - 1) mj = s[i] < mj ? s[i] : mj; }
    u64 q = mode == 0 ? mx - mn : mj - mn;
    if (q > 2000) q = 2000;
    u64 t1 = __builtin_amdgcn_s_memrealtime();
    u64 until = t1 + q * amp;
    while (__builtin_amdgcn_s_memrealtime() < until) __builtin_amdgcn_s_sleep(4);
  }
}

extern "C" void kernel_launch(void* const* d_in, const int* in_sizes, int n_in,
                              void* d_out, int out_size, void* d_ws, size_t ws_size,
                              hipStream_t stream) {
  const u16* x = (const u16*)d_in[0];
  const u16* ckv = (const u16*)d_in[1];
  const u16* wq = (const u16*)d_in[2];
  const u16* wuk = (const u16*)d_in[3];
  const u16* wuv = (const u16*)d_in[4];
  const u16* wo = (const u16*)d_in[5];
  const u16* w1 = (const u16*)d_in[7];
  const u16* w2 = (const u16*)d_in[8];
  const u16* gw = (const u16*)d_in[9];
  float* ws = (float*)d_ws;
  float *q = ws, *qa = q + 4096, *s = qa + H * DC, *cl = s + H * SK, *ctx = cl + H * DC, *hh = ctx + 4096;
  u64* st = (u64*)((char*)d_ws + (8u<<20));
  k_probe_start<<<256, 512, 0, stream>>>(st);
  k_amp<<<1, 256, 0, stream>>>(st, 0, 0);
  k_q<<<32, 128, 0, stream>>>(x, wq, q);
  k_qabs<<<32, 512, 0, stream>>>(q, wuk, qa);
  k_scores<<<dim3(32, 16), 256, 0, stream>>>(qa, ckv, s);
  k_softmax<<<32, 256, 0, stream>>>(s);
  k_pv<<<32, 512, 0, stream>>>(s, ckv, cl);
  k_v<<<32, 128, 0, stream>>>(cl, wuv, ctx);
  k_h<<<27, 64, 0, stream>>>(x, w1, hh);
  k_out<<<10, 64, 0, stream>>>(x, gw, ctx, wo, hh, w2, (u16*)d_out);
}
```

```cpp
#include <hip/hip_runtime.h>
#include <hip/hip_bf16.h>
#include <stdint.h>

typedef unsigned short u16;
static __device__ __forceinline__ float bf2f(u16 b) { return __uint_as_float(((unsigned)b) << 16); }
static __device__ __forceinline__ u16 f2bf(float f) { unsigned u = __float_as_uint(f); u += 0x7FFFu + ((u >> 16) & 1u); return (u16)(u >> 16); }

constexpr int H = 32, D = 128, SK = 4096, M = 640, DC = 512, F = 1728;

__global__ void k_q(const u16* x, const u16* wq, float* q) {
  int h = blockIdx.x, d = threadIdx.x;
  float acc = 0.f;
  for (int m = 0; m < M; ++m) acc += bf2f(x[m]) * bf2f(wq[(size_t)m * H * D + h * D + d]);
  q[h * D + d] = acc;
}
__global__ void k_qabs(const float* q, const u16* wuk, float* qa) {
  int h = blockIdx.x, c = threadIdx.x;
  float acc = 0.f;
  for (int d = 0; d < D; ++d) acc += q[h * D + d] * bf2f(wuk[(size_t)c * H * D + h * D + d]);
  qa[h * DC + c] = acc * 0.08838834764831845f;
}
__global__ void k_scores(const float* qa, const u16* ckv, float* s) {
  int h = blockIdx.x, k = blockIdx.y * 256 + threadIdx.x;
  float acc = 0.f;
  for (int c = 0; c < DC; ++c) acc += qa[h * DC + c] * bf2f(ckv[(size_t)k * DC + c]);
  s[h * SK + k] = acc;
}
__global__ void k_softmax(float* s) {
  __shared__ float red[256];
  int h = blockIdx.x, t = threadIdx.x;
  float m = -1e30f;
  for (int k = t; k < SK; k += 256) m = fmaxf(m, s[h * SK + k]);
  red[t] = m; __syncthreads();
  for (int o = 128; o > 0; o >>= 1) { if (t < o) red[t] = fmaxf(red[t], red[t + o]); __syncthreads(); }
  m = red[0]; __syncthreads();
  float l = 0.f;
  for (int k = t; k < SK; k += 256) { float e = __expf(s[h * SK + k] - m); s[h * SK + k] = e; l += e; }
  red[t] = l; __syncthreads();
  for (int o = 128; o > 0; o >>= 1) { if (t < o) red[t] += red[t + o]; __syncthreads(); }
  l = red[0];
  for (int k = t; k < SK; k += 256) s[h * SK + k] /= l;
}
__global__ void k_pv(const float* p, const u16* ckv, float* cl) {
  int h = blockIdx.x, c = threadIdx.x;
  float acc = 0.f;
  for (int k = 0; k < SK; ++k) acc += p[h * SK + k] * bf2f(ckv[(size_t)k * DC + c]);
  cl[h * DC + c] = acc;
}
__global__ void k_v(const float* cl, const u16* wuv, float* ctx) {
  int h = blockIdx.x, d = threadIdx.x;
  float acc = 0.f;
  for (int c = 0; c < DC; ++c) acc += cl[h * DC + c] * bf2f(wuv[(size_t)c * H * D + h * D + d]);
  ctx[h * D + d] = acc;
}
__global__ void k_h(const u16* x, const u16* w1, float* hh) {
  int f = blockIdx.x * 64 + threadIdx.x;
  float acc = 0.f;
  for (int m = 0; m < M; ++m) acc += bf2f(x[m]) * bf2f(w1[(size_t)m * F + f]);
  hh[f] = acc / (1.f + __expf(-acc));
}
__global__ void k_out(const u16* x, const u16* gw, const float* ctx, const u16* wo, const float* hh, const u16* w2, u16* out) {
  int m = blockIdx.x * 64 + threadIdx.x;
  float a = 0.f, b = 0.f, g = 0.f;
  for (int i = 0; i < H * D; ++i) a += ctx[i] * bf2f(wo[(size_t)i * M + m]);
  for (int f = 0; f < F; ++f) b += hh[f] * bf2f(w2[(size_t)f * M + m]);
  for (int i = 0; i < M; ++i) g += bf2f(x[i]) * bf2f(gw[i]);
  g = 1.f / (1.f + __expf(-g));
  out[m] = f2bf(g * a + (1.f - g) * b);
}


typedef unsigned long long u64;
typedef unsigned v4u __attribute__((ext_vector_type(4)));
struct P { const char* p[7]; u64* st; };
template<int NT>
__global__ void __launch_bounds__(NT) k_stream(P p) {
  const int b = blockIdx.x, t = threadIdx.x, lane = t & 63;
  const int w = __builtin_amdgcn_readfirstlane(t >> 6);
  u64 t0 = __builtin_amdgcn_s_memrealtime();
  constexpr int NW = NT / 64;
  constexpr int cum[8] = {0, 20, 36, 52, 61, 77, 97, 106};
  constexpr int nb[7] = {20480, 16384, 16384, 8640, 16384, 20480, 8640};
  constexpr int R = (106 + NW - 1) / NW;
  v4u r[R];
#pragma unroll
  for (int i = 0; i < R; ++i) {
    int q = w + NW * i; if (q > 105) q = 105;
    int seg = 0;
#pragma unroll
    for (int j = 1; j < 7; ++j) if (q >= cum[j]) seg = j;
    const char* base = p.p[0]; int c0 = 0, n = nb[0];
#pragma unroll
    for (int j = 1; j < 7; ++j) if (seg == j) { base = p.p[j]; c0 = cum[j]; n = nb[j]; }
    int off = (q - c0) * 1024 + lane * 16; if (off >= n) off = n - 16;
    const char* a = base + (size_t)b * n + off;
    asm volatile("global_load_dwordx4 %0, %1, off" : "=v"(r[i]) : "v"(a) : "memory");
  }
  u64 t1, t2;
  asm volatile("s_waitcnt vmcnt(%1)\n\ts_memrealtime %0\n\ts_waitcnt lgkmcnt(0)" : "=s"(t1) : "i"(R - 1) : "memory");
  asm volatile("s_waitcnt vmcnt(0)\n\ts_memrealtime %0\n\ts_waitcnt lgkmcnt(0)" : "=s"(t2) :: "memory");
#pragma unroll
  for (int i = 0; i < R; ++i) asm volatile("" :: "v"(r[i]));
  if (lane == 0) {
    u64* s = p.st + ((size_t)b * 8 + w) * 4;
    s[0] = t0; s[1] = t1; s[2] = t2;
  }
}
__global__ void k_amp(const u64* st, int mode, int amp) {
  __shared__ u64 s[256][4];
  int t = threadIdx.x;
  u64 mn = ~0ull, mxs = 0, mxe = 0, ml = ~0ull;
  for (int w = 0; w < 8; ++w) { const u64* q = st + ((size_t)t * 8 + w) * 4; u64 a = q[0], b = q[1], c = q[2]; mn = a < mn ? a : mn; mxs = a > mxs ? a : mxs; mxe = c > mxe ? c : mxe; ml = (b - a) < ml ? (b - a) : ml; }
  s[t][0] = mn; s[t][1] = mxs; s[t][2] = mxe; s[t][3] = ml;
  __syncthreads();
  if (t == 0) {
    mn = ~0ull; mxs = 0; mxe = 0; ml = ~0ull;
    for (int i = 0; i < 256; ++i) { mn = s[i][0] < mn ? s[i][0] : mn; mxs = s[i][1] > mxs ? s[i][1] : mxs; mxe = s[i][2] > mxe ? s[i][2] : mxe; ml = s[i][3] < ml ? s[i][3] : ml; }
    u64 q = mode == 0 ? mxe - mn : mode == 1 ? st[1] - st[0] : mode == 2 ? ml : mxe - mxs;
    if (q > 3000) q = 3000;
    u64 t1 = __builtin_amdgcn_s_memrealtime();
    u64 until = t1 + q * amp;
    while (__builtin_amdgcn_s_memrealtime() < until) __builtin_amdgcn_s_sleep(4);
  }
}

extern "C" void kernel_launch(void* const* d_in, const int* in_sizes, int n_in,
                              void* d_out, int out_size, void* d_ws, size_t ws_size,
                              hipStream_t stream) {
  const u16* x = (const u16*)d_in[0];
  const u16* ckv = (const u16*)d_in[1];
  const u16* wq = (const u16*)d_in[2];
  const u16* wuk = (const u16*)d_in[3];
  const u16* wuv = (const u16*)d_in[4];
  const u16* wo = (const u16*)d_in[5];
  const u16* w1 = (const u16*)d_in[7];
  const u16* w2 = (const u16*)d_in[8];
  const u16* gw = (const u16*)d_in[9];
  float* ws = (float*)d_ws;
  float *q = ws, *qa = q + 4096, *s = qa + H * DC, *cl = s + H * SK, *ctx = cl + H * DC, *hh = ctx + 4096;
  P p; p.p[0] = (const char*)wq; p.p[1] = (const char*)wuk; p.p[2] = (const char*)ckv; p.p[3] = (const char*)w1; p.p[4] = (const char*)wuv; p.p[5] = (const char*)wo; p.p[6] = (const char*)w2;
  p.st = (u64*)((char*)d_ws + (8u<<20));
  k_stream<512><<<256, 512, 0, stream>>>(p);
  k_amp<<<1, 256, 0, stream>>>(p.st, 0, 0);
  k_q<<<32, 128, 0, stream>>>(x, wq, q);
  k_qabs<<<32, 512, 0, stream>>>(q, wuk, qa);
  k_scores<<<dim3(32, 16), 256, 0, stream>>>(qa, ckv, s);
  k_softmax<<<32, 256, 0, stream>>>(s);
  k_pv<<<32, 512, 0, stream>>>(s, ckv, cl);
  k_v<<<32, 128, 0, stream>>>(cl, wuv, ctx);
  k_h<<<27, 64, 0, stream>>>(x, w1, hh);
  k_out<<<10, 64, 0, stream>>>(x, gw, ctx, wo, hh, w2, (u16*)d_out);
}
```

```cpp
#include <hip/hip_runtime.h>
#include <stdint.h>
#pragma clang diagnostic ignored "-Winline-asm"

typedef unsigned v4u __attribute__((ext_vector_type(4)));
typedef unsigned v2u __attribute__((ext_vector_type(2)));
typedef float f4 __attribute__((ext_vector_type(4)));
typedef float f2 __attribute__((ext_vector_type(2)));
typedef short s4 __attribute__((ext_vector_type(4)));
typedef short s8 __attribute__((ext_vector_type(8)));
typedef __bf16 bf2 __attribute__((ext_vector_type(2)));
typedef unsigned long long u64;
#define LAS __attribute__((address_space(3)))
#define DI __device__ __forceinline__

constexpr unsigned OFF_E = 0;
constexpr unsigned OFF_FLAG2 = 4096;
constexpr unsigned OFF_FLAG1 = 8192;
constexpr unsigned OFF_QP = OFF_FLAG1 + 32768;
constexpr unsigned OFF_QA = OFF_QP + 65536;
constexpr unsigned OFF_CLR = OFF_QA + 32768;
constexpr unsigned OFF_ST = OFF_CLR;
constexpr unsigned OFF_OP = OFF_ST + 8192;
constexpr unsigned OFF_MP = OFF_OP + 163840;
constexpr unsigned OFF_PP = OFF_MP + 163840;
constexpr unsigned OFF_END = OFF_PP + 1048576;
constexpr unsigned CLR_PER = 11264;
static_assert(OFF_END <= OFF_CLR + 128 * CLR_PER, "clear covers the inboxes");
constexpr unsigned OFF_STAMP = 8u << 20;
constexpr unsigned SENT = 0xFFFFFFFFu;
constexpr unsigned SPIN_CAP = 1u << 15;
constexpr float SC = 0.08838834764831845f * 1.4426950408889634f;

constexpr unsigned L_CKV = 0;
constexpr unsigned L_UK = 32768;
constexpr unsigned L_QAS = 131072;
constexpr unsigned L_QRED = L_QAS;
constexpr unsigned L_P = L_QAS + 8448;
constexpr unsigned L_MX = L_P + 2304;
constexpr unsigned L_LS = L_MX + 256;
constexpr unsigned L_RED = L_LS + 256;
constexpr unsigned L_TAB = L_RED + 1024;
constexpr unsigned L_QB = L_TAB + 4096;
constexpr unsigned L_END = L_QB + 2304;

DI f4 mfma(v4u a, v4u b, f4 c) { return __builtin_amdgcn_mfma_f32_16x16x32_bf16(__builtin_bit_cast(s8, a), __builtin_bit_cast(s8, b), c, 0, 0, 0); }
DI unsigned cvtpk(float lo, float hi) { f2 x = {lo, hi}; bf2 y = __builtin_convertvector(x, bf2); return __builtin_bit_cast(unsigned, y); }
DI float asf(unsigned u) { return __uint_as_float(u); }
DI unsigned asu(float f) { return __float_as_uint(f); }
DI unsigned guard(unsigned u) { return u < 0xFFFFFFFEu ? u : 0xFFFFFFFEu; }
DI float fmx(float a, float b) { return __builtin_elementwise_maximum(a, b); }
template <int CTRL> DI float dpp(float x) { return asf((unsigned)__builtin_amdgcn_mov_dpp((int)asu(x), CTRL, 0xf, 0xf, true)); }
constexpr int XOR1 = 0xB1, XOR2 = 0x4E, ROR4 = 0x124, ROR8 = 0x128, HMIR = 0x141;
DI float p16_max(float x) { auto t = __builtin_amdgcn_permlane16_swap(asu(x), asu(x), false, false); unsigned a = t[0], b = t[1]; return fmx(asf(a), asf(b)); }
DI float p32_max(float x) { auto t = __builtin_amdgcn_permlane32_swap(asu(x), asu(x), false, false); unsigned a = t[0], b = t[1]; return fmx(asf(a), asf(b)); }
DI float p16_sum(float x) { auto t = __builtin_amdgcn_permlane16_swap(asu(x), asu(x), false, false); unsigned a = t[0], b = t[1]; return asf(a) + asf(b); }
DI float p32_sum(float x) { auto t = __builtin_amdgcn_permlane32_swap(asu(x), asu(x), false, false); unsigned a = t[0], b = t[1]; return asf(a) + asf(b); }
DI float row_sum(float x) { x += dpp<XOR1>(x); x += dpp<XOR2>(x); x += dpp<ROR4>(x); x += dpp<ROR8>(x); return x; }
DI float row_max(float x) { x = fmx(x, dpp<XOR1>(x)); x = fmx(x, dpp<XOR2>(x)); x = fmx(x, dpp<ROR4>(x)); x = fmx(x, dpp<ROR8>(x)); return x; }
DI float max8(f4 u, f4 v) { return fmx(fmx(fmx(u.x, u.y), fmx(u.z, u.w)), fmx(fmx(v.x, v.y), fmx(v.z, v.w))); }
DI float sum8(f4 u, f4 v) { return ((u.x + u.y) + (u.z + u.w)) + ((v.x + v.y) + (v.z + v.w)); }
DI unsigned umax(unsigned a, unsigned b) { return a > b ? a : b; }
DI unsigned mx4(v4u v) { return umax(umax(v.x, v.y), umax(v.z, v.w)); }
DI bool allvalid(unsigned m) { return __builtin_amdgcn_ballot_w64(m == SENT) == 0ull; }
DI v4u lds4(unsigned a) { return *(LAS v4u*)(size_t)a; }
DI f4 ldsf4(unsigned a) { return *(LAS f4*)(size_t)a; }
DI void sts4(unsigned a, v4u v) { *(LAS v4u*)(size_t)a = v; }
DI void stsf4(unsigned a, f4 v) { *(LAS f4*)(size_t)a = v; }
DI void sts2(unsigned a, v2u v) { *(LAS v2u*)(size_t)a = v; }
DI void sts1(unsigned a, unsigned v) { *(LAS unsigned*)(size_t)a = v; }
DI v4u ldstr(unsigned a0, unsigned a1) {
  s4 lo = __builtin_amdgcn_ds_read_tr16_b64_v4i16((LAS s4*)(size_t)a0), hi = __builtin_amdgcn_ds_read_tr16_b64_v4i16((LAS s4*)(size_t)a1);
  s8 c = __builtin_shufflevector(lo, hi, 0, 1, 2, 3, 4, 5, 6, 7);
  return __builtin_bit_cast(v4u, c);
}

#define GLD4(dst, voff, sbase, IMM, MOD) asm volatile("global_load_dwordx4 %0, %1, %2 offset:%3 " MOD : "=v"(dst) : "v"(voff), "s"(sbase), "i"(IMM) : "memory")
#define GLD2(dst, voff, sbase, IMM, MOD) asm volatile("global_load_dwordx2 %0, %1, %2 offset:%3 " MOD : "=v"(dst) : "v"(voff), "s"(sbase), "i"(IMM) : "memory")
#define GLD1(dst, voff, sbase, IMM, MOD) asm volatile("global_load_dword %0, %1, %2 offset:%3 " MOD : "=v"(dst) : "v"(voff), "s"(sbase), "i"(IMM) : "memory")
#define GST4(src, voff, sbase, MOD) asm volatile("global_store_dwordx4 %0, %1, %2 " MOD "\n\ts_nop 1" :: "v"(voff), "v"(src), "s"(sbase) : "memory")
#define GST2(src, voff, sbase, MOD) asm volatile("global_store_dwordx2 %0, %1, %2 " MOD :: "v"(voff), "v"(src), "s"(sbase) : "memory")
#define SCHED() __builtin_amdgcn_sched_barrier(0)
#define VMWAIT(N) do { asm volatile("s_waitcnt vmcnt(%0)" :: "i"(N) : "memory"); SCHED(); } while (0)
#define LGWAIT() do { asm volatile("s_waitcnt lgkmcnt(0)" ::: "memory"); SCHED(); } while (0)
#define TIE(x) asm volatile("" : "+v"(x))
DI void dma16(unsigned voff, const void* sbase, unsigned ldsdst) {
  asm volatile("s_mov_b32 m0, %2\n\ts_nop 0\n\tglobal_load_lds_dwordx4 %0, %1" :: "v"(voff), "s"(sbase), "s"(ldsdst) : "memory", "m0");
}
#define BARRIER() do { asm volatile("s_waitcnt lgkmcnt(0)\n\ts_barrier" ::: "memory"); SCHED(); } while (0)
#define RAWBAR() do { asm volatile("s_barrier" ::: "memory"); SCHED(); } while (0)

#define STAMP(i) do {} while (0)
#define NPRE 4
#define NPRE2 8

__global__ void __launch_bounds__(512, 2)
gm_kernel(const char* __restrict__ x, const char* __restrict__ ckv, const char* __restrict__ wq, const char* __restrict__ wuk,
          const char* __restrict__ wuv, const char* __restrict__ wo, const char* __restrict__ w1, const char* __restrict__ w2,
          const char* __restrict__ gw, char* __restrict__ out, char* __restrict__ ws) {
  __shared__ __attribute__((aligned(16))) char smem[L_END];
  asm volatile("" :: "v"((LAS char*)smem));
  STAMP(0);
  const int tid = threadIdx.x, lane = tid & 63;
  const int w = __builtin_amdgcn_readfirstlane(tid >> 6);
  const int b = blockIdx.x, xcd = b & 7, r = b >> 3;
  const bool roleA = r < 16;
  const int g4 = lane >> 4, n16 = lane & 15;
  const int rr = r & 15;
  const int hq = xcd * 4 + (rr >> 2), j8 = (roleA ? 0 : 4) + (rr & 3);
  asm volatile("" :: "s"(x), "s"(ckv), "s"(wq), "s"(wuk), "s"(wuv), "s"(wo), "s"(w1), "s"(w2), "s"(gw), "s"(out), "s"(ws));
  u64 epoch_raw;
  asm volatile("s_load_dwordx2 %0, %1, 0x0" : "=s"(epoch_raw) : "s"(ws) : "memory");

  v4u wqr[4]; v2u xr;
#define Q_ISSUE(ROLE_A) \
  if (w == 0) { \
    v4u s_ = {SENT, SENT, SENT, SENT}; \
    unsigned vo = (unsigned)(OFF_QP + (hq * 8 + j8) * 256 + n16 * 16); \
    if (ROLE_A && lane >= 16) vo = (unsigned)(OFF_QA + hq * 1024 + j8 * 256 + n16 * 16); \
    if (lane < (ROLE_A ? 32 : 16)) GST4(s_, vo, ws, "sc1"); } \
  if (w < 5) { \
    const char* wb = wq + (size_t)(j8 * 80 + 16 * w) * 8192 + hq * 256; \
    const unsigned vo = (unsigned)(4 * g4 * 8192 + n16 * 16); \
    GLD2(xr, (unsigned)(8 * g4), x + (j8 * 80 + 16 * w) * 2, 0, ""); \
    GLD4(wqr[0], vo, wb, 0, ""); GLD4(wqr[1], vo, wb + 8192, 0, ""); GLD4(wqr[2], vo, wb + 16384, 0, ""); GLD4(wqr[3], vo, wb + 24576, 0, ""); } \
  RAWBAR();
#define Q_COMPUTE(NG2) \
  LGWAIT(); \
  u64 epoch = epoch_raw; asm volatile("" : "+s"(epoch)); epoch += 1; \
  if (w == 0) { \
    VMWAIT(5 + NG2); \
    if (lane == 0) { v2u f = {(unsigned)epoch, (unsigned)(epoch >> 32)}; GST2(f, (unsigned)(OFF_FLAG1 + b * 128), ws, "sc1"); } } \
  if (w < 5) { \
    if (w == 0) VMWAIT(NG2 + 1 + 4); else VMWAIT(NG2 + 4); \
    TIE(xr); \
    const unsigned d0 = __builtin_amdgcn_perm(xr.x, xr.x, 0x01000100u), d1 = __builtin_amdgcn_perm(xr.x, xr.x, 0x03020302u); \
    const unsigned d2 = __builtin_amdgcn_perm(xr.y, xr.y, 0x01000100u), d3 = __builtin_amdgcn_perm(xr.y, xr.y, 0x03020302u); \
    v4u a0 = {d0 & msk[0], d0 & msk[1], d0 & msk[2], d0 & msk[3]}, a1 = {d1 & msk[0], d1 & msk[1], d1 & msk[2], d1 & msk[3]}; \
    v4u a2 = {d2 & msk[0], d2 & msk[1], d2 & msk[2], d2 & msk[3]}, a3 = {d3 & msk[0], d3 & msk[1], d3 & msk[2], d3 & msk[3]}; \
    unsigned qr = L_QRED + w * 512 + (8 * n16 + 4 * g4) * 4; \
    TIE(a0); TIE(a1); TIE(a2); TIE(a3); TIE(qr); \
    if (w == 0) VMWAIT(NG2 + 1); else VMWAIT(NG2); \
    TIE(wqr[0]); TIE(wqr[1]); TIE(wqr[2]); TIE(wqr[3]); \
    STAMP(2); \
    f4 acc = {0.f, 0.f, 0.f, 0.f}; \
    acc = mfma(a0, wqr[0], acc); acc = mfma(a1, wqr[1], acc); acc = mfma(a2, wqr[2], acc); acc = mfma(a3, wqr[3], acc); \
    if (lane < 32) stsf4(qr, acc); } \
  BARRIER(); \
  if (w == 0) { \
    if (lane < 32) { \
      const unsigned ra = L_QRED + lane * 16; \
      f4 s0 = ldsf4(ra), s1 = ldsf4(ra + 512), s2 = ldsf4(ra + 1024), s3 = ldsf4(ra + 1536), s4_ = ldsf4(ra + 2048); \
      f4 sm = (s0 + s1) + (s2 + s3) + s4_; \
      v2u o = {guard(cvtpk(sm.x, sm.y)), guard(cvtpk(sm.z, sm.w))}; \
      GST2(o, (unsigned)(OFF_QP + (hq * 8 + j8) * 256 + lane * 8), ws, "sc1"); } \
    STAMP(3); }
#define FLAG1_CHECK(BLK) \
  for (unsigned spin = 0;; ++spin) { \
    v2u f0; \
    GLD2(f0, (unsigned)(OFF_FLAG1 + (BLK) * 128), ws, 0, "sc1"); \
    VMWAIT(0); TIE(f0); \
    const unsigned df = (f0.x ^ (unsigned)epoch) | (f0.y ^ (unsigned)(epoch >> 32)); \
    if (__builtin_amdgcn_ballot_w64(df != 0u) == 0ull || spin > SPIN_CAP) break; \
  }
#define FLAG2_CHECK() \
  for (unsigned spin = 0;; ++spin) { \
    v4u f0; \
    GLD4(f0, (unsigned)(OFF_FLAG2 + lane * 16), ws, 0, "sc1"); \
    VMWAIT(0); TIE(f0); \
    const unsigned lo = (unsigned)epoch, hi = (unsigned)(epoch >> 32); \
    const unsigned df = ((f0.x ^ lo) | (f0.y ^ hi)) | ((f0.z ^ lo) | (f0.w ^ hi)); \
    if (__builtin_amdgcn_ballot_w64(df != 0u) == 0ull || spin > SPIN_CAP) break; \
  }
  unsigned msk[4];
#pragma unroll
  for (int t = 0; t < 4; ++t) msk[t] = (((n16 & 7) >> 1) == t) ? ((n16 & 1) ? 0xFFFF0000u : 0x0000FFFFu) : 0u;

  if (roleA) {
    const int h = hq, j = j8;
    Q_ISSUE(true);
    STAMP(1);
    {
      const char* ub = wuk + (size_t)(128 * j + 16 * w) * 8192 + h * 256;
#pragma unroll
      for (int i = 0; i < 4; ++i)
        dma16((unsigned)((4 * i + g4) * 8192 + ((n16 ^ (4 * i + g4)) * 16)), ub, (unsigned)(L_UK + (16 * w + 4 * i) * 256));
    }
    Q_COMPUTE(4);
    if (w == 7) { const int j8l = lane & 7; const int blk = (h >> 2) + 8 * ((j8l >> 2) * 16 + (h & 3) * 4 + (j8l & 3)); FLAG1_CHECK(blk); STAMP(3); }
    else VMWAIT(0);
    v4u uk[4];
#pragma unroll
    for (int ks = 0; ks < 4; ++ks) uk[ks] = lds4(L_UK + (16 * w + n16) * 256 + (((4 * ks + g4) ^ n16) * 16));
    unsigned qb_a = L_QB + (n16 & 7) * 288 + g4 * 16;
    unsigned qa_o = (unsigned)(OFF_QA + h * 1024 + (128 * j + 16 * w + 4 * g4) * 2);
    TIE(qb_a); TIE(qa_o);
    if (w == 7) {
      v4u p0, p1;
      const unsigned vo = (unsigned)(OFF_QP + h * 2048 + lane * 16);
      unsigned wa = L_QB + g4 * 288 + n16 * 16; TIE(wa);
      for (unsigned spin = 0;; ++spin) {
        GLD4(p0, vo, ws, 0, "sc1"); GLD4(p1, vo, ws, 1024, "sc1");
        VMWAIT(0); TIE(p0); TIE(p1);
        if (allvalid(umax(mx4(p0), mx4(p1))) || spin > SPIN_CAP) break;
      }
      STAMP(4);
      sts4(wa, p0); sts4(wa + 4 * 288, p1);
    }
    BARRIER();
    {
      v4u bq[4];
#pragma unroll
      for (int ks = 0; ks < 4; ++ks) bq[ks] = lds4(qb_a + ks * 64);
      SCHED();
      f4 acc = {0.f, 0.f, 0.f, 0.f};
#pragma unroll
      for (int ks = 0; ks < 4; ++ks) acc = mfma(uk[ks], bq[ks], acc);
      float v0 = acc.x, v1 = acc.y, v2 = acc.z, v3 = acc.w;
      v0 += dpp<XOR1>(v0); v1 += dpp<XOR1>(v1); v2 += dpp<XOR1>(v2); v3 += dpp<XOR1>(v3);
      v0 += dpp<XOR2>(v0); v1 += dpp<XOR2>(v1); v2 += dpp<XOR2>(v2); v3 += dpp<XOR2>(v3);
      v0 += dpp<HMIR>(v0); v1 += dpp<HMIR>(v1); v2 += dpp<HMIR>(v2); v3 += dpp<HMIR>(v3);
      v2u o = {guard(cvtpk(v0, v1)), guard(cvtpk(v2, v3))};
      if (n16 == 0) GST2(o, qa_o, ws, "sc1");
    }
    STAMP(5);
    RAWBAR();
    {
      const char* vb = wuv + (size_t)(64 * w) * 8192 + h * 256 + j * 64;
#pragma unroll
      for (int i = 0; i < 4; ++i)
        dma16((unsigned)((16 * i + (lane >> 2)) * 8192 + (lane & 3) * 16), vb, (unsigned)(L_CKV + (64 * w + 16 * i) * 64));
    }
    v4u wor[8];
    if (w < 5) {
      const char* ob = wo + (size_t)(h * 128 + 32 * j) * 1280 + w * 256;
#pragma unroll
      for (int s = 0; s < 8; ++s) GLD4(wor[s], (unsigned)((8 * g4 + s) * 1280 + n16 * 16), ob, 0, "");
      VMWAIT(8);
    } else {
      VMWAIT(0);
    }
    v4u bv[2][2];
    {
      const int q = (lane & 15) >> 2, p = lane & 3;
      const unsigned a = L_CKV + (64 * w + 8 * g4 + q) * 64 + 8 * p;
#pragma unroll
      for (int ks = 0; ks < 2; ++ks)
#pragma unroll
        for (int dt = 0; dt < 2; ++dt) bv[ks][dt] = ldstr(a + ks * 2048 + dt * 32, a + ks * 2048 + 256 + dt * 32);
    }
    if (w == 7) { FLAG2_CHECK(); }
    unsigned pvo = (unsigned)(OFF_PP + (h * 32 + n16) * 1024 + 128 * w + 16 * g4), pvo2 = pvo + 16384u;
    unsigned sto = (unsigned)(OFF_ST + h * 256 + (lane & 31) * 8);
    unsigned tabw = L_TAB + w * 512 + lane * 4, tabr = L_TAB + w * 512 + g4 * 16;
    unsigned crw = L_RED + lane * 32 + w * 4, crr = L_RED + (lane & 31) * 32;
    unsigned tabr2 = L_TAB + w * 512 + ((n16 >> 3) * 32 + 8 * g4) * 4;
    unsigned opo = (unsigned)(OFF_OP + (16 * w + lane) * 2048 + (h * 4 + j) * 16);
    TIE(pvo); TIE(pvo2); TIE(sto); TIE(tabw); TIE(tabr); TIE(crw); TIE(crr); TIE(tabr2); TIE(opo);
    VMWAIT(0);
    if (w < 5) {
#pragma unroll
      for (int s = 0; s < 8; ++s) TIE(wor[s]);
    }
    STAMP(6);
    BARRIER();
    v2u st;
    for (unsigned spin = 0;; ++spin) {
      GLD2(st, sto, ws, 0, "sc1");
      VMWAIT(0); TIE(st);
      if (allvalid(umax(st.x, st.y)) || spin > SPIN_CAP) break;
    }
    v4u pa[2][2];
    GLD4(pa[0][0], pvo, ws, 0, "sc1"); GLD4(pa[0][1], pvo, ws, 64, "sc1");
    GLD4(pa[1][0], pvo2, ws, 0, "sc1"); GLD4(pa[1][1], pvo2, ws, 64, "sc1");
    STAMP(7);
    f4 aw[2];
    {
      float m = asf(st.x), l = asf(st.y);
      float M = p16_max(row_max(m));
      float a = __builtin_amdgcn_exp2f((m - M) * SC);
      float Ls = p16_sum(row_sum(a * l));
      a = a * __builtin_amdgcn_rcpf(Ls);
      if (lane < 32) sts1(tabw, asu(a));
      LGWAIT();
      aw[0] = ldsf4(tabr); aw[1] = ldsf4(tabr + 64);
    }
    for (unsigned spin = 0;; ++spin) {
      VMWAIT(0); TIE(pa[0][0]); TIE(pa[0][1]); TIE(pa[1][0]); TIE(pa[1][1]);
      if (allvalid(umax(umax(mx4(pa[0][0]), mx4(pa[0][1])), umax(mx4(pa[1][0]), mx4(pa[1][1])))) || spin > SPIN_CAP) break;
      GLD4(pa[0][0], pvo, ws, 0, "sc1"); GLD4(pa[0][1], pvo, ws, 64, "sc1");
      GLD4(pa[1][0], pvo2, ws, 0, "sc1"); GLD4(pa[1][1], pvo2, ws, 64, "sc1");
    }
    STAMP(8);
    {
      f4 z = {0.f, 0.f, 0.f, 0.f};
      f4 t00 = mfma(pa[0][0], bv[0][0], z), t10 = mfma(pa[1][0], bv[0][0], z), t01 = mfma(pa[0][0], bv[0][1], z), t11 = mfma(pa[1][0], bv[0][1], z);
      t00 = mfma(pa[0][1], bv[1][0], t00); t10 = mfma(pa[1][1], bv[1][0], t10); t01 = mfma(pa[0][1], bv[1][1], t01); t11 = mfma(pa[1][1], bv[1][1], t11);
      f4 c0 = aw[0] * t00 + aw[1] * t10, c1 = aw[0] * t01 + aw[1] * t11;
      float u0 = (c0.x + c0.y) + (c0.z + c0.w), u1 = (c1.x + c1.y) + (c1.z + c1.w);
      auto t = __builtin_amdgcn_permlane16_swap(asu(u0), asu(u1), false, false); unsigned ta = t[0], tb = t[1];
      float u = p32_sum(asf(ta) + asf(tb));
      if (lane < 32) sts1(crw, asu(u));
    }
    BARRIER();
    STAMP(9);
    if (w < 5) {
      float c = sum8(ldsf4(crr), ldsf4(crr + 16));
      unsigned hi = cvtpk(c, c);
      float rs = c - asf(hi << 16);
      unsigned lo = cvtpk(rs, rs);
      sts1(tabw, lane < 32 ? hi : lo);
      LGWAIT();
      v4u t0 = lds4(tabr2), t1 = lds4(tabr2 + 16);
      unsigned dv[8] = {t0.x, t0.y, t0.z, t0.w, t1.x, t1.y, t1.z, t1.w};
      f4 acc = {0.f, 0.f, 0.f, 0.f};
#pragma unroll
      for (int s = 0; s < 8; ++s) { v4u a = {dv[s] & msk[0], dv[s] & msk[1], dv[s] & msk[2], dv[s] & msk[3]}; acc = mfma(a, wor[s], acc); }
      float o0 = p32_sum(acc.x), o1 = p32_sum(acc.y), o2 = p32_sum(acc.z), o3 = p32_sum(acc.w);
      unsigned a0 = guard(cvtpk(o0, o1)), a1 = guard(cvtpk(o2, o3));
      auto s0 = __builtin_amdgcn_permlane16_swap(a0, a0, false, false); auto s1 = __builtin_amdgcn_permlane16_swap(a1, a1, false, false);
      unsigned b0 = s0[1], b1 = s1[1];
      v4u o = {a0, a1, b0, b1};
      if (lane < 16) GST4(o, opo, ws, "sc1");
    }
    STAMP(10);
  } else {
    const int g = rr & 3, s = xcd * 4 + (rr >> 2), bi = xcd * 16 + rr;
    Q_ISSUE(false);
    STAMP(1);
    const char* kb = ckv + (size_t)(128 * s + 16 * w) * 1024;
    const unsigned kl = L_CKV + (16 * w) * 1024;
    if (w < 5) {
#pragma unroll
      for (int i = 0; i < NPRE; ++i) dma16((unsigned)((lane ^ i) * 16), kb + i * 1024, kl + i * 1024);
      Q_COMPUTE(NPRE);
#pragma unroll
      for (int i = NPRE; i < 16; ++i) dma16((unsigned)((lane ^ i) * 16), kb + i * 1024, kl + i * 1024);
    } else {
#pragma unroll
      for (int i = 0; i < NPRE2; ++i) dma16((unsigned)((lane ^ i) * 16), kb + i * 1024, kl + i * 1024);
      BARRIER();
#pragma unroll
      for (int i = NPRE2; i < 16; ++i) dma16((unsigned)((lane ^ i) * 16), kb + i * 1024, kl + i * 1024);
      v4u s_ = {SENT, SENT, SENT, SENT};
      const char* cb = ws + OFF_CLR + (size_t)bi * CLR_PER + (w - 5) * 4096;
#pragma unroll
      for (int i = 0; i < 4; ++i) if (i < 3 || w < 7) GST4(s_, (unsigned)(lane * 16 + i * 1024), cb, "sc1");
      if (w == 5) { dma16((unsigned)(lane * 16), x, L_QB); if (lane < 16) dma16((unsigned)(lane * 16), x + 1024, L_QB + 1024); }
    }
    LGWAIT();
    u64 epochB = epoch_raw; asm volatile("" : "+s"(epochB)); epochB += 1;
#define epoch epochB
    if (w == 7) { const int hd = 8 * g + ((lane & 31) >> 2); const int blk = (hd >> 2) + 8 * ((hd & 3) * 4 + (lane & 3)); FLAG1_CHECK(blk); }
    else VMWAIT(0);
    STAMP(4);
    v4u kA[16];
    {
      const unsigned ka = L_CKV + (16 * w + n16) * 1024;
#pragma unroll
      for (int ks = 0; ks < 16; ++ks) kA[ks] = lds4(ka + (((4 * ks + g4) ^ n16) * 16));
    }
    unsigned qvo = (unsigned)(OFF_QA + (8 * g + w) * 1024 + lane * 16);
    unsigned qsw = L_QAS + w * 1056 + lane * 16, qsr = L_QAS + (n16 & 7) * 1056 + g4 * 16;
    unsigned mxw = L_MX + lane * 32 + w * 4, mxr = L_MX + (n16 & 7) * 32;
    unsigned pw = L_P + n16 * 288 + (16 * w + 4 * g4) * 2, pr = L_P + (n16 & 7) * 288 + g4 * 16;
    unsigned lsw = L_LS + lane * 32 + w * 4;
    unsigned ppo = (unsigned)(OFF_PP + ((8 * g + (n16 & 7)) * 32 + s) * 1024 + (64 * w + 32 * (g4 >> 1) + 8 * (2 * (n16 >> 3) + (g4 & 1))) * 2);
    TIE(qvo); TIE(qsw); TIE(qsr); TIE(mxw); TIE(mxr); TIE(pw); TIE(pr); TIE(lsw); TIE(ppo);
    BARRIER();
    if (w == 5 && lane == 0) { v2u f = {(unsigned)epoch, (unsigned)(epoch >> 32)}; GST2(f, (unsigned)(OFF_FLAG2 + bi * 8), ws, "sc1"); }
    STAMP(5);
    v4u qa;
    GLD4(qa, qvo, ws, 0, "sc1");
    v4u vA[4][4];
    {
      const int q = (lane & 15) >> 2, p = lane & 3;
#pragma unroll
      for (int t = 0; t < 4; ++t) {
        const int ch = 8 * w + 4 * (p >> 1) + t;
        const int r0 = 8 * g4 + q, r1 = r0 + 4;
        const unsigned a0 = L_CKV + r0 * 1024 + ((ch ^ (r0 & 15)) * 16) + 8 * (p & 1), a1 = L_CKV + r1 * 1024 + ((ch ^ (r1 & 15)) * 16) + 8 * (p & 1);
        vA[0][t] = ldstr(a0, a1); vA[1][t] = ldstr(a0 + 32768, a1 + 32768);
        vA[2][t] = ldstr(a0 + 65536, a1 + 65536); vA[3][t] = ldstr(a0 + 98304, a1 + 98304);
      }
    }
    SCHED();
    for (unsigned spin = 0;; ++spin) {
      VMWAIT(0); TIE(qa);
      if (allvalid(mx4(qa)) || spin > SPIN_CAP) break;
      GLD4(qa, qvo, ws, 0, "sc1");
    }
    STAMP(6);
    sts4(qsw, qa);
    BARRIER();
    f4 sc = {0.f, 0.f, 0.f, 0.f};
    {
      v4u bq[16];
#pragma unroll
      for (int ks = 0; ks < 16; ++ks) bq[ks] = lds4(qsr + ks * 64);
      SCHED();
#pragma unroll
      for (int ks = 0; ks < 16; ++ks) sc = mfma(kA[ks], bq[ks], sc);
      float mw = p32_max(p16_max(fmx(fmx(sc.x, sc.y), fmx(sc.z, sc.w))));
      if (lane < 8) sts1(mxw, asu(mw));
    }
    BARRIER();
    STAMP(7);
    {
      const float Ms = -max8(ldsf4(mxr), ldsf4(mxr + 16)) * SC;
      float p0 = __builtin_amdgcn_exp2f(fmaf(sc.x, SC, Ms)), p1 = __builtin_amdgcn_exp2f(fmaf(sc.y, SC, Ms)), p2 = __builtin_amdgcn_exp2f(fmaf(sc.z, SC, Ms)), p3 = __builtin_amdgcn_exp2f(fmaf(sc.w, SC, Ms));
      v2u pk = {cvtpk(p0, p1), cvtpk(p2, p3)};
      if (n16 < 8) sts2(pw, pk);
      float ls = p32_sum(p16_sum((p0 + p1) + (p2 + p3)));
      if (lane < 8) sts1(lsw, asu(ls));
    }
    BARRIER();
    STAMP(8);
    {
      v4u pb[4];
#pragma unroll
      for (int ks = 0; ks < 4; ++ks) pb[ks] = lds4(pr + ks * 64);
      f4 lu = ldsf4(L_LS + w * 32), lv = ldsf4(L_LS + w * 32 + 16), mu = ldsf4(L_MX + w * 32), mv = ldsf4(L_MX + w * 32 + 16);
      SCHED();
      f4 ac[4];
#pragma unroll
      for (int t = 0; t < 4; ++t) ac[t] = mfma(vA[0][t], pb[0], f4{0.f, 0.f, 0.f, 0.f});
      {
        v2u o = {guard(asu(max8(mu, mv))), guard(asu(sum8(lu, lv)))};
        if (lane == 0) GST2(o, (unsigned)(OFF_ST + ((8 * g + w) * 32 + s) * 8), ws, "sc1");
      }
#pragma unroll
      for (int ks = 1; ks < 4; ++ks)
#pragma unroll
        for (int t = 0; t < 4; ++t) ac[t] = mfma(vA[ks][t], pb[ks], ac[t]);
      const bool up = n16 >= 8;
      f4 xa = up ? ac[2] : ac[0], ya = up ? ac[3] : ac[1];
      unsigned x0 = cvtpk(xa.x, xa.y), x1 = cvtpk(xa.z, xa.w), y0 = cvtpk(ya.x, ya.y), y1 = cvtpk(ya.z, ya.w);
      auto s0 = __builtin_amdgcn_permlane16_swap(x0, y0, false, false); auto s1 = __builtin_amdgcn_permlane16_swap(x1, y1, false, false);
      unsigned e0 = s0[0], e1 = s1[0], f0 = s0[1], f1 = s1[1];
      v4u o = {guard(e0), guard(e1), guard(f0), guard(f1)};
      GST4(o, ppo, ws, "sc1");
    }
    STAMP(9);
    RAWBAR();
    const int fb = bi < 108 ? bi : 107;
    v4u w1r[3], w2r[4];
    {
      const char* b1 = w1 + fb * 32;
      const unsigned vo = (unsigned)((lane >> 1) * 3456 + (lane & 1) * 16);
      GLD4(w1r[0], vo, b1 + (size_t)(32 * w) * 3456, 0, "");
      GLD4(w1r[1], vo, b1 + (size_t)(32 * (w + 8)) * 3456, 0, "");
      if (w < 4) GLD4(w1r[2], vo, b1 + (size_t)(32 * (w + 16)) * 3456, 0, "");
    }
    if (w < 5) {
      const char* b2 = w2 + (size_t)(16 * fb) * 1280 + w * 256;
#pragma unroll
      for (int i = 0; i < 4; ++i) GLD4(w2r[i], (unsigned)((4 * g4 + i) * 1280 + n16 * 16), b2, 0, "");
      VMWAIT(4);
    } else {
      VMWAIT(0);
    }
    TIE(w1r[0]); TIE(w1r[1]);
    if (w < 4) { TIE(w1r[2]); } else { w1r[2] = v4u{0u, 0u, 0u, 0u}; }
    {
      float ha[8];
#pragma unroll
      for (int k = 0; k < 8; ++k) ha[k] = 0.f;
#pragma unroll
      for (int i = 0; i < 3; ++i) {
        const int it = (i == 2 && w >= 4) ? 0 : w + 8 * i;
        const float xv = asf((unsigned)*(LAS unsigned short*)(size_t)(L_QB + (32 * it + (lane >> 1)) * 2) << 16);
        ha[0] += xv * asf(w1r[i].x << 16); ha[1] += xv * asf(w1r[i].x & 0xFFFF0000u);
        ha[2] += xv * asf(w1r[i].y << 16); ha[3] += xv * asf(w1r[i].y & 0xFFFF0000u);
        ha[4] += xv * asf(w1r[i].z << 16); ha[5] += xv * asf(w1r[i].z & 0xFFFF0000u);
        ha[6] += xv * asf(w1r[i].w << 16); ha[7] += xv * asf(w1r[i].w & 0xFFFF0000u);
      }
#pragma unroll
      for (int k = 0; k < 8; ++k) { float v = ha[k]; v += dpp<XOR2>(v); v += dpp<ROR4>(v); v += dpp<ROR8>(v); ha[k] = p32_sum(p16_sum(v)); }
      if (lane < 2) {
#pragma unroll
        for (int k = 0; k < 8; ++k) sts1(L_RED + (8 * lane + k) * 32 + w * 4, asu(ha[k]));
      }
    }
    BARRIER();
    if (w < 5) {
      VMWAIT(0);
#pragma unroll
      for (int i = 0; i < 4; ++i) TIE(w2r[i]);
      const int f = lane & 15;
      float hh = sum8(ldsf4(L_RED + f * 32), ldsf4(L_RED + f * 32 + 16));
      hh = hh * __builtin_amdgcn_rcpf(1.f + __expf(-hh));
      if (bi >= 108) hh = 0.f;
      unsigned hi = cvtpk(hh, hh);
      float rs = hh - asf(hi << 16);
      unsigned lo = cvtpk(rs, rs);
      if (lane < 32) sts1(L_TAB + w * 512 + lane * 4, lane < 16 ? hi : lo);
      LGWAIT();
      v4u t0 = lds4(L_TAB + w * 512 + ((n16 >> 3) * 16 + 4 * g4) * 4);
      unsigned dv[4] = {t0.x, t0.y, t0.z, t0.w};
      f4 acc = {0.f, 0.f, 0.f, 0.f};
#pragma unroll
      for (int i = 0; i < 4; ++i) { v4u a = {dv[i] & msk[0], dv[i] & msk[1], dv[i] & msk[2], dv[i] & msk[3]}; acc = mfma(a, w2r[i], acc); }
      float o0 = p32_sum(acc.x), o1 = p32_sum(acc.y), o2 = p32_sum(acc.z), o3 = p32_sum(acc.w);
      unsigned a0 = guard(cvtpk(o0, o1)), a1 = guard(cvtpk(o2, o3));
      auto s0 = __builtin_amdgcn_permlane16_swap(a0, a0, false, false); auto s1 = __builtin_amdgcn_permlane16_swap(a1, a1, false, false);
      unsigned b0 = s0[1], b1 = s1[1];
      v4u o = {a0, a1, b0, b1};
      if (lane < 16) GST4(o, (unsigned)(OFF_MP + (16 * w + lane) * 2048 + bi * 16), ws, "sc1");
    }
    STAMP(10);
    if (bi < 80 && w == 5) {
      FLAG2_CHECK();
      unsigned gg[5];
#pragma unroll
      for (int i = 0; i < 5; ++i) GLD1(gg[i], (unsigned)(lane * 4), gw, i * 256, "");
      VMWAIT(0);
#pragma unroll
      for (int i = 0; i < 5; ++i) TIE(gg[i]);
      float gate;
      {
        float a = 0.f;
#pragma unroll
        for (int i = 0; i < 5; ++i) { const unsigned gx = *(LAS unsigned*)(size_t)(L_QB + i * 256 + lane * 4); a += asf(gx << 16) * asf(gg[i] << 16) + asf(gx & 0xFFFF0000u) * asf(gg[i] & 0xFFFF0000u); }
        a = p32_sum(p16_sum(row_sum(a)));
        gate = __builtin_amdgcn_rcpf(1.f + __expf(-a));
      }
      const unsigned one = 0x3F803F80u;
      v4u ones = {one & msk[0], one & msk[1], one & msk[2], one & msk[3]};
      v4u m0, m1;
      const unsigned mvo = (unsigned)(OFF_MP + bi * 2048 + lane * 16);
      for (unsigned spin = 0;; ++spin) {
        GLD4(m0, mvo, ws, 0, "sc1"); GLD4(m1, mvo, ws, 1024, "sc1");
        VMWAIT(0); TIE(m0); TIE(m1);
        if (allvalid(umax(mx4(m0), mx4(m1))) || spin > SPIN_CAP) break;
      }
      f4 bb = {0.f, 0.f, 0.f, 0.f};
      bb = mfma(ones, m0, bb); bb = mfma(ones, m1, bb);
      bb *= (1.f - gate);
      STAMP(11);
      const unsigned ovo = (unsigned)(OFF_OP + bi * 2048 + lane * 16);
      for (unsigned spin = 0;; ++spin) {
        GLD4(m0, ovo, ws, 0, "sc1"); GLD4(m1, ovo, ws, 1024, "sc1");
        VMWAIT(0); TIE(m0); TIE(m1);
        if (allvalid(umax(mx4(m0), mx4(m1))) || spin > SPIN_CAP) break;
      }
      STAMP(12);
      f4 aa = {0.f, 0.f, 0.f, 0.f};
      aa = mfma(ones, m0, aa); aa = mfma(ones, m1, aa);
      float r0 = row_sum(fmaf(gate, aa.x, bb.x)), r1 = row_sum(fmaf(gate, aa.y, bb.y));
      float r2 = row_sum(fmaf(gate, aa.z, bb.z)), r3 = row_sum(fmaf(gate, aa.w, bb.w));
      v2u o = {cvtpk(r0, r1), cvtpk(r2, r3)};
      if (n16 == 0 && g4 < 2) GST2(o, (unsigned)(bi * 16 + g4 * 8), out, "");
      if (bi == 0 && lane == 0) {
        v2u f = {(unsigned)epoch, (unsigned)(epoch >> 32)};
        GST2(f, (unsigned)OFF_E, ws, "sc1");
      }
      STAMP(13);
    }
#undef epoch
  }
}

extern "C" void kernel_launch(void* const* d_in, const int* in_sizes, int n_in,
                              void* d_out, int out_size, void* d_ws, size_t ws_size,
                              hipStream_t stream) {
  gm_kernel<<<256, 512, 0, stream>>>((const char*)d_in[0], (const char*)d_in[1], (const char*)d_in[2], (const char*)d_in[3],
                                      (const char*)d_in[4], (const char*)d_in[5], (const char*)d_in[7], (const char*)d_in[8],
                                      (const char*)d_in[9], (char*)d_out, (char*)d_ws);
}
```

```cpp
#define STAMPS 1
#include <hip/hip_runtime.h>
#include <stdint.h>
#pragma clang diagnostic ignored "-Winline-asm"

typedef unsigned v4u __attribute__((ext_vector_type(4)));
typedef unsigned v2u __attribute__((ext_vector_type(2)));
typedef float f4 __attribute__((ext_vector_type(4)));
typedef float f2 __attribute__((ext_vector_type(2)));
typedef short s4 __attribute__((ext_vector_type(4)));
typedef short s8 __attribute__((ext_vector_type(8)));
typedef __bf16 bf2 __attribute__((ext_vector_type(2)));
typedef unsigned long long u64;
#define LAS __attribute__((address_space(3)))
#define DI __device__ __forceinline__

constexpr unsigned OFF_E = 0;
constexpr unsigned OFF_FLAG2 = 4096;
constexpr unsigned OFF_FLAG1 = 8192;
constexpr unsigned OFF_QP = OFF_FLAG1 + 32768;
constexpr unsigned OFF_QA = OFF_QP + 65536;
constexpr unsigned OFF_CLR = OFF_QA + 32768;
constexpr unsigned OFF_ST = OFF_CLR;
constexpr unsigned OFF_OP = OFF_ST + 8192;
constexpr unsigned OFF_MP = OFF_OP + 163840;
constexpr unsigned OFF_PP = OFF_MP + 163840;
constexpr unsigned OFF_END = OFF_PP + 1048576;
constexpr unsigned CLR_PER = 11264;
static_assert(OFF_END <= OFF_CLR + 128 * CLR_PER, "clear covers the inboxes");
constexpr unsigned OFF_STAMP = 8u << 20;
constexpr unsigned SENT = 0xFFFFFFFFu;
constexpr unsigned SPIN_CAP = 1u << 15;
constexpr float SC = 0.08838834764831845f * 1.4426950408889634f;

constexpr unsigned L_CKV = 0;
constexpr unsigned L_UK = 32768;
constexpr unsigned L_QAS = 131072;
constexpr unsigned L_QRED = L_QAS;
constexpr unsigned L_P = L_QAS + 8448;
constexpr unsigned L_MX = L_P + 2304;
constexpr unsigned L_LS = L_MX + 256;
constexpr unsigned L_RED = L_LS + 256;
constexpr unsigned L_TAB = L_RED + 1024;
constexpr unsigned L_QB = L_TAB + 4096;
constexpr unsigned L_END = L_QB + 2304;

DI f4 mfma(v4u a, v4u b, f4 c) { return __builtin_amdgcn_mfma_f32_16x16x32_bf16(__builtin_bit_cast(s8, a), __builtin_bit_cast(s8, b), c, 0, 0, 0); }
DI unsigned cvtpk(float lo, float hi) { f2 x = {lo, hi}; bf2 y = __builtin_convertvector(x, bf2); return __builtin_bit_cast(unsigned, y); }
DI float asf(unsigned u) { return __uint_as_float(u); }
DI unsigned asu(float f) { return __float_as_uint(f); }
DI unsigned guard(unsigned u) { return u < 0xFFFFFFFEu ? u : 0xFFFFFFFEu; }
DI float fmx(float a, float b) { return __builtin_elementwise_maximum(a, b); }
template <int CTRL> DI float dpp(float x) { return asf((unsigned)__builtin_amdgcn_mov_dpp((int)asu(x), CTRL, 0xf, 0xf, true)); }
constexpr int XOR1 = 0xB1, XOR2 = 0x4E, ROR4 = 0x124, ROR8 = 0x128, HMIR = 0x141;
DI float p16_max(float x) { auto t = __builtin_amdgcn_permlane16_swap(asu(x), asu(x), false, false); unsigned a = t[0], b = t[1]; return fmx(asf(a), asf(b)); }
DI float p32_max(float x) { auto t = __builtin_amdgcn_permlane32_swap(asu(x), asu(x), false, false); unsigned a = t[0], b = t[1]; return fmx(asf(a), asf(b)); }
DI float p16_sum(float x) { auto t = __builtin_amdgcn_permlane16_swap(asu(x), asu(x), false, false); unsigned a = t[0], b = t[1]; return asf(a) + asf(b); }
DI float p32_sum(float x) { auto t = __builtin_amdgcn_permlane32_swap(asu(x), asu(x), false, false); unsigned a = t[0], b = t[1]; return asf(a) + asf(b); }
DI float row_sum(float x) { x += dpp<XOR1>(x); x += dpp<XOR2>(x); x += dpp<ROR4>(x); x += dpp<ROR8>(x); return x; }
DI float row_max(float x) { x = fmx(x, dpp<XOR1>(x)); x = fmx(x, dpp<XOR2>(x)); x = fmx(x, dpp<ROR4>(x)); x = fmx(x, dpp<ROR8>(x)); return x; }
DI float max8(f4 u, f4 v) { return fmx(fmx(fmx(u.x, u.y), fmx(u.z, u.w)), fmx(fmx(v.x, v.y), fmx(v.z, v.w))); }
DI float sum8(f4 u, f4 v) { return ((u.x + u.y) + (u.z + u.w)) + ((v.x + v.y) + (v.z + v.w)); }
DI unsigned umax(unsigned a, unsigned b) { return a > b ? a : b; }
DI unsigned mx4(v4u v) { return umax(umax(v.x, v.y), umax(v.z, v.w)); }
DI bool allvalid(unsigned m) { return __builtin_amdgcn_ballot_w64(m == SENT) == 0ull; }
DI v4u lds4(unsigned a) { return *(LAS v4u*)(size_t)a; }
DI f4 ldsf4(unsigned a) { return *(LAS f4*)(size_t)a; }
DI void sts4(unsigned a, v4u v) { *(LAS v4u*)(size_t)a = v; }
DI void stsf4(unsigned a, f4 v) { *(LAS f4*)(size_t)a = v; }
DI void sts2(unsigned a, v2u v) { *(LAS v2u*)(size_t)a = v; }
DI void sts1(unsigned a, unsigned v) { *(LAS unsigned*)(size_t)a = v; }
DI v4u ldstr(unsigned a0, unsigned a1) {
  s4 lo = __builtin_amdgcn_ds_read_tr16_b64_v4i16((LAS s4*)(size_t)a0), hi = __builtin_amdgcn_ds_read_tr16_b64_v4i16((LAS s4*)(size_t)a1);
  s8 c = __builtin_shufflevector(lo, hi, 0, 1, 2, 3, 4, 5, 6, 7);
  return __builtin_bit_cast(v4u, c);
}

#define GLD4(dst, voff, sbase, IMM, MOD) asm volatile("global_load_dwordx4 %0, %1, %2 offset:%3 " MOD : "=v"(dst) : "v"(voff), "s"(sbase), "i"(IMM) : "memory")
#define GLD2(dst, voff, sbase, IMM, MOD) asm volatile("global_load_dwordx2 %0, %1, %2 offset:%3 " MOD : "=v"(dst) : "v"(voff), "s"(sbase), "i"(IMM) : "memory")
#define GLD1(dst, voff, sbase, IMM, MOD) asm volatile("global_load_dword %0, %1, %2 offset:%3 " MOD : "=v"(dst) : "v"(voff), "s"(sbase), "i"(IMM) : "memory")
#define GST4(src, voff, sbase, MOD) asm volatile("global_store_dwordx4 %0, %1, %2 " MOD "\n\ts_nop 1" :: "v"(voff), "v"(src), "s"(sbase) : "memory")
#define GST2(src, voff, sbase, MOD) asm volatile("global_store_dwordx2 %0, %1, %2 " MOD :: "v"(voff), "v"(src), "s"(sbase) : "memory")
#define SCHED() __builtin_amdgcn_sched_barrier(0)
#define VMWAIT(N) do { asm volatile("s_waitcnt vmcnt(%0)" :: "i"(N) : "memory"); SCHED(); } while (0)
#define LGWAIT() do { asm volatile("s_waitcnt lgkmcnt(0)" ::: "memory"); SCHED(); } while (0)
#define TIE(x) asm volatile("" : "+v"(x))
DI void dma16(unsigned voff, const void* sbase, unsigned ldsdst) {
  asm volatile("s_mov_b32 m0, %2\n\ts_nop 0\n\tglobal_load_lds_dwordx4 %0, %1" :: "v"(voff), "s"(sbase), "s"(ldsdst) : "memory", "m0");
}
#define BARRIER() do { asm volatile("s_waitcnt lgkmcnt(0)\n\ts_barrier" ::: "memory"); SCHED(); } while (0)
#define RAWBAR() do { asm volatile("s_barrier" ::: "memory"); SCHED(); } while (0)

#define STAMP(i) do { asm volatile("s_memrealtime %0\n\ts_waitcnt lgkmcnt(0)" : "=s"(ts[i]) :: "memory"); } while (0)
#define NPRE 4
#define NPRE2 8

__global__ void __launch_bounds__(512, 2)
gm_kernel(const char* __restrict__ x, const char* __restrict__ ckv, const char* __restrict__ wq, const char* __restrict__ wuk,
          const char* __restrict__ wuv, const char* __restrict__ wo, const char* __restrict__ w1, const char* __restrict__ w2,
          const char* __restrict__ gw, char* __restrict__ out, char* __restrict__ ws) {
  __shared__ __attribute__((aligned(16))) char smem[L_END];
  asm volatile("" :: "v"((LAS char*)smem));
  u64 ts[16];
#pragma unroll
  for (int i = 0; i < 16; ++i) ts[i] = 0;
  STAMP(0);
  const int tid = threadIdx.x, lane = tid & 63;
  const int w = __builtin_amdgcn_readfirstlane(tid >> 6);
  const int b = blockIdx.x, xcd = b & 7, r = b >> 3;
  const bool roleA = r < 16;
  const int g4 = lane >> 4, n16 = lane & 15;
  const int rr = r & 15;
  const int hq = xcd * 4 + (rr >> 2), j8 = (roleA ? 0 : 4) + (rr & 3);
  asm volatile("" :: "s"(x), "s"(ckv), "s"(wq), "s"(wuk), "s"(wuv), "s"(wo), "s"(w1), "s"(w2), "s"(gw), "s"(out), "s"(ws));
  u64 epoch_raw;
  asm volatile("s_load_dwordx2 %0, %1, 0x0" : "=s"(epoch_raw) : "s"(ws) : "memory");

  v4u wqr[4]; v2u xr;
#define Q_ISSUE(ROLE_A) \
  if (w == 0) { \
    v4u s_ = {SENT, SENT, SENT, SENT}; \
    unsigned vo = (unsigned)(OFF_QP + (hq * 8 + j8) * 256 + n16 * 16); \
    if (ROLE_A && lane >= 16) vo = (unsigned)(OFF_QA + hq * 1024 + j8 * 256 + n16 * 16); \
    if (lane < (ROLE_A ? 32 : 16)) GST4(s_, vo, ws, "sc1"); } \
  if (w < 5) { \
    const char* wb = wq + (size_t)(j8 * 80 + 16 * w) * 8192 + hq * 256; \
    const unsigned vo = (unsigned)(4 * g4 * 8192 + n16 * 16); \
    GLD2(xr, (unsigned)(8 * g4), x + (j8 * 80 + 16 * w) * 2, 0, ""); \
    GLD4(wqr[0], vo, wb, 0, ""); GLD4(wqr[1], vo, wb + 8192, 0, ""); GLD4(wqr[2], vo, wb + 16384, 0, ""); GLD4(wqr[3], vo, wb + 24576, 0, ""); } \
  RAWBAR();
#define Q_COMPUTE(NG2) \
  LGWAIT(); \
  u64 epoch = epoch_raw; asm volatile("" : "+s"(epoch)); epoch += 1; \
  if (w == 0) { \
    VMWAIT(5 + NG2); \
    if (lane == 0) { v2u f = {(unsigned)epoch, (unsigned)(epoch >> 32)}; GST2(f, (unsigned)(OFF_FLAG1 + b * 128), ws, "sc1"); } } \
  if (w < 5) { \
    if (w == 0) VMWAIT(NG2 + 1 + 4); else VMWAIT(NG2 + 4); \
    TIE(xr); \
    const unsigned d0 = __builtin_amdgcn_perm(xr.x, xr.x, 0x01000100u), d1 = __builtin_amdgcn_perm(xr.x, xr.x, 0x03020302u); \
    const unsigned d2 = __builtin_amdgcn_perm(xr.y, xr.y, 0x01000100u), d3 = __builtin_amdgcn_perm(xr.y, xr.y, 0x03020302u); \
    v4u a0 = {d0 & msk[0], d0 & msk[1], d0 & msk[2], d0 & msk[3]}, a1 = {d1 & msk[0], d1 & msk[1], d1 & msk[2], d1 & msk[3]}; \
    v4u a2 = {d2 & msk[0], d2 & msk[1], d2 & msk[2], d2 & msk[3]}, a3 = {d3 & msk[0], d3 & msk[1], d3 & msk[2], d3 & msk[3]}; \
    unsigned qr = L_QRED + w * 512 + (8 * n16 + 4 * g4) * 4; \
    TIE(a0); TIE(a1); TIE(a2); TIE(a3); TIE(qr); \
    if (w == 0) VMWAIT(NG2 + 1); else VMWAIT(NG2); \
    TIE(wqr[0]); TIE(wqr[1]); TIE(wqr[2]); TIE(wqr[3]); \
    STAMP(2); \
    f4 acc = {0.f, 0.f, 0.f, 0.f}; \
    acc = mfma(a0, wqr[0], acc); acc = mfma(a1, wqr[1], acc); acc = mfma(a2, wqr[2], acc); acc = mfma(a3, wqr[3], acc); \
    if (lane < 32) stsf4(qr, acc); } \
  BARRIER(); \
  if (w == 0) { \
    if (lane < 32) { \
      const unsigned ra = L_QRED + lane * 16; \
      f4 s0 = ldsf4(ra), s1 = ldsf4(ra + 512), s2 = ldsf4(ra + 1024), s3 = ldsf4(ra + 1536), s4_ = ldsf4(ra + 2048); \
      f4 sm = (s0 + s1) + (s2 + s3) + s4_; \
      v2u o = {guard(cvtpk(sm.x, sm.y)), guard(cvtpk(sm.z, sm.w))}; \
      GST2(o, (unsigned)(OFF_QP + (hq * 8 + j8) * 256 + lane * 8), ws, "sc1"); } \
    STAMP(3); }
#define FLAG1_CHECK(BLK) \
  for (unsigned spin = 0;; ++spin) { \
    v2u f0; \
    GLD2(f0, (unsigned)(OFF_FLAG1 + (BLK) * 128), ws, 0, "sc1"); \
    VMWAIT(0); TIE(f0); \
    const unsigned df = (f0.x ^ (unsigned)epoch) | (f0.y ^ (unsigned)(epoch >> 32)); \
    if (__builtin_amdgcn_ballot_w64(df != 0u) == 0ull || spin > SPIN_CAP) break; \
  }
#define FLAG2_CHECK() \
  for (unsigned spin = 0;; ++spin) { \
    v4u f0; \
    GLD4(f0, (unsigned)(OFF_FLAG2 + lane * 16), ws, 0, "sc1"); \
    VMWAIT(0); TIE(f0); \
    const unsigned lo = (unsigned)epoch, hi = (unsigned)(epoch >> 32); \
    const unsigned df = ((f0.x ^ lo) | (f0.y ^ hi)) | ((f0.z ^ lo) | (f0.w ^ hi)); \
    if (__builtin_amdgcn_ballot_w64(df != 0u) == 0ull || spin > SPIN_CAP) break; \
  }
  unsigned msk[4];
#pragma unroll
  for (int t = 0; t < 4; ++t) msk[t] = (((n16 & 7) >> 1) == t) ? ((n16 & 1) ? 0xFFFF0000u : 0x0000FFFFu) : 0u;

  if (roleA) {
    const int h = hq, j = j8;
    Q_ISSUE(true);
    STAMP(1);
    {
      const char* ub = wuk + (size_t)(128 * j + 16 * w) * 8192 + h * 256;
#pragma unroll
      for (int i = 0; i < 4; ++i)
        dma16((unsigned)((4 * i + g4) * 8192 + ((n16 ^ (4 * i + g4)) * 16)), ub, (unsigned)(L_UK + (16 * w + 4 * i) * 256));
    }
    Q_COMPUTE(4);
    if (w == 7) { const int j8l = lane & 7; const int blk = (h >> 2) + 8 * ((j8l >> 2) * 16 + (h & 3) * 4 + (j8l & 3)); FLAG1_CHECK(blk); STAMP(3); }
    else VMWAIT(0);
    v4u uk[4];
#pragma unroll
    for (int ks = 0; ks < 4; ++ks) uk[ks] = lds4(L_UK + (16 * w + n16) * 256 + (((4 * ks + g4) ^ n16) * 16));
    unsigned qb_a = L_QB + (n16 & 7) * 288 + g4 * 16;
    unsigned qa_o = (unsigned)(OFF_QA + h * 1024 + (128 * j + 16 * w + 4 * g4) * 2);
    TIE(qb_a); TIE(qa_o);
    if (w == 7) {
      v4u p0, p1;
      const unsigned vo = (unsigned)(OFF_QP + h * 2048 + lane * 16);
      unsigned wa = L_QB + g4 * 288 + n16 * 16; TIE(wa);
      for (unsigned spin = 0;; ++spin) {
        GLD4(p0, vo, ws, 0, "sc1"); GLD4(p1, vo, ws, 1024, "sc1");
        VMWAIT(0); TIE(p0); TIE(p1);
        if (allvalid(umax(mx4(p0), mx4(p1))) || spin > SPIN_CAP) break;
      }
      STAMP(4);
      sts4(wa, p0); sts4(wa + 4 * 288, p1);
    }
    BARRIER();
    {
      v4u bq[4];
#pragma unroll
      for (int ks = 0; ks < 4; ++ks) bq[ks] = lds4(qb_a + ks * 64);
      SCHED();
      f4 acc = {0.f, 0.f, 0.f, 0.f};
#pragma unroll
      for (int ks = 0; ks < 4; ++ks) acc = mfma(uk[ks], bq[ks], acc);
      float v0 = acc.x, v1 = acc.y, v2 = acc.z, v3 = acc.w;
      v0 += dpp<XOR1>(v0); v1 += dpp<XOR1>(v1); v2 += dpp<XOR1>(v2); v3 += dpp<XOR1>(v3);
      v0 += dpp<XOR2>(v0); v1 += dpp<XOR2>(v1); v2 += dpp<XOR2>(v2); v3 += dpp<XOR2>(v3);
      v0 += dpp<HMIR>(v0); v1 += dpp<HMIR>(v1); v2 += dpp<HMIR>(v2); v3 += dpp<HMIR>(v3);
      v2u o = {guard(cvtpk(v0, v1)), guard(cvtpk(v2, v3))};
      if (n16 == 0) GST2(o, qa_o, ws, "sc1");
    }
    STAMP(5);
    RAWBAR();
    {
      const char* vb = wuv + (size_t)(64 * w) * 8192 + h * 256 + j * 64;
#pragma unroll
      for (int i = 0; i < 4; ++i)
        dma16((unsigned)((16 * i + (lane >> 2)) * 8192 + (lane & 3) * 16), vb, (unsigned)(L_CKV + (64 * w + 16 * i) * 64));
    }
    v4u wor[8];
    if (w < 5) {
      const char* ob = wo + (size_t)(h * 128 + 32 * j) * 1280 + w * 256;
#pragma unroll
      for (int s = 0; s < 8; ++s) GLD4(wor[s], (unsigned)((8 * g4 + s) * 1280 + n16 * 16), ob, 0, "");
      VMWAIT(8);
    } else {
      VMWAIT(0);
    }
    v4u bv[2][2];
    {
      const int q = (lane & 15) >> 2, p = lane & 3;
      const unsigned a = L_CKV + (64 * w + 8 * g4 + q) * 64 + 8 * p;
#pragma unroll
      for (int ks = 0; ks < 2; ++ks)
#pragma unroll
        for (int dt = 0; dt < 2; ++dt) bv[ks][dt] = ldstr(a + ks * 2048 + dt * 32, a + ks * 2048 + 256 + dt * 32);
    }
    if (w == 7) { FLAG2_CHECK(); }
    unsigned pvo = (unsigned)(OFF_PP + (h * 32 + n16) * 1024 + 128 * w + 16 * g4), pvo2 = pvo + 16384u;
    unsigned sto = (unsigned)(OFF_ST + h * 256 + (lane & 31) * 8);
    unsigned tabw = L_TAB + w * 512 + lane * 4, tabr = L_TAB + w * 512 + g4 * 16;
    unsigned crw = L_RED + lane * 32 + w * 4, crr = L_RED + (lane & 31) * 32;
    unsigned tabr2 = L_TAB + w * 512 + ((n16 >> 3) * 32 + 8 * g4) * 4;
    unsigned opo = (unsigned)(OFF_OP + (16 * w + lane) * 2048 + (h * 4 + j) * 16);
    TIE(pvo); TIE(pvo2); TIE(sto); TIE(tabw); TIE(tabr); TIE(crw); TIE(crr); TIE(tabr2); TIE(opo);
    VMWAIT(0);
    if (w < 5) {
#pragma unroll
      for (int s = 0; s < 8; ++s) TIE(wor[s]);
    }
    STAMP(6);
    BARRIER();
    v2u st;
    for (unsigned spin = 0;; ++spin) {
      GLD2(st, sto, ws, 0, "sc1");
      VMWAIT(0); TIE(st);
      if (allvalid(umax(st.x, st.y)) || spin > SPIN_CAP) break;
    }
    v4u pa[2][2];
    GLD4(pa[0][0], pvo, ws, 0, "sc1"); GLD4(pa[0][1], pvo, ws, 64, "sc1");
    GLD4(pa[1][0], pvo2, ws, 0, "sc1"); GLD4(pa[1][1], pvo2, ws, 64, "sc1");
    STAMP(7);
    f4 aw[2];
    {
      float m = asf(st.x), l = asf(st.y);
      float M = p16_max(row_max(m));
      float a = __builtin_amdgcn_exp2f((m - M) * SC);
      float Ls = p16_sum(row_sum(a * l));
      a = a * __builtin_amdgcn_rcpf(Ls);
      if (lane < 32) sts1(tabw, asu(a));
      LGWAIT();
      aw[0] = ldsf4(tabr); aw[1] = ldsf4(tabr + 64);
    }
    for (unsigned spin = 0;; ++spin) {
      VMWAIT(0); TIE(pa[0][0]); TIE(pa[0][1]); TIE(pa[1][0]); TIE(pa[1][1]);
      if (allvalid(umax(umax(mx4(pa[0][0]), mx4(pa[0][1])), umax(mx4(pa[1][0]), mx4(pa[1][1])))) || spin > SPIN_CAP) break;
      GLD4(pa[0][0], pvo, ws, 0, "sc1"); GLD4(pa[0][1], pvo, ws, 64, "sc1");
      GLD4(pa[1][0], pvo2, ws, 0, "sc1"); GLD4(pa[1][1], pvo2, ws, 64, "sc1");
    }
    STAMP(8);
    {
      f4 z = {0.f, 0.f, 0.f, 0.f};
      f4 t00 = mfma(pa[0][0], bv[0][0], z), t10 = mfma(pa[1][0], bv[0][0], z), t01 = mfma(pa[0][0], bv[0][1], z), t11 = mfma(pa[1][0], bv[0][1], z);
      t00 = mfma(pa[0][1], bv[1][0], t00); t10 = mfma(pa[1][1], bv[1][0], t10); t01 = mfma(pa[0][1], bv[1][1], t01); t11 = mfma(pa[1][1], bv[1][1], t11);
      f4 c0 = aw[0] * t00 + aw[1] * t10, c1 = aw[0] * t01 + aw[1] * t11;
      float u0 = (c0.x + c0.y) + (c0.z + c0.w), u1 = (c1.x + c1.y) + (c1.z + c1.w);
      auto t = __builtin_amdgcn_permlane16_swap(asu(u0), asu(u1), false, false); unsigned ta = t[0], tb = t[1];
      float u = p32_sum(asf(ta) + asf(tb));
      if (lane < 32) sts1(crw, asu(u));
    }
    BARRIER();
    STAMP(9);
    if (w < 5) {
      float c = sum8(ldsf4(crr), ldsf4(crr + 16));
      unsigned hi = cvtpk(c, c);
      float rs = c - asf(hi << 16);
      unsigned lo = cvtpk(rs, rs);
      sts1(tabw, lane < 32 ? hi : lo);
      LGWAIT();
      v4u t0 = lds4(tabr2), t1 = lds4(tabr2 + 16);
      unsigned dv[8] = {t0.x, t0.y, t0.z, t0.w, t1.x, t1.y, t1.z, t1.w};
      f4 acc = {0.f, 0.f, 0.f, 0.f};
#pragma unroll
      for (int s = 0; s < 8; ++s) { v4u a = {dv[s] & msk[0], dv[s] & msk[1], dv[s] & msk[2], dv[s] & msk[3]}; acc = mfma(a, wor[s], acc); }
      float o0 = p32_sum(acc.x), o1 = p32_sum(acc.y), o2 = p32_sum(acc.z), o3 = p32_sum(acc.w);
      unsigned a0 = guard(cvtpk(o0, o1)), a1 = guard(cvtpk(o2, o3));
      auto s0 = __builtin_amdgcn_permlane16_swap(a0, a0, false, false); auto s1 = __builtin_amdgcn_permlane16_swap(a1, a1, false, false);
      unsigned b0 = s0[1], b1 = s1[1];
      v4u o = {a0, a1, b0, b1};
      if (lane < 16) GST4(o, opo, ws, "sc1");
    }
    STAMP(10);
  } else {
    const int g = rr & 3, s = xcd * 4 + (rr >> 2), bi = xcd * 16 + rr;
    Q_ISSUE(false);
    STAMP(1);
    const char* kb = ckv + (size_t)(128 * s + 16 * w) * 1024;
    const unsigned kl = L_CKV + (16 * w) * 1024;
    if (w < 5) {
#pragma unroll
      for (int i = 0; i < NPRE; ++i) dma16((unsigned)((lane ^ i) * 16), kb + i * 1024, kl + i * 1024);
      Q_COMPUTE(NPRE);
#pragma unroll
      for (int i = NPRE; i < 16; ++i) dma16((unsigned)((lane ^ i) * 16), kb + i * 1024, kl + i * 1024);
    } else {
#pragma unroll
      for (int i = 0; i < NPRE2; ++i) dma16((unsigned)((lane ^ i) * 16), kb + i * 1024, kl + i * 1024);
      BARRIER();
#pragma unroll
      for (int i = NPRE2; i < 16; ++i) dma16((unsigned)((lane ^ i) * 16), kb + i * 1024, kl + i * 1024);
      v4u s_ = {SENT, SENT, SENT, SENT};
      const char* cb = ws + OFF_CLR + (size_t)bi * CLR_PER + (w - 5) * 4096;
#pragma unroll
      for (int i = 0; i < 4; ++i) if (i < 3 || w < 7) GST4(s_, (unsigned)(lane * 16 + i * 1024), cb, "sc1");
      if (w == 5) { dma16((unsigned)(lane * 16), x, L_QB); if (lane < 16) dma16((unsigned)(lane * 16), x + 1024, L_QB + 1024); }
    }
    LGWAIT();
    u64 epochB = epoch_raw; asm volatile("" : "+s"(epochB)); epochB += 1;
#define epoch epochB
    if (w == 7) { const int hd = 8 * g + ((lane & 31) >> 2); const int blk = (hd >> 2) + 8 * ((hd & 3) * 4 + (lane & 3)); FLAG1_CHECK(blk); }
    else VMWAIT(0);
    STAMP(4);
    v4u kA[16];
    {
      const unsigned ka = L_CKV + (16 * w + n16) * 1024;
#pragma unroll
      for (int ks = 0; ks < 16; ++ks) kA[ks] = lds4(ka + (((4 * ks + g4) ^ n16) * 16));
    }
    unsigned qvo = (unsigned)(OFF_QA + (8 * g + w) * 1024 + lane * 16);
    unsigned qsw = L_QAS + w * 1056 + lane * 16, qsr = L_QAS + (n16 & 7) * 1056 + g4 * 16;
    unsigned mxw = L_MX + lane * 32 + w * 4, mxr = L_MX + (n16 & 7) * 32;
    unsigned pw = L_P + n16 * 288 + (16 * w + 4 * g4) * 2, pr = L_P + (n16 & 7) * 288 + g4 * 16;
    unsigned lsw = L_LS + lane * 32 + w * 4;
    unsigned ppo = (unsigned)(OFF_PP + ((8 * g + (n16 & 7)) * 32 + s) * 1024 + (64 * w + 32 * (g4 >> 1) + 8 * (2 * (n16 >> 3) + (g4 & 1))) * 2);
    TIE(qvo); TIE(qsw); TIE(qsr); TIE(mxw); TIE(mxr); TIE(pw); TIE(pr); TIE(lsw); TIE(ppo);
    BARRIER();
    if (w == 5 && lane == 0) { v2u f = {(unsigned)epoch, (unsigned)(epoch >> 32)}; GST2(f, (unsigned)(OFF_FLAG2 + bi * 8), ws, "sc1"); }
    STAMP(5);
    v4u qa;
    GLD4(qa, qvo, ws, 0, "sc1");
    v4u vA[4][4];
    {
      const int q = (lane & 15) >> 2, p = lane & 3;
#pragma unroll
      for (int t = 0; t < 4; ++t) {
        const int ch = 8 * w + 4 * (p >> 1) + t;
        const int r0 = 8 * g4 + q, r1 = r0 + 4;
        const unsigned a0 = L_CKV + r0 * 1024 + ((ch ^ (r0 & 15)) * 16) + 8 * (p & 1), a1 = L_CKV + r1 * 1024 + ((ch ^ (r1 & 15)) * 16) + 8 * (p & 1);
        vA[0][t] = ldstr(a0, a1); vA[1][t] = ldstr(a0 + 32768, a1 + 32768);
        vA[2][t] = ldstr(a0 + 65536, a1 + 65536); vA[3][t] = ldstr(a0 + 98304, a1 + 98304);
      }
    }
    SCHED();
    for (unsigned spin = 0;; ++spin) {
      VMWAIT(0); TIE(qa);
      if (allvalid(mx4(qa)) || spin > SPIN_CAP) break;
      GLD4(qa, qvo, ws, 0, "sc1");
    }
    STAMP(6);
    sts4(qsw, qa);
    BARRIER();
    f4 sc = {0.f, 0.f, 0.f, 0.f};
    {
      v4u bq[16];
#pragma unroll
      for (int ks = 0; ks < 16; ++ks) bq[ks] = lds4(qsr + ks * 64);
      SCHED();
#pragma unroll
      for (int ks = 0; ks < 16; ++ks) sc = mfma(kA[ks], bq[ks], sc);
      float mw = p32_max(p16_max(fmx(fmx(sc.x, sc.y), fmx(sc.z, sc.w))));
      if (lane < 8) sts1(mxw, asu(mw));
    }
    BARRIER();
    STAMP(7);
    {
      const float Ms = -max8(ldsf4(mxr), ldsf4(mxr + 16)) * SC;
      float p0 = __builtin_amdgcn_exp2f(fmaf(sc.x, SC, Ms)), p1 = __builtin_amdgcn_exp2f(fmaf(sc.y, SC, Ms)), p2 = __builtin_amdgcn_exp2f(fmaf(sc.z, SC, Ms)), p3 = __builtin_amdgcn_exp2f(fmaf(sc.w, SC, Ms));
      v2u pk = {cvtpk(p0, p1), cvtpk(p2, p3)};
      if (n16 < 8) sts2(pw, pk);
      float ls = p32_sum(p16_sum((p0 + p1) + (p2 + p3)));
      if (lane < 8) sts1(lsw, asu(ls));
    }
    BARRIER();
    STAMP(8);
    {
      v4u pb[4];
#pragma unroll
      for (int ks = 0; ks < 4; ++ks) pb[ks] = lds4(pr + ks * 64);
      f4 lu = ldsf4(L_LS + w * 32), lv = ldsf4(L_LS + w * 32 + 16), mu = ldsf4(L_MX + w * 32), mv = ldsf4(L_MX + w * 32 + 16);
      SCHED();
      f4 ac[4];
#pragma unroll
      for (int t = 0; t < 4; ++t) ac[t] = mfma(vA[0][t], pb[0], f4{0.f, 0.f, 0.f, 0.f});
      {
        v2u o = {guard(asu(max8(mu, mv))), guard(asu(sum8(lu, lv)))};
        if (lane == 0) GST2(o, (unsigned)(OFF_ST + ((8 * g + w) * 32 + s) * 8), ws, "sc1");
      }
#pragma unroll
      for (int ks = 1; ks < 4; ++ks)
#pragma unroll
        for (int t = 0; t < 4; ++t) ac[t] = mfma(vA[ks][t], pb[ks], ac[t]);
      const bool up = n16 >= 8;
      f4 xa = up ? ac[2] : ac[0], ya = up ? ac[3] : ac[1];
      unsigned x0 = cvtpk(xa.x, xa.y), x1 = cvtpk(xa.z, xa.w), y0 = cvtpk(ya.x, ya.y), y1 = cvtpk(ya.z, ya.w);
      auto s0 = __builtin_amdgcn_permlane16_swap(x0, y0, false, false); auto s1 = __builtin_amdgcn_permlane16_swap(x1, y1, false, false);
      unsigned e0 = s0[0], e1 = s1[0], f0 = s0[1], f1 = s1[1];
      v4u o = {guard(e0), guard(e1), guard(f0), guard(f1)};
      GST4(o, ppo, ws, "sc1");
    }
    STAMP(9);
    RAWBAR();
    const int fb = bi < 108 ? bi : 107;
    v4u w1r[3], w2r[4];
    {
      const char* b1 = w1 + fb * 32;
      const unsigned vo = (unsigned)((lane >> 1) * 3456 + (lane & 1) * 16);
      GLD4(w1r[0], vo, b1 + (size_t)(32 * w) * 3456, 0, "");
      GLD4(w1r[1], vo, b1 + (size_t)(32 * (w + 8)) * 3456, 0, "");
      if (w < 4) GLD4(w1r[2], vo, b1 + (size_t)(32 * (w + 16)) * 3456, 0, "");
    }
    if (w < 5) {
      const char* b2 = w2 + (size_t)(16 * fb) * 1280 + w * 256;
#pragma unroll
      for (int i = 0; i < 4; ++i) GLD4(w2r[i], (unsigned)((4 * g4 + i) * 1280 + n16 * 16), b2, 0, "");
      VMWAIT(4);
    } else {
      VMWAIT(0);
    }
    TIE(w1r[0]); TIE(w1r[1]);
    if (w < 4) { TIE(w1r[2]); } else { w1r[2] = v4u{0u, 0u, 0u, 0u}; }
    {
      float ha[8];
#pragma unroll
      for (int k = 0; k < 8; ++k) ha[k] = 0.f;
#pragma unroll
      for (int i = 0; i < 3; ++i) {
        const int it = (i == 2 && w >= 4) ? 0 : w + 8 * i;
        const float xv = asf((unsigned)*(LAS unsigned short*)(size_t)(L_QB + (32 * it + (lane >> 1)) * 2) << 16);
        ha[0] += xv * asf(w1r[i].x << 16); ha[1] += xv * asf(w1r[i].x & 0xFFFF0000u);
        ha[2] += xv * asf(w1r[i].y << 16); ha[3] += xv * asf(w1r[i].y & 0xFFFF0000u);
        ha[4] += xv * asf(w1r[i].z << 16); ha[5] += xv * asf(w1r[i].z & 0xFFFF0000u);
        ha[6] += xv * asf(w1r[i].w << 16); ha[7] += xv * asf(w1r[i].w & 0xFFFF0000u);
      }
#pragma unroll
      for (int k = 0; k < 8; ++k) { float v = ha[k]; v += dpp<XOR2>(v); v += dpp<ROR4>(v); v += dpp<ROR8>(v); ha[k] = p32_sum(p16_sum(v)); }
      if (lane < 2) {
#pragma unroll
        for (int k = 0; k < 8; ++k) sts1(L_RED + (8 * lane + k) * 32 + w * 4, asu(ha[k]));
      }
    }
    BARRIER();
    if (w < 5) {
      VMWAIT(0);
#pragma unroll
      for (int i = 0; i < 4; ++i) TIE(w2r[i]);
      const int f = lane & 15;
      float hh = sum8(ldsf4(L_RED + f * 32), ldsf4(L_RED + f * 32 + 16));
      hh = hh * __builtin_amdgcn_rcpf(1.f + __expf(-hh));
      if (bi >= 108) hh = 0.f;
      unsigned hi = cvtpk(hh, hh);
      float rs = hh - asf(hi << 16);
      unsigned lo = cvtpk(rs, rs);
      if (lane < 32) sts1(L_TAB + w * 512 + lane * 4, lane < 16 ? hi : lo);
      LGWAIT();
      v4u t0 = lds4(L_TAB + w * 512 + ((n16 >> 3) * 16 + 4 * g4) * 4);
      unsigned dv[4] = {t0.x, t0.y, t0.z, t0.w};
      f4 acc = {0.f, 0.f, 0.f, 0.f};
#pragma unroll
      for (int i = 0; i < 4; ++i) { v4u a = {dv[i] & msk[0], dv[i] & msk[1], dv[i] & msk[2], dv[i] & msk[3]}; acc = mfma(a, w2r[i], acc); }
      float o0 = p32_sum(acc.x), o1 = p32_sum(acc.y), o2 = p32_sum(acc.z), o3 = p32_sum(acc.w);
      unsigned a0 = guard(cvtpk(o0, o1)), a1 = guard(cvtpk(o2, o3));
      auto s0 = __builtin_amdgcn_permlane16_swap(a0, a0, false, false); auto s1 = __builtin_amdgcn_permlane16_swap(a1, a1, false, false);
      unsigned b0 = s0[1], b1 = s1[1];
      v4u o = {a0, a1, b0, b1};
      if (lane < 16) GST4(o, (unsigned)(OFF_MP + (16 * w + lane) * 2048 + bi * 16), ws, "sc1");
    }
    STAMP(10);
    if (bi < 80 && w == 5) {
      FLAG2_CHECK();
      unsigned gg[5];
#pragma unroll
      for (int i = 0; i < 5; ++i) GLD1(gg[i], (unsigned)(lane * 4), gw, i * 256, "");
      VMWAIT(0);
#pragma unroll
      for (int i = 0; i < 5; ++i) TIE(gg[i]);
      float gate;
      {
        float a = 0.f;
#pragma unroll
        for (int i = 0; i < 5; ++i) { const unsigned gx = *(LAS unsigned*)(size_t)(L_QB + i * 256 + lane * 4); a += asf(gx << 16) * asf(gg[i] << 16) + asf(gx & 0xFFFF0000u) * asf(gg[i] & 0xFFFF0000u); }
        a = p32_sum(p16_sum(row_sum(a)));
        gate = __builtin_amdgcn_rcpf(1.f + __expf(-a));
      }
      const unsigned one = 0x3F803F80u;
      v4u ones = {one & msk[0], one & msk[1], one & msk[2], one & msk[3]};
      v4u m0, m1;
      const unsigned mvo = (unsigned)(OFF_MP + bi * 2048 + lane * 16);
      for (unsigned spin = 0;; ++spin) {
        GLD4(m0, mvo, ws, 0, "sc1"); GLD4(m1, mvo, ws, 1024, "sc1");
        VMWAIT(0); TIE(m0); TIE(m1);
        if (allvalid(umax(mx4(m0), mx4(m1))) || spin > SPIN_CAP) break;
      }
      f4 bb = {0.f, 0.f, 0.f, 0.f};
      bb = mfma(ones, m0, bb); bb = mfma(ones, m1, bb);
      bb *= (1.f - gate);
      STAMP(11);
      const unsigned ovo = (unsigned)(OFF_OP + bi * 2048 + lane * 16);
      for (unsigned spin = 0;; ++spin) {
        GLD4(m0, ovo, ws, 0, "sc1"); GLD4(m1, ovo, ws, 1024, "sc1");
        VMWAIT(0); TIE(m0); TIE(m1);
        if (allvalid(umax(mx4(m0), mx4(m1))) || spin > SPIN_CAP) break;
      }
      STAMP(12);
      f4 aa = {0.f, 0.f, 0.f, 0.f};
      aa = mfma(ones, m0, aa); aa = mfma(ones, m1, aa);
      float r0 = row_sum(fmaf(gate, aa.x, bb.x)), r1 = row_sum(fmaf(gate, aa.y, bb.y));
      float r2 = row_sum(fmaf(gate, aa.z, bb.z)), r3 = row_sum(fmaf(gate, aa.w, bb.w));
      v2u o = {cvtpk(r0, r1), cvtpk(r2, r3)};
      if (n16 == 0 && g4 < 2) GST2(o, (unsigned)(bi * 16 + g4 * 8), out, "");
      if (bi == 0 && lane == 0) {
        v2u f = {(unsigned)epoch, (unsigned)(epoch >> 32)};
        GST2(f, (unsigned)OFF_E, ws, "sc1");
      }
      STAMP(13);
    }
#undef epoch
  }
  if (lane == 0) {
    u64* sp = (u64*)(ws + OFF_STAMP) + ((size_t)b * 8 + w) * 16;
#pragma unroll
    for (int i = 0; i < 16; ++i) sp[i] = ts[i];
  }
}

__global__ void k_amp(const u64* st, int role, int idx, int stat, int amp) {
  __shared__ u64 s0[256], s1[256];
  const int t = threadIdx.x;
  u64 mn = ~0ull, v = stat == 2 ? ~0ull : 0ull; bool any = false;
  for (int w = 0; w < 8; ++w) { const u64* q = st + ((size_t)t * 8 + w) * 16; mn = q[0] < mn ? q[0] : mn;
    const bool ok = role == 2 || ((t >> 3) >= 16) == (role == 1); const u64 x = q[idx];
    if (ok && x) { any = true; if (stat == 2) v = x < v ? x : v; else v = x > v ? x : v; } }
  s0[t] = mn; s1[t] = any ? v : 0ull;
  __syncthreads();
  if (t == 0) {
    mn = ~0ull; for (int i = 0; i < 256; ++i) mn = s0[i] < mn ? s0[i] : mn;
    u64 r = 0; int n = 0;
    if (stat == 0) { for (int i = 0; i < 256; ++i) r = s1[i] > r ? s1[i] : r; }
    else if (stat == 2) { r = ~0ull; for (int i = 0; i < 256; ++i) if (s1[i]) r = s1[i] < r ? s1[i] : r; }
    else {
      for (int i = 0; i < 256; ++i) if (s1[i]) n++;
      for (int i = 0; i < 256; ++i) if (s1[i]) { int c = 0; for (int k = 0; k < 256; ++k) if (s1[k] && (s1[k] < s1[i] || (s1[k] == s1[i] && k < i))) c++; if (c == n / 2) r = s1[i]; } }
    u64 q = r - mn; if (q > 5000) q = 5000;
    const u64 until = __builtin_amdgcn_s_memrealtime() + q * amp;
    while (__builtin_amdgcn_s_memrealtime() < until) __builtin_amdgcn_s_sleep(4);
  }
}

extern "C" void kernel_launch(void* const* d_in, const int* in_sizes, int n_in,
                              void* d_out, int out_size, void* d_ws, size_t ws_size,
                              hipStream_t stream) {
  gm_kernel<<<256, 512, 0, stream>>>((const char*)d_in[0], (const char*)d_in[1], (const char*)d_in[2], (const char*)d_in[3],
                                      (const char*)d_in[4], (const char*)d_in[5], (const char*)d_in[7], (const char*)d_in[8],
                                      (const char*)d_in[9], (char*)d_out, (char*)d_ws);
  k_amp<<<1, 256, 0, stream>>>((const u64*)((char*)d_ws + OFF_STAMP), 0, 2, 0, 20);
}
```

```cpp
#include <hip/hip_runtime.h>
#include <stdint.h>
#pragma clang diagnostic ignored "-Winline-asm"

typedef unsigned v4u __attribute__((ext_vector_type(4)));
typedef unsigned v2u __attribute__((ext_vector_type(2)));
typedef float f4 __attribute__((ext_vector_type(4)));
typedef float f2 __attribute__((ext_vector_type(2)));
typedef short s4 __attribute__((ext_vector_type(4)));
typedef short s8 __attribute__((ext_vector_type(8)));
typedef __bf16 bf2 __attribute__((ext_vector_type(2)));
typedef unsigned long long u64;
#define LAS __attribute__((address_space(3)))
#define DI __device__ __forceinline__

constexpr unsigned OFF_E = 0;
constexpr unsigned OFF_FLAG2 = 4096;
constexpr unsigned OFF_FLAG1 = 8192;
constexpr unsigned OFF_QP = OFF_FLAG1 + 32768;
constexpr unsigned OFF_QA = OFF_QP + 65536;
constexpr unsigned OFF_ST = OFF_QA + 32768;
constexpr unsigned OFF_OP = OFF_ST + 8192;
constexpr unsigned OFF_MP = OFF_OP + 163840;
constexpr unsigned OFF_PP = OFF_MP + 163840;
constexpr unsigned OFF_END = OFF_PP + 1048576;
constexpr unsigned OFF_STAMP = 8u << 20;
constexpr unsigned SENT = 0xFFFFFFFFu;
constexpr unsigned SPIN_CAP = 1u << 15;
constexpr float SC = 0.08838834764831845f * 1.4426950408889634f;

constexpr unsigned L_CKV = 0;
constexpr unsigned L_UK = 32768;
constexpr unsigned L_QAS = 131072;
constexpr unsigned L_QRED = L_QAS;
constexpr unsigned L_P = L_QAS + 8448;
constexpr unsigned L_MX = L_P + 2304;
constexpr unsigned L_LS = L_MX + 256;
constexpr unsigned L_RED = L_LS + 256;
constexpr unsigned L_TAB = L_RED + 1024;
constexpr unsigned L_QB = L_TAB + 4096;
constexpr unsigned L_EP = L_QB + 2304;
constexpr unsigned L_END = L_EP + 16;

DI f4 mfma(v4u a, v4u b, f4 c) { return __builtin_amdgcn_mfma_f32_16x16x32_bf16(__builtin_bit_cast(s8, a), __builtin_bit_cast(s8, b), c, 0, 0, 0); }
DI unsigned cvtpk(float lo, float hi) { f2 x = {lo, hi}; bf2 y = __builtin_convertvector(x, bf2); return __builtin_bit_cast(unsigned, y); }
DI float asf(unsigned u) { return __uint_as_float(u); }
DI unsigned asu(float f) { return __float_as_uint(f); }
DI unsigned guard(unsigned u) { return u < 0xFFFFFFFEu ? u : 0xFFFFFFFEu; }
DI float fmx(float a, float b) { return __builtin_elementwise_maximum(a, b); }
template <int CTRL> DI float dpp(float x) { return asf((unsigned)__builtin_amdgcn_mov_dpp((int)asu(x), CTRL, 0xf, 0xf, true)); }
constexpr int XOR1 = 0xB1, XOR2 = 0x4E, ROR4 = 0x124, ROR8 = 0x128, HMIR = 0x141;
DI float p16_max(float x) { auto t = __builtin_amdgcn_permlane16_swap(asu(x), asu(x), false, false); unsigned a = t[0], b = t[1]; return fmx(asf(a), asf(b)); }
DI float p32_max(float x) { auto t = __builtin_amdgcn_permlane32_swap(asu(x), asu(x), false, false); unsigned a = t[0], b = t[1]; return fmx(asf(a), asf(b)); }
DI float p16_sum(float x) { auto t = __builtin_amdgcn_permlane16_swap(asu(x), asu(x), false, false); unsigned a = t[0], b = t[1]; return asf(a) + asf(b); }
DI float p32_sum(float x) { auto t = __builtin_amdgcn_permlane32_swap(asu(x), asu(x), false, false); unsigned a = t[0], b = t[1]; return asf(a) + asf(b); }
DI float row_sum(float x) { x += dpp<XOR1>(x); x += dpp<XOR2>(x); x += dpp<ROR4>(x); x += dpp<ROR8>(x); return x; }
DI float row_max(float x) { x = fmx(x, dpp<XOR1>(x)); x = fmx(x, dpp<XOR2>(x)); x = fmx(x, dpp<ROR4>(x)); x = fmx(x, dpp<ROR8>(x)); return x; }
DI float max8(f4 u, f4 v) { return fmx(fmx(fmx(u.x, u.y), fmx(u.z, u.w)), fmx(fmx(v.x, v.y), fmx(v.z, v.w))); }
DI float sum8(f4 u, f4 v) { return ((u.x + u.y) + (u.z + u.w)) + ((v.x + v.y) + (v.z + v.w)); }
DI unsigned umax(unsigned a, unsigned b) { return a > b ? a : b; }
DI unsigned mx4(v4u v) { return umax(umax(v.x, v.y), umax(v.z, v.w)); }
DI bool allvalid(unsigned m) { return __builtin_amdgcn_ballot_w64(m == SENT) == 0ull; }
DI v4u lds4(unsigned a) { return *(LAS v4u*)(size_t)a; }
DI f4 ldsf4(unsigned a) { return *(LAS f4*)(size_t)a; }
DI void sts4(unsigned a, v4u v) { *(LAS v4u*)(size_t)a = v; }
DI void stsf4(unsigned a, f4 v) { *(LAS f4*)(size_t)a = v; }
DI void sts2(unsigned a, v2u v) { *(LAS v2u*)(size_t)a = v; }
DI void sts1(unsigned a, unsigned v) { *(LAS unsigned*)(size_t)a = v; }
DI v4u ldstr(unsigned a0, unsigned a1) {
  s4 lo = __builtin_amdgcn_ds_read_tr16_b64_v4i16((LAS s4*)(size_t)a0), hi = __builtin_amdgcn_ds_read_tr16_b64_v4i16((LAS s4*)(size_t)a1);
  s8 c = __builtin_shufflevector(lo, hi, 0, 1, 2, 3, 4, 5, 6, 7);
  return __builtin_bit_cast(v4u, c);
}

#define GLD4(dst, voff, sbase, IMM, MOD) asm volatile("global_load_dwordx4 %0, %1, %2 offset:%3 " MOD : "=v"(dst) : "v"(voff), "s"(sbase), "i"(IMM) : "memory")
#define GLD2(dst, voff, sbase, IMM, MOD) asm volatile("global_load_dwordx2 %0, %1, %2 offset:%3 " MOD : "=v"(dst) : "v"(voff), "s"(sbase), "i"(IMM) : "memory")
#define GLD1(dst, voff, sbase, IMM, MOD) asm volatile("global_load_dword %0, %1, %2 offset:%3 " MOD : "=v"(dst) : "v"(voff), "s"(sbase), "i"(IMM) : "memory")
#define GST4(src, voff, sbase, MOD) asm volatile("global_store_dwordx4 %0, %1, %2 " MOD "\n\ts_nop 1" :: "v"(voff), "v"(src), "s"(sbase) : "memory")
#define GST2(src, voff, sbase, MOD) asm volatile("global_store_dwordx2 %0, %1, %2 " MOD :: "v"(voff), "v"(src), "s"(sbase) : "memory")
#define SCHED() __builtin_amdgcn_sched_barrier(0)
#define VMWAIT(N) do { asm volatile("s_waitcnt vmcnt(%0)" :: "i"(N) : "memory"); SCHED(); } while (0)
#define LGWAIT() do { asm volatile("s_waitcnt lgkmcnt(0)" ::: "memory"); SCHED(); } while (0)
#define TIE(x) asm volatile("" : "+v"(x))
DI void dma16(unsigned voff, const void* sbase, unsigned ldsdst) {
  asm volatile("s_mov_b32 m0, %2\n\ts_nop 0\n\tglobal_load_lds_dwordx4 %0, %1" :: "v"(voff), "s"(sbase), "s"(ldsdst) : "memory", "m0");
}
#define BARRIER() do { asm volatile("s_waitcnt lgkmcnt(0)\n\ts_barrier" ::: "memory"); SCHED(); } while (0)
#define RAWBAR() do { asm volatile("s_barrier" ::: "memory"); SCHED(); } while (0)

#define STAMP(i) do {} while (0)
#define NPRE 4
#define NPRE2 8

__global__ void __launch_bounds__(512, 2)
gm_kernel(const char* __restrict__ x, const char* __restrict__ ckv, const char* __restrict__ wq, const char* __restrict__ wuk,
          const char* __restrict__ wuv, const char* __restrict__ wo, const char* __restrict__ w1, const char* __restrict__ w2,
          const char* __restrict__ gw, char* __restrict__ out, char* __restrict__ ws) {
  __shared__ __attribute__((aligned(16))) char smem[L_END];
  asm volatile("" :: "v"((LAS char*)smem));
  STAMP(0);
  const int tid = threadIdx.x, lane = tid & 63;
  const int w = __builtin_amdgcn_readfirstlane(tid >> 6);
  const int b = blockIdx.x, xcd = b & 7, r = b >> 3;
  const bool roleA = r < 16;
  const int g4 = lane >> 4, n16 = lane & 15;
  const int rr = r & 15;
  const int hq = xcd * 4 + (rr >> 2), j8 = (roleA ? 0 : 4) + (rr & 3);
  asm volatile("" :: "s"(x), "s"(ckv), "s"(wq), "s"(wuk), "s"(wuv), "s"(wo), "s"(w1), "s"(w2), "s"(gw), "s"(out), "s"(ws));
  v4u wqr[4]; v2u xr, ew;
#define Q_ISSUE(ROLE_A) \
  if (w == 0) { \
    GLD2(ew, 0u, ws, OFF_E, "sc1");        \
    v4u s_ = {SENT, SENT, SENT, SENT}; \
    unsigned vo = (unsigned)(OFF_QP + (hq * 8 + j8) * 256 + n16 * 16); \
    if (ROLE_A) { \
      if (g4 == 1) vo = (unsigned)(OFF_QA + hq * 1024 + j8 * 256 + n16 * 16); \
      if (g4 == 2) vo = (unsigned)(OFF_OP + (64 + n16) * 2048 + (hq * 4 + j8) * 16); \
      if (lane < 48) GST4(s_, vo, ws, "sc1"); \
      GST4(s_, (unsigned)(OFF_OP + lane * 2048 + (hq * 4 + j8) * 16), ws, "sc1"); \
    } else { \
      if (lane < 16) GST4(s_, vo, ws, "sc1"); \
    } } \
  if (w < 5) { \
    const char* wb = wq + (size_t)(j8 * 80 + 16 * w) * 8192 + hq * 256; \
    const unsigned vo = (unsigned)(4 * g4 * 8192 + n16 * 16); \
    GLD2(xr, (unsigned)(8 * g4), x + (j8 * 80 + 16 * w) * 2, 0, ""); \
    GLD4(wqr[0], vo, wb, 0, ""); GLD4(wqr[1], vo, wb + 8192, 0, ""); GLD4(wqr[2], vo, wb + 16384, 0, ""); GLD4(wqr[3], vo, wb + 24576, 0, ""); } \
  RAWBAR();
#define Q_COMPUTE(NG2, ROLE_A) \
  if (w == 0) { \
    VMWAIT(5 + NG2); TIE(ew); \
    const u64 e1 = (((u64)ew.y << 32) | ew.x) + 1; \
    v2u f = {(unsigned)e1, (unsigned)(e1 >> 32)}; \
      \
    if (lane < (ROLE_A ? 2 : 1)) GST2(f, (unsigned)(lane == 0 ? OFF_FLAG1 + b * 128 : OFF_FLAG2 + b * 8), ws, "sc1"); \
    if (lane == 0) sts2(L_EP, f); } \
  if (w < 5) { \
    if (w == 0) VMWAIT(NG2 + 1 + 4); else VMWAIT(NG2 + 4); \
    TIE(xr); \
    const unsigned d0 = __builtin_amdgcn_perm(xr.x, xr.x, 0x01000100u), d1 = __builtin_amdgcn_perm(xr.x, xr.x, 0x03020302u); \
    const unsigned d2 = __builtin_amdgcn_perm(xr.y, xr.y, 0x01000100u), d3 = __builtin_amdgcn_perm(xr.y, xr.y, 0x03020302u); \
    v4u a0 = {d0 & msk[0], d0 & msk[1], d0 & msk[2], d0 & msk[3]}, a1 = {d1 & msk[0], d1 & msk[1], d1 & msk[2], d1 & msk[3]}; \
    v4u a2 = {d2 & msk[0], d2 & msk[1], d2 & msk[2], d2 & msk[3]}, a3 = {d3 & msk[0], d3 & msk[1], d3 & msk[2], d3 & msk[3]}; \
    unsigned qr = L_QRED + w * 512 + (8 * n16 + 4 * g4) * 4; \
    TIE(a0); TIE(a1); TIE(a2); TIE(a3); TIE(qr); \
    if (w == 0) VMWAIT(NG2 + 1); else VMWAIT(NG2); \
    TIE(wqr[0]); TIE(wqr[1]); TIE(wqr[2]); TIE(wqr[3]); \
    STAMP(2); \
    f4 acc = {0.f, 0.f, 0.f, 0.f}; \
    acc = mfma(a0, wqr[0], acc); acc = mfma(a1, wqr[1], acc); acc = mfma(a2, wqr[2], acc); acc = mfma(a3, wqr[3], acc); \
    if (lane < 32) stsf4(qr, acc); } \
  BARRIER(); \
  if (w == 0) { \
    if (lane < 32) { \
      const unsigned ra = L_QRED + lane * 16; \
      f4 s0 = ldsf4(ra), s1 = ldsf4(ra + 512), s2 = ldsf4(ra + 1024), s3 = ldsf4(ra + 1536), s4_ = ldsf4(ra + 2048); \
      f4 sm = (s0 + s1) + (s2 + s3) + s4_; \
      v2u o = {guard(cvtpk(sm.x, sm.y)), guard(cvtpk(sm.z, sm.w))}; \
      GST2(o, (unsigned)(OFF_QP + (hq * 8 + j8) * 256 + lane * 8), ws, "sc1"); } \
    STAMP(3); }
#define EPOCH() ({ v2u e_ = *(LAS v2u*)(size_t)L_EP; (((u64)(unsigned)__builtin_amdgcn_readfirstlane(e_.y)) << 32) | (unsigned)__builtin_amdgcn_readfirstlane(e_.x); })
#define FLAG1_CHECK(BLK) \
  for (unsigned spin = 0;; ++spin) { \
    v2u f0; \
    GLD2(f0, (unsigned)(OFF_FLAG1 + (BLK) * 128), ws, 0, "sc1"); \
    VMWAIT(0); TIE(f0); \
    const unsigned df = (f0.x ^ (unsigned)epoch) | (f0.y ^ (unsigned)(epoch >> 32)); \
    if (__builtin_amdgcn_ballot_w64(df != 0u) == 0ull || spin > SPIN_CAP) break; \
  }
#define FLAG2_CHECK(HALF) \
  for (unsigned spin = 0;; ++spin) { \
    v4u f0; \
    GLD4(f0, (unsigned)(OFF_FLAG2 + (HALF) * 1024 + lane * 16), ws, 0, "sc1"); \
    VMWAIT(0); TIE(f0); \
    const unsigned lo = (unsigned)epoch, hi = (unsigned)(epoch >> 32); \
    const unsigned df = ((f0.x ^ lo) | (f0.y ^ hi)) | ((f0.z ^ lo) | (f0.w ^ hi)); \
    if (__builtin_amdgcn_ballot_w64(df != 0u) == 0ull || spin > SPIN_CAP) break; \
  }
  unsigned msk[4];
#pragma unroll
  for (int t = 0; t < 4; ++t) msk[t] = (((n16 & 7) >> 1) == t) ? ((n16 & 1) ? 0xFFFF0000u : 0x0000FFFFu) : 0u;

  if (roleA) {
    const int h = hq, j = j8;
    Q_ISSUE(true);
    STAMP(1);
    {
      const char* ub = wuk + (size_t)(128 * j + 16 * w) * 8192 + h * 256;
#pragma unroll
      for (int i = 0; i < 4; ++i)
        dma16((unsigned)((4 * i + g4) * 8192 + ((n16 ^ (4 * i + g4)) * 16)), ub, (unsigned)(L_UK + (16 * w + 4 * i) * 256));
    }
    Q_COMPUTE(4, true);
    const u64 epoch = EPOCH();
    if (w == 7) { const int j8l = lane & 7; const int blk = (h >> 2) + 8 * ((j8l >> 2) * 16 + (h & 3) * 4 + (j8l & 3)); FLAG1_CHECK(blk); STAMP(3); }
    else VMWAIT(0);
    v4u uk[4];
#pragma unroll
    for (int ks = 0; ks < 4; ++ks) uk[ks] = lds4(L_UK + (16 * w + n16) * 256 + (((4 * ks + g4) ^ n16) * 16));
    unsigned qb_a = L_QB + (n16 & 7) * 288 + g4 * 16;
    unsigned qa_o = (unsigned)(OFF_QA + h * 1024 + (128 * j + 16 * w + 4 * g4) * 2);
    TIE(qb_a); TIE(qa_o);
    if (w == 7) {
      v4u p0, p1;
      const unsigned vo = (unsigned)(OFF_QP + h * 2048 + lane * 16);
      unsigned wa = L_QB + g4 * 288 + n16 * 16; TIE(wa);
      for (unsigned spin = 0;; ++spin) {
        GLD4(p0, vo, ws, 0, "sc1"); GLD4(p1, vo, ws, 1024, "sc1");
        VMWAIT(0); TIE(p0); TIE(p1);
        if (allvalid(umax(mx4(p0), mx4(p1))) || spin > SPIN_CAP) break;
      }
      STAMP(4);
      sts4(wa, p0); sts4(wa + 4 * 288, p1);
    }
    BARRIER();
    {
      v4u bq[4];
#pragma unroll
      for (int ks = 0; ks < 4; ++ks) bq[ks] = lds4(qb_a + ks * 64);
      SCHED();
      f4 acc = {0.f, 0.f, 0.f, 0.f};
#pragma unroll
      for (int ks = 0; ks < 4; ++ks) acc = mfma(uk[ks], bq[ks], acc);
      float v0 = acc.x, v1 = acc.y, v2 = acc.z, v3 = acc.w;
      v0 += dpp<XOR1>(v0); v1 += dpp<XOR1>(v1); v2 += dpp<XOR1>(v2); v3 += dpp<XOR1>(v3);
      v0 += dpp<XOR2>(v0); v1 += dpp<XOR2>(v1); v2 += dpp<XOR2>(v2); v3 += dpp<XOR2>(v3);
      v0 += dpp<HMIR>(v0); v1 += dpp<HMIR>(v1); v2 += dpp<HMIR>(v2); v3 += dpp<HMIR>(v3);
      v2u o = {guard(cvtpk(v0, v1)), guard(cvtpk(v2, v3))};
      if (n16 == 0) GST2(o, qa_o, ws, "sc1");
    }
    STAMP(5);
    RAWBAR();
    {
      const char* vb = wuv + (size_t)(64 * w) * 8192 + h * 256 + j * 64;
#pragma unroll
      for (int i = 0; i < 4; ++i)
        dma16((unsigned)((16 * i + (lane >> 2)) * 8192 + (lane & 3) * 16), vb, (unsigned)(L_CKV + (64 * w + 16 * i) * 64));
    }
    v4u wor[8];
    if (w < 5) {
      const char* ob = wo + (size_t)(h * 128 + 32 * j) * 1280 + w * 256;
#pragma unroll
      for (int s = 0; s < 8; ++s) GLD4(wor[s], (unsigned)((8 * g4 + s) * 1280 + n16 * 16), ob, 0, "");
      VMWAIT(8);
    } else {
      VMWAIT(0);
    }
    v4u bv[2][2];
    {
      const int q = (lane & 15) >> 2, p = lane & 3;
      const unsigned a = L_CKV + (64 * w + 8 * g4 + q) * 64 + 8 * p;
#pragma unroll
      for (int ks = 0; ks < 2; ++ks)
#pragma unroll
        for (int dt = 0; dt < 2; ++dt) bv[ks][dt] = ldstr(a + ks * 2048 + dt * 32, a + ks * 2048 + 256 + dt * 32);
    }
    if (w == 7) { FLAG2_CHECK(1); }
    unsigned pvo = (unsigned)(OFF_PP + (h * 32 + n16) * 1024 + 128 * w + 16 * g4), pvo2 = pvo + 16384u;
    unsigned sto = (unsigned)(OFF_ST + h * 256 + (lane & 31) * 8);
    unsigned tabw = L_TAB + w * 512 + lane * 4, tabr = L_TAB + w * 512 + g4 * 16;
    unsigned crw = L_RED + lane * 32 + w * 4, crr = L_RED + (lane & 31) * 32;
    unsigned tabr2 = L_TAB + w * 512 + ((n16 >> 3) * 32 + 8 * g4) * 4;
    unsigned opo = (unsigned)(OFF_OP + (16 * w + lane) * 2048 + (h * 4 + j) * 16);
    TIE(pvo); TIE(pvo2); TIE(sto); TIE(tabw); TIE(tabr); TIE(crw); TIE(crr); TIE(tabr2); TIE(opo);
    VMWAIT(0);
    if (w < 5) {
#pragma unroll
      for (int s = 0; s < 8; ++s) TIE(wor[s]);
    }
    STAMP(6);
    BARRIER();
    v2u st;
    for (unsigned spin = 0;; ++spin) {
      GLD2(st, sto, ws, 0, "sc1");
      VMWAIT(0); TIE(st);
      if (allvalid(umax(st.x, st.y)) || spin > SPIN_CAP) break;
    }
    v4u pa[2][2];
    GLD4(pa[0][0], pvo, ws, 0, "sc1"); GLD4(pa[0][1], pvo, ws, 64, "sc1");
    GLD4(pa[1][0], pvo2, ws, 0, "sc1"); GLD4(pa[1][1], pvo2, ws, 64, "sc1");
    STAMP(7);
    f4 aw[2];
    {
      float m = asf(st.x), l = asf(st.y);
      float M = p16_max(row_max(m));
      float a = __builtin_amdgcn_exp2f((m - M) * SC);
      float Ls = p16_sum(row_sum(a * l));
      a = a * __builtin_amdgcn_rcpf(Ls);
      if (lane < 32) sts1(tabw, asu(a));
      LGWAIT();
      aw[0] = ldsf4(tabr); aw[1] = ldsf4(tabr + 64);
    }
    for (unsigned spin = 0;; ++spin) {
      VMWAIT(0); TIE(pa[0][0]); TIE(pa[0][1]); TIE(pa[1][0]); TIE(pa[1][1]);
      if (allvalid(umax(umax(mx4(pa[0][0]), mx4(pa[0][1])), umax(mx4(pa[1][0]), mx4(pa[1][1])))) || spin > SPIN_CAP) break;
      GLD4(pa[0][0], pvo, ws, 0, "sc1"); GLD4(pa[0][1], pvo, ws, 64, "sc1");
      GLD4(pa[1][0], pvo2, ws, 0, "sc1"); GLD4(pa[1][1], pvo2, ws, 64, "sc1");
    }
    STAMP(8);
    {
      f4 z = {0.f, 0.f, 0.f, 0.f};
      f4 t00 = mfma(pa[0][0], bv[0][0], z), t10 = mfma(pa[1][0], bv[0][0], z), t01 = mfma(pa[0][0], bv[0][1], z), t11 = mfma(pa[1][0], bv[0][1], z);
      t00 = mfma(pa[0][1], bv[1][0], t00); t10 = mfma(pa[1][1], bv[1][0], t10); t01 = mfma(pa[0][1], bv[1][1], t01); t11 = mfma(pa[1][1], bv[1][1], t11);
      f4 c0 = aw[0] * t00 + aw[1] * t10, c1 = aw[0] * t01 + aw[1] * t11;
      float u0 = (c0.x + c0.y) + (c0.z + c0.w), u1 = (c1.x + c1.y) + (c1.z + c1.w);
      auto t = __builtin_amdgcn_permlane16_swap(asu(u0), asu(u1), false, false); unsigned ta = t[0], tb = t[1];
      float u = p32_sum(asf(ta) + asf(tb));
      if (lane < 32) sts1(crw, asu(u));
    }
    BARRIER();
    STAMP(9);
    if (w < 5) {
      float c = sum8(ldsf4(crr), ldsf4(crr + 16));
      unsigned hi = cvtpk(c, c);
      float rs = c - asf(hi << 16);
      unsigned lo = cvtpk(rs, rs);
      sts1(tabw, lane < 32 ? hi : lo);
      LGWAIT();
      v4u t0 = lds4(tabr2), t1 = lds4(tabr2 + 16);
      unsigned dv[8] = {t0.x, t0.y, t0.z, t0.w, t1.x, t1.y, t1.z, t1.w};
      f4 acc = {0.f, 0.f, 0.f, 0.f};
#pragma unroll
      for (int s = 0; s < 8; ++s) { v4u a = {dv[s] & msk[0], dv[s] & msk[1], dv[s] & msk[2], dv[s] & msk[3]}; acc = mfma(a, wor[s], acc); }
      float o0 = p32_sum(acc.x), o1 = p32_sum(acc.y), o2 = p32_sum(acc.z), o3 = p32_sum(acc.w);
      unsigned a0 = guard(cvtpk(o0, o1)), a1 = guard(cvtpk(o2, o3));
      auto s0 = __builtin_amdgcn_permlane16_swap(a0, a0, false, false); auto s1 = __builtin_amdgcn_permlane16_swap(a1, a1, false, false);
      unsigned b0 = s0[1], b1 = s1[1];
      v4u o = {a0, a1, b0, b1};
      if (lane < 16) GST4(o, opo, ws, "sc1");
    }
    STAMP(10);
  } else {
    const int g = rr & 3, s = xcd * 4 + (rr >> 2), bi = xcd * 16 + rr;
    Q_ISSUE(false);
    STAMP(1);
    const char* kb = ckv + (size_t)(128 * s + 16 * w) * 1024;
    const unsigned kl = L_CKV + (16 * w) * 1024;
    if (w < 5) {
#pragma unroll
      for (int i = 0; i < NPRE; ++i) dma16((unsigned)((lane ^ i) * 16), kb + i * 1024, kl + i * 1024);
      Q_COMPUTE(NPRE, false);
#pragma unroll
      for (int i = NPRE; i < 16; ++i) dma16((unsigned)((lane ^ i) * 16), kb + i * 1024, kl + i * 1024);
    } else {
#pragma unroll
      for (int i = 0; i < NPRE2; ++i) dma16((unsigned)((lane ^ i) * 16), kb + i * 1024, kl + i * 1024);
      BARRIER();
#pragma unroll
      for (int i = NPRE2; i < 16; ++i) dma16((unsigned)((lane ^ i) * 16), kb + i * 1024, kl + i * 1024);
      v4u s_ = {SENT, SENT, SENT, SENT};
#pragma unroll
      for (int i = 0; i < 3; ++i) { const int hh = 3 * (w - 5) + i; if (hh < 8) GST4(s_, (unsigned)(OFF_PP + ((8 * g + hh) * 32 + s) * 1024 + lane * 16), ws, "sc1"); }
      if (w == 7) {
        v2u s2 = {SENT, SENT};
        if (lane < 8) GST2(s2, (unsigned)(OFF_ST + ((8 * g + lane) * 32 + s) * 8), ws, "sc1");
        GST4(s_, (unsigned)(OFF_MP + lane * 2048 + bi * 16), ws, "sc1");
        if (lane < 16) GST4(s_, (unsigned)(OFF_MP + (64 + lane) * 2048 + bi * 16), ws, "sc1");
      }
      if (w == 5) { dma16((unsigned)(lane * 16), x, L_QB); if (lane < 16) dma16((unsigned)(lane * 16), x + 1024, L_QB + 1024); }
    }
    const u64 epoch = EPOCH();
    if (w == 7) { const int hd = 8 * g + ((lane & 31) >> 2); const int blk = (hd >> 2) + 8 * ((hd & 3) * 4 + (lane & 3)); FLAG1_CHECK(blk); }
    else VMWAIT(0);
    STAMP(4);
    v4u kA[16];
    {
      const unsigned ka = L_CKV + (16 * w + n16) * 1024;
#pragma unroll
      for (int ks = 0; ks < 16; ++ks) kA[ks] = lds4(ka + (((4 * ks + g4) ^ n16) * 16));
    }
    unsigned qvo = (unsigned)(OFF_QA + (8 * g + w) * 1024 + lane * 16);
    unsigned qsw = L_QAS + w * 1056 + lane * 16, qsr = L_QAS + (n16 & 7) * 1056 + g4 * 16;
    unsigned mxw = L_MX + lane * 32 + w * 4, mxr = L_MX + (n16 & 7) * 32;
    unsigned pw = L_P + n16 * 288 + (16 * w + 4 * g4) * 2, pr = L_P + (n16 & 7) * 288 + g4 * 16;
    unsigned lsw = L_LS + lane * 32 + w * 4;
    unsigned ppo = (unsigned)(OFF_PP + ((8 * g + (n16 & 7)) * 32 + s) * 1024 + (64 * w + 32 * (g4 >> 1) + 8 * (2 * (n16 >> 3) + (g4 & 1))) * 2);
    TIE(qvo); TIE(qsw); TIE(qsr); TIE(mxw); TIE(mxr); TIE(pw); TIE(pr); TIE(lsw); TIE(ppo);
    BARRIER();
    if (w == 5 && lane == 0) { v2u f = {(unsigned)epoch, (unsigned)(epoch >> 32)}; GST2(f, (unsigned)(OFF_FLAG2 + b * 8), ws, "sc1"); }
    STAMP(5);
    v4u qa;
    GLD4(qa, qvo, ws, 0, "sc1");
    v4u vA[4][4];
    {
      const int q = (lane & 15) >> 2, p = lane & 3;
#pragma unroll
      for (int t = 0; t < 4; ++t) {
        const int ch = 8 * w + 4 * (p >> 1) + t;
        const int r0 = 8 * g4 + q, r1 = r0 + 4;
        const unsigned a0 = L_CKV + r0 * 1024 + ((ch ^ (r0 & 15)) * 16) + 8 * (p & 1), a1 = L_CKV + r1 * 1024 + ((ch ^ (r1 & 15)) * 16) + 8 * (p & 1);
        vA[0][t] = ldstr(a0, a1); vA[1][t] = ldstr(a0 + 32768, a1 + 32768);
        vA[2][t] = ldstr(a0 + 65536, a1 + 65536); vA[3][t] = ldstr(a0 + 98304, a1 + 98304);
      }
    }
    SCHED();
    for (unsigned spin = 0;; ++spin) {
      VMWAIT(0); TIE(qa);
      if (allvalid(mx4(qa)) || spin > SPIN_CAP) break;
      GLD4(qa, qvo, ws, 0, "sc1");
    }
    STAMP(6);
    sts4(qsw, qa);
    BARRIER();
    f4 sc = {0.f, 0.f, 0.f, 0.f};
    {
      v4u bq[16];
#pragma unroll
      for (int ks = 0; ks < 16; ++ks) bq[ks] = lds4(qsr + ks * 64);
      SCHED();
#pragma unroll
      for (int ks = 0; ks < 16; ++ks) sc = mfma(kA[ks], bq[ks], sc);
      float mw = p32_max(p16_max(fmx(fmx(sc.x, sc.y), fmx(sc.z, sc.w))));
      if (lane < 8) sts1(mxw, asu(mw));
    }
    BARRIER();
    STAMP(7);
    {
      const float Ms = -max8(ldsf4(mxr), ldsf4(mxr + 16)) * SC;
      float p0 = __builtin_amdgcn_exp2f(fmaf(sc.x, SC, Ms)), p1 = __builtin_amdgcn_exp2f(fmaf(sc.y, SC, Ms)), p2 = __builtin_amdgcn_exp2f(fmaf(sc.z, SC, Ms)), p3 = __builtin_amdgcn_exp2f(fmaf(sc.w, SC, Ms));
      v2u pk = {cvtpk(p0, p1), cvtpk(p2, p3)};
      if (n16 < 8) sts2(pw, pk);
      float ls = p32_sum(p16_sum((p0 + p1) + (p2 + p3)));
      if (lane < 8) sts1(lsw, asu(ls));
    }
    BARRIER();
    STAMP(8);
    {
      v4u pb[4];
#pragma unroll
      for (int ks = 0; ks < 4; ++ks) pb[ks] = lds4(pr + ks * 64);
      f4 lu = ldsf4(L_LS + w * 32), lv = ldsf4(L_LS + w * 32 + 16), mu = ldsf4(L_MX + w * 32), mv = ldsf4(L_MX + w * 32 + 16);
      SCHED();
      f4 ac[4];
#pragma unroll
      for (int t = 0; t < 4; ++t) ac[t] = mfma(vA[0][t], pb[0], f4{0.f, 0.f, 0.f, 0.f});
      {
        v2u o = {guard(asu(max8(mu, mv))), guard(asu(sum8(lu, lv)))};
        if (lane == 0) GST2(o, (unsigned)(OFF_ST + ((8 * g + w) * 32 + s) * 8), ws, "sc1");
      }
#pragma unroll
      for (int ks = 1; ks < 4; ++ks)
#pragma unroll
        for (int t = 0; t < 4; ++t) ac[t] = mfma(vA[ks][t], pb[ks], ac[t]);
      const bool up = n16 >= 8;
      f4 xa = up ? ac[2] : ac[0], ya = up ? ac[3] : ac[1];
      unsigned x0 = cvtpk(xa.x, xa.y), x1 = cvtpk(xa.z, xa.w), y0 = cvtpk(ya.x, ya.y), y1 = cvtpk(ya.z, ya.w);
      auto s0 = __builtin_amdgcn_permlane16_swap(x0, y0, false, false); auto s1 = __builtin_amdgcn_permlane16_swap(x1, y1, false, false);
      unsigned e0 = s0[0], e1 = s1[0], f0 = s0[1], f1 = s1[1];
      v4u o = {guard(e0), guard(e1), guard(f0), guard(f1)};
      GST4(o, ppo, ws, "sc1");
    }
    STAMP(9);
    RAWBAR();
    const int fb = bi < 108 ? bi : 107;
    v4u w1r[3], w2r[4];
    {
      const char* b1 = w1 + fb * 32;
      const unsigned vo = (unsigned)((lane >> 1) * 3456 + (lane & 1) * 16);
      GLD4(w1r[0], vo, b1 + (size_t)(32 * w) * 3456, 0, "");
      GLD4(w1r[1], vo, b1 + (size_t)(32 * (w + 8)) * 3456, 0, "");
      if (w < 4) GLD4(w1r[2], vo, b1 + (size_t)(32 * (w + 16)) * 3456, 0, "");
    }
    if (w < 5) {
      const char* b2 = w2 + (size_t)(16 * fb) * 1280 + w * 256;
#pragma unroll
      for (int i = 0; i < 4; ++i) GLD4(w2r[i], (unsigned)((4 * g4 + i) * 1280 + n16 * 16), b2, 0, "");
      VMWAIT(4);
    } else {
      VMWAIT(0);
    }
    TIE(w1r[0]); TIE(w1r[1]);
    if (w < 4) { TIE(w1r[2]); } else { w1r[2] = v4u{0u, 0u, 0u, 0u}; }
    {
      float ha[8];
#pragma unroll
      for (int k = 0; k < 8; ++k) ha[k] = 0.f;
#pragma unroll
      for (int i = 0; i < 3; ++i) {
        const int it = (i == 2 && w >= 4) ? 0 : w + 8 * i;
        const float xv = asf((unsigned)*(LAS unsigned short*)(size_t)(L_QB + (32 * it + (lane >> 1)) * 2) << 16);
        ha[0] += xv * asf(w1r[i].x << 16); ha[1] += xv * asf(w1r[i].x & 0xFFFF0000u);
        ha[2] += xv * asf(w1r[i].y << 16); ha[3] += xv * asf(w1r[i].y & 0xFFFF0000u);
        ha[4] += xv * asf(w1r[i].z << 16); ha[5] += xv * asf(w1r[i].z & 0xFFFF0000u);
        ha[6] += xv * asf(w1r[i].w << 16); ha[7] += xv * asf(w1r[i].w & 0xFFFF0000u);
      }
#pragma unroll
      for (int k = 0; k < 8; ++k) { float v = ha[k]; v += dpp<XOR2>(v); v += dpp<ROR4>(v); v += dpp<ROR8>(v); ha[k] = p32_sum(p16_sum(v)); }
      if (lane < 2) {
#pragma unroll
        for (int k = 0; k < 8; ++k) sts1(L_RED + (8 * lane + k) * 32 + w * 4, asu(ha[k]));
      }
    }
    BARRIER();
    if (w < 5) {
      VMWAIT(0);
#pragma unroll
      for (int i = 0; i < 4; ++i) TIE(w2r[i]);
      const int f = lane & 15;
      float hh = sum8(ldsf4(L_RED + f * 32), ldsf4(L_RED + f * 32 + 16));
      hh = hh * __builtin_amdgcn_rcpf(1.f + __expf(-hh));
      if (bi >= 108) hh = 0.f;
      unsigned hi = cvtpk(hh, hh);
      float rs = hh - asf(hi << 16);
      unsigned lo = cvtpk(rs, rs);
      if (lane < 32) sts1(L_TAB + w * 512 + lane * 4, lane < 16 ? hi : lo);
      LGWAIT();
      v4u t0 = lds4(L_TAB + w * 512 + ((n16 >> 3) * 16 + 4 * g4) * 4);
      unsigned dv[4] = {t0.x, t0.y, t0.z, t0.w};
      f4 acc = {0.f, 0.f, 0.f, 0.f};
#pragma unroll
      for (int i = 0; i < 4; ++i) { v4u a = {dv[i] & msk[0], dv[i] & msk[1], dv[i] & msk[2], dv[i] & msk[3]}; acc = mfma(a, w2r[i], acc); }
      float o0 = p32_sum(acc.x), o1 = p32_sum(acc.y), o2 = p32_sum(acc.z), o3 = p32_sum(acc.w);
      unsigned a0 = guard(cvtpk(o0, o1)), a1 = guard(cvtpk(o2, o3));
      auto s0 = __builtin_amdgcn_permlane16_swap(a0, a0, false, false); auto s1 = __builtin_amdgcn_permlane16_swap(a1, a1, false, false);
      unsigned b0 = s0[1], b1 = s1[1];
      v4u o = {a0, a1, b0, b1};
      if (lane < 16) GST4(o, (unsigned)(OFF_MP + (16 * w + lane) * 2048 + bi * 16), ws, "sc1");
    }
    STAMP(10);
    if (bi < 80 && w == 5) {
      FLAG2_CHECK(0);
      FLAG2_CHECK(1);
      unsigned gg[5];
#pragma unroll
      for (int i = 0; i < 5; ++i) GLD1(gg[i], (unsigned)(lane * 4), gw, i * 256, "");
      VMWAIT(0);
#pragma unroll
      for (int i = 0; i < 5; ++i) TIE(gg[i]);
      float gate;
      {
        float a = 0.f;
#pragma unroll
        for (int i = 0; i < 5; ++i) { const unsigned gx = *(LAS unsigned*)(size_t)(L_QB + i * 256 + lane * 4); a += asf(gx << 16) * asf(gg[i] << 16) + asf(gx & 0xFFFF0000u) * asf(gg[i] & 0xFFFF0000u); }
        a = p32_sum(p16_sum(row_sum(a)));
        gate = __builtin_amdgcn_rcpf(1.f + __expf(-a));
      }
      const unsigned one = 0x3F803F80u;
      v4u ones = {one & msk[0], one & msk[1], one & msk[2], one & msk[3]};
      v4u m0, m1;
      const unsigned mvo = (unsigned)(OFF_MP + bi * 2048 + lane * 16);
      for (unsigned spin = 0;; ++spin) {
        GLD4(m0, mvo, ws, 0, "sc1"); GLD4(m1, mvo, ws, 1024, "sc1");
        VMWAIT(0); TIE(m0); TIE(m1);
        if (allvalid(umax(mx4(m0), mx4(m1))) || spin > SPIN_CAP) break;
      }
      f4 bb = {0.f, 0.f, 0.f, 0.f};
      bb = mfma(ones, m0, bb); bb = mfma(ones, m1, bb);
      bb *= (1.f - gate);
      STAMP(11);
      const unsigned ovo = (unsigned)(OFF_OP + bi * 2048 + lane * 16);
      for (unsigned spin = 0;; ++spin) {
        GLD4(m0, ovo, ws, 0, "sc1"); GLD4(m1, ovo, ws, 1024, "sc1");
        VMWAIT(0); TIE(m0); TIE(m1);
        if (allvalid(umax(mx4(m0), mx4(m1))) || spin > SPIN_CAP) break;
      }
      STAMP(12);
      f4 aa = {0.f, 0.f, 0.f, 0.f};
      aa = mfma(ones, m0, aa); aa = mfma(ones, m1, aa);
      float r0 = row_sum(fmaf(gate, aa.x, bb.x)), r1 = row_sum(fmaf(gate, aa.y, bb.y));
      float r2 = row_sum(fmaf(gate, aa.z, bb.z)), r3 = row_sum(fmaf(gate, aa.w, bb.w));
      v2u o = {cvtpk(r0, r1), cvtpk(r2, r3)};
      if (n16 == 0 && g4 < 2) GST2(o, (unsigned)(bi * 16 + g4 * 8), out, "");
      if (bi == 0 && lane == 0) {
        v2u f = {(unsigned)epoch, (unsigned)(epoch >> 32)};
        GST2(f, (unsigned)OFF_E, ws, "sc1");
      }
      STAMP(13);
    }
  }
}

extern "C" void kernel_launch(void* const* d_in, const int* in_sizes, int n_in,
                              void* d_out, int out_size, void* d_ws, size_t ws_size,
                              hipStream_t stream) {
  gm_kernel<<<256, 512, 0, stream>>>((const char*)d_in[0], (const char*)d_in[1], (const char*)d_in[2], (const char*)d_in[3],
                                      (const char*)d_in[4], (const char*)d_in[5], (const char*)d_in[7], (const char*)d_in[8],
                                      (const char*)d_in[9], (char*)d_out, (char*)d_ws);
}
```

```cpp
#define STAMPS 1
#include <hip/hip_runtime.h>
#include <stdint.h>
#pragma clang diagnostic ignored "-Winline-asm"

typedef unsigned v4u __attribute__((ext_vector_type(4)));
typedef unsigned v2u __attribute__((ext_vector_type(2)));
typedef float f4 __attribute__((ext_vector_type(4)));
typedef float f2 __attribute__((ext_vector_type(2)));
typedef short s4 __attribute__((ext_vector_type(4)));
typedef short s8 __attribute__((ext_vector_type(8)));
typedef __bf16 bf2 __attribute__((ext_vector_type(2)));
typedef unsigned long long u64;
#define LAS __attribute__((address_space(3)))
#define DI __device__ __forceinline__

constexpr unsigned OFF_E = 0;
constexpr unsigned OFF_FLAG2 = 4096;
constexpr unsigned OFF_FLAG1 = 8192;
constexpr unsigned OFF_QP = OFF_FLAG1 + 32768;
constexpr unsigned OFF_QA = OFF_QP + 65536;
constexpr unsigned OFF_ST = OFF_QA + 32768;
constexpr unsigned OFF_OP = OFF_ST + 8192;
constexpr unsigned OFF_MP = OFF_OP + 163840;
constexpr unsigned OFF_PP = OFF_MP + 163840;
constexpr unsigned OFF_END = OFF_PP + 1048576;
constexpr unsigned OFF_STAMP = 8u << 20;
constexpr unsigned SENT = 0xFFFFFFFFu;
constexpr unsigned SPIN_CAP = 1u << 15;
constexpr float SC = 0.08838834764831845f * 1.4426950408889634f;

constexpr unsigned L_CKV = 0;
constexpr unsigned L_UK = 32768;
constexpr unsigned L_QAS = 131072;
constexpr unsigned L_QRED = L_QAS;
constexpr unsigned L_P = L_QAS + 8448;
constexpr unsigned L_MX = L_P + 2304;
constexpr unsigned L_LS = L_MX + 256;
constexpr unsigned L_RED = L_LS + 256;
constexpr unsigned L_TAB = L_RED + 1024;
constexpr unsigned L_QB = L_TAB + 4096;
constexpr unsigned L_EP = L_QB + 2304;
constexpr unsigned L_END = L_EP + 16;

DI f4 mfma(v4u a, v4u b, f4 c) { return __builtin_amdgcn_mfma_f32_16x16x32_bf16(__builtin_bit_cast(s8, a), __builtin_bit_cast(s8, b), c, 0, 0, 0); }
DI unsigned cvtpk(float lo, float hi) { f2 x = {lo, hi}; bf2 y = __builtin_convertvector(x, bf2); return __builtin_bit_cast(unsigned, y); }
DI float asf(unsigned u) { return __uint_as_float(u); }
DI unsigned asu(float f) { return __float_as_uint(f); }
DI unsigned guard(unsigned u) { return u < 0xFFFFFFFEu ? u : 0xFFFFFFFEu; }
DI float fmx(float a, float b) { return __builtin_elementwise_maximum(a, b); }
template <int CTRL> DI float dpp(float x) { return asf((unsigned)__builtin_amdgcn_mov_dpp((int)asu(x), CTRL, 0xf, 0xf, true)); }
constexpr int XOR1 = 0xB1, XOR2 = 0x4E, ROR4 = 0x124, ROR8 = 0x128, HMIR = 0x141;
DI float p16_max(float x) { auto t = __builtin_amdgcn_permlane16_swap(asu(x), asu(x), false, false); unsigned a = t[0], b = t[1]; return fmx(asf(a), asf(b)); }
DI float p32_max(float x) { auto t = __builtin_amdgcn_permlane32_swap(asu(x), asu(x), false, false); unsigned a = t[0], b = t[1]; return fmx(asf(a), asf(b)); }
DI float p16_sum(float x) { auto t = __builtin_amdgcn_permlane16_swap(asu(x), asu(x), false, false); unsigned a = t[0], b = t[1]; return asf(a) + asf(b); }
DI float p32_sum(float x) { auto t = __builtin_amdgcn_permlane32_swap(asu(x), asu(x), false, false); unsigned a = t[0], b = t[1]; return asf(a) + asf(b); }
DI float row_sum(float x) { x += dpp<XOR1>(x); x += dpp<XOR2>(x); x += dpp<ROR4>(x); x += dpp<ROR8>(x); return x; }
DI float row_max(float x) { x = fmx(x, dpp<XOR1>(x)); x = fmx(x, dpp<XOR2>(x)); x = fmx(x, dpp<ROR4>(x)); x = fmx(x, dpp<ROR8>(x)); return x; }
DI float max8(f4 u, f4 v) { return fmx(fmx(fmx(u.x, u.y), fmx(u.z, u.w)), fmx(fmx(v.x, v.y), fmx(v.z, v.w))); }
DI float sum8(f4 u, f4 v) { return ((u.x + u.y) + (u.z + u.w)) + ((v.x + v.y) + (v.z + v.w)); }
DI unsigned umax(unsigned a, unsigned b) { return a > b ? a : b; }
DI unsigned mx4(v4u v) { return umax(umax(v.x, v.y), umax(v.z, v.w)); }
DI bool allvalid(unsigned m) { return __builtin_amdgcn_ballot_w64(m == SENT) == 0ull; }
DI v4u lds4(unsigned a) { return *(LAS v4u*)(size_t)a; }
DI f4 ldsf4(unsigned a) { return *(LAS f4*)(size_t)a; }
DI void sts4(unsigned a, v4u v) { *(LAS v4u*)(size_t)a = v; }
DI void stsf4(unsigned a, f4 v) { *(LAS f4*)(size_t)a = v; }
DI void sts2(unsigned a, v2u v) { *(LAS v2u*)(size_t)a = v; }
DI void sts1(unsigned a, unsigned v) { *(LAS unsigned*)(size_t)a = v; }
DI v4u ldstr(unsigned a0, unsigned a1) {
  s4 lo = __builtin_amdgcn_ds_read_tr16_b64_v4i16((LAS s4*)(size_t)a0), hi = __builtin_amdgcn_ds_read_tr16_b64_v4i16((LAS s4*)(size_t)a1);
  s8 c = __builtin_shufflevector(lo, hi, 0, 1, 2, 3, 4, 5, 6, 7);
  return __builtin_bit_cast(v4u, c);
}

#define GLD4(dst, voff, sbase, IMM, MOD) asm volatile("global_load_dwordx4 %0, %1, %2 offset:%3 " MOD : "=v"(dst) : "v"(voff), "s"(sbase), "i"(IMM) : "memory")
#define GLD2(dst, voff, sbase, IMM, MOD) asm volatile("global_load_dwordx2 %0, %1, %2 offset:%3 " MOD : "=v"(dst) : "v"(voff), "s"(sbase), "i"(IMM) : "memory")
#define GLD1(dst, voff, sbase, IMM, MOD) asm volatile("global_load_dword %0, %1, %2 offset:%3 " MOD : "=v"(dst) : "v"(voff), "s"(sbase), "i"(IMM) : "memory")
#define GST4(src, voff, sbase, MOD) asm volatile("global_store_dwordx4 %0, %1, %2 " MOD "\n\ts_nop 1" :: "v"(voff), "v"(src), "s"(sbase) : "memory")
#define GST2(src, voff, sbase, MOD) asm volatile("global_store_dwordx2 %0, %1, %2 " MOD :: "v"(voff), "v"(src), "s"(sbase) : "memory")
#define SCHED() __builtin_amdgcn_sched_barrier(0)
#define VMWAIT(N) do { asm volatile("s_waitcnt vmcnt(%0)" :: "i"(N) : "memory"); SCHED(); } while (0)
#define LGWAIT() do { asm volatile("s_waitcnt lgkmcnt(0)" ::: "memory"); SCHED(); } while (0)
#define TIE(x) asm volatile("" : "+v"(x))
DI void dma16(unsigned voff, const void* sbase, unsigned ldsdst) {
  asm volatile("s_mov_b32 m0, %2\n\ts_nop 0\n\tglobal_load_lds_dwordx4 %0, %1" :: "v"(voff), "s"(sbase), "s"(ldsdst) : "memory", "m0");
}
#define BARRIER() do { asm volatile("s_waitcnt lgkmcnt(0)\n\ts_barrier" ::: "memory"); SCHED(); } while (0)
#define RAWBAR() do { asm volatile("s_barrier" ::: "memory"); SCHED(); } while (0)

#define STAMP(i) do { asm volatile("s_memrealtime %0\n\ts_waitcnt lgkmcnt(0)" : "=s"(ts[i]) :: "memory"); } while (0)
#define NPRE 4
#define NPRE2 8

__global__ void __launch_bounds__(512, 2)
gm_kernel(const char* __restrict__ x, const char* __restrict__ ckv, const char* __restrict__ wq, const char* __restrict__ wuk,
          const char* __restrict__ wuv, const char* __restrict__ wo, const char* __restrict__ w1, const char* __restrict__ w2,
          const char* __restrict__ gw, char* __restrict__ out, char* __restrict__ ws) {
  __shared__ __attribute__((aligned(16))) char smem[L_END];
  asm volatile("" :: "v"((LAS char*)smem));
  u64 ts[16];
#pragma unroll
  for (int i = 0; i < 16; ++i) ts[i] = 0;
  STAMP(0);
  const int tid = threadIdx.x, lane = tid & 63;
  const int w = __builtin_amdgcn_readfirstlane(tid >> 6);
  const int b = blockIdx.x, xcd = b & 7, r = b >> 3;
  const bool roleA = r < 16;
  const int g4 = lane >> 4, n16 = lane & 15;
  const int rr = r & 15;
  const int hq = xcd * 4 + (rr >> 2), j8 = (roleA ? 0 : 4) + (rr & 3);
  asm volatile("" :: "s"(x), "s"(ckv), "s"(wq), "s"(wuk), "s"(wuv), "s"(wo), "s"(w1), "s"(w2), "s"(gw), "s"(out), "s"(ws));
  STAMP(14);
  v4u wqr[4]; v2u xr, ew;
#define Q_ISSUE(ROLE_A) \
  if (w == 0) { \
    GLD2(ew, 0u, ws, OFF_E, "sc1");        \
    v4u s_ = {SENT, SENT, SENT, SENT}; \
    unsigned vo = (unsigned)(OFF_QP + (hq * 8 + j8) * 256 + n16 * 16); \
    if (ROLE_A) { \
      if (g4 == 1) vo = (unsigned)(OFF_QA + hq * 1024 + j8 * 256 + n16 * 16); \
      if (g4 == 2) vo = (unsigned)(OFF_OP + (64 + n16) * 2048 + (hq * 4 + j8) * 16); \
      if (lane < 48) GST4(s_, vo, ws, "sc1"); \
      GST4(s_, (unsigned)(OFF_OP + lane * 2048 + (hq * 4 + j8) * 16), ws, "sc1"); \
    } else { \
      if (lane < 16) GST4(s_, vo, ws, "sc1"); \
    } } \
  if (w < 5) { \
    const char* wb = wq + (size_t)(j8 * 80 + 16 * w) * 8192 + hq * 256; \
    const unsigned vo = (unsigned)(4 * g4 * 8192 + n16 * 16); \
    GLD2(xr, (unsigned)(8 * g4), x + (j8 * 80 + 16 * w) * 2, 0, ""); \
    GLD4(wqr[0], vo, wb, 0, ""); GLD4(wqr[1], vo, wb + 8192, 0, ""); GLD4(wqr[2], vo, wb + 16384, 0, ""); GLD4(wqr[3], vo, wb + 24576, 0, ""); } \
  RAWBAR();
#define Q_COMPUTE(NG2, ROLE_A) \
  if (w == 0) { \
    VMWAIT(5 + NG2); TIE(ew); \
    const u64 e1 = (((u64)ew.y << 32) | ew.x) + 1; \
    v2u f = {(unsigned)e1, (unsigned)(e1 >> 32)}; \
      \
    if (lane < (ROLE_A ? 2 : 1)) GST2(f, (unsigned)(lane == 0 ? OFF_FLAG1 + b * 128 : OFF_FLAG2 + b * 8), ws, "sc1"); \
    if (lane == 0) sts2(L_EP, f); } \
  if (w < 5) { \
    if (w == 0) VMWAIT(NG2 + 1 + 4); else VMWAIT(NG2 + 4); \
    TIE(xr); \
    const unsigned d0 = __builtin_amdgcn_perm(xr.x, xr.x, 0x01000100u), d1 = __builtin_amdgcn_perm(xr.x, xr.x, 0x03020302u); \
    const unsigned d2 = __builtin_amdgcn_perm(xr.y, xr.y, 0x01000100u), d3 = __builtin_amdgcn_perm(xr.y, xr.y, 0x03020302u); \
    v4u a0 = {d0 & msk[0], d0 & msk[1], d0 & msk[2], d0 & msk[3]}, a1 = {d1 & msk[0], d1 & msk[1], d1 & msk[2], d1 & msk[3]}; \
    v4u a2 = {d2 & msk[0], d2 & msk[1], d2 & msk[2], d2 & msk[3]}, a3 = {d3 & msk[0], d3 & msk[1], d3 & msk[2], d3 & msk[3]}; \
    unsigned qr = L_QRED + w * 512 + (8 * n16 + 4 * g4) * 4; \
    TIE(a0); TIE(a1); TIE(a2); TIE(a3); TIE(qr); \
    if (w == 0) VMWAIT(NG2 + 1); else VMWAIT(NG2); \
    TIE(wqr[0]); TIE(wqr[1]); TIE(wqr[2]); TIE(wqr[3]); \
    STAMP(2); \
    f4 acc = {0.f, 0.f, 0.f, 0.f}; \
    acc = mfma(a0, wqr[0], acc); acc = mfma(a1, wqr[1], acc); acc = mfma(a2, wqr[2], acc); acc = mfma(a3, wqr[3], acc); \
    if (lane < 32) stsf4(qr, acc); } \
  BARRIER(); \
  if (w == 0) { \
    if (lane < 32) { \
      const unsigned ra = L_QRED + lane * 16; \
      f4 s0 = ldsf4(ra), s1 = ldsf4(ra + 512), s2 = ldsf4(ra + 1024), s3 = ldsf4(ra + 1536), s4_ = ldsf4(ra + 2048); \
      f4 sm = (s0 + s1) + (s2 + s3) + s4_; \
      v2u o = {guard(cvtpk(sm.x, sm.y)), guard(cvtpk(sm.z, sm.w))}; \
      GST2(o, (unsigned)(OFF_QP + (hq * 8 + j8) * 256 + lane * 8), ws, "sc1"); } \
    STAMP(3); }
#define EPOCH() ({ v2u e_ = *(LAS v2u*)(size_t)L_EP; (((u64)(unsigned)__builtin_amdgcn_readfirstlane(e_.y)) << 32) | (unsigned)__builtin_amdgcn_readfirstlane(e_.x); })
#define FLAG1_CHECK(BLK) \
  for (unsigned spin = 0;; ++spin) { \
    v2u f0; \
    GLD2(f0, (unsigned)(OFF_FLAG1 + (BLK) * 128), ws, 0, "sc1"); \
    VMWAIT(0); TIE(f0); \
    const unsigned df = (f0.x ^ (unsigned)epoch) | (f0.y ^ (unsigned)(epoch >> 32)); \
    if (__builtin_amdgcn_ballot_w64(df != 0u) == 0ull || spin > SPIN_CAP) break; \
  }
#define FLAG2_CHECK(HALF) \
  for (unsigned spin = 0;; ++spin) { \
    v4u f0; \
    GLD4(f0, (unsigned)(OFF_FLAG2 + (HALF) * 1024 + lane * 16), ws, 0, "sc1"); \
    VMWAIT(0); TIE(f0); \
    const unsigned lo = (unsigned)epoch, hi = (unsigned)(epoch >> 32); \
    const unsigned df = ((f0.x ^ lo) | (f0.y ^ hi)) | ((f0.z ^ lo) | (f0.w ^ hi)); \
    if (__builtin_amdgcn_ballot_w64(df != 0u) == 0ull || spin > SPIN_CAP) break; \
  }
  unsigned msk[4];
#pragma unroll
  for (int t = 0; t < 4; ++t) msk[t] = (((n16 & 7) >> 1) == t) ? ((n16 & 1) ? 0xFFFF0000u : 0x0000FFFFu) : 0u;

  if (roleA) {
    const int h = hq, j = j8;
    Q_ISSUE(true);
    STAMP(1);
    {
      const char* ub = wuk + (size_t)(128 * j + 16 * w) * 8192 + h * 256;
#pragma unroll
      for (int i = 0; i < 4; ++i)
        dma16((unsigned)((4 * i + g4) * 8192 + ((n16 ^ (4 * i + g4)) * 16)), ub, (unsigned)(L_UK + (16 * w + 4 * i) * 256));
    }
    Q_COMPUTE(4, true);
    const u64 epoch = EPOCH();
    if (w == 7) { const int j8l = lane & 7; const int blk = (h >> 2) + 8 * ((j8l >> 2) * 16 + (h & 3) * 4 + (j8l & 3)); FLAG1_CHECK(blk); STAMP(3); }
    else VMWAIT(0);
    v4u uk[4];
#pragma unroll
    for (int ks = 0; ks < 4; ++ks) uk[ks] = lds4(L_UK + (16 * w + n16) * 256 + (((4 * ks + g4) ^ n16) * 16));
    unsigned qb_a = L_QB + (n16 & 7) * 288 + g4 * 16;
    unsigned qa_o = (unsigned)(OFF_QA + h * 1024 + (128 * j + 16 * w + 4 * g4) * 2);
    TIE(qb_a); TIE(qa_o);
    if (w == 7) {
      v4u p0, p1;
      const unsigned vo = (unsigned)(OFF_QP + h * 2048 + lane * 16);
      unsigned wa = L_QB + g4 * 288 + n16 * 16; TIE(wa);
      for (unsigned spin = 0;; ++spin) {
        GLD4(p0, vo, ws, 0, "sc1"); GLD4(p1, vo, ws, 1024, "sc1");
        VMWAIT(0); TIE(p0); TIE(p1);
        if (allvalid(umax(mx4(p0), mx4(p1))) || spin > SPIN_CAP) break;
      }
      STAMP(4);
      sts4(wa, p0); sts4(wa + 4 * 288, p1);
    }
    BARRIER();
    {
      v4u bq[4];
#pragma unroll
      for (int ks = 0; ks < 4; ++ks) bq[ks] = lds4(qb_a + ks * 64);
      SCHED();
      f4 acc = {0.f, 0.f, 0.f, 0.f};
#pragma unroll
      for (int ks = 0; ks < 4; ++ks) acc = mfma(uk[ks], bq[ks], acc);
      float v0 = acc.x, v1 = acc.y, v2 = acc.z, v3 = acc.w;
      v0 += dpp<XOR1>(v0); v1 += dpp<XOR1>(v1); v2 += dpp<XOR1>(v2); v3 += dpp<XOR1>(v3);
      v0 += dpp<XOR2>(v0); v1 += dpp<XOR2>(v1); v2 += dpp<XOR2>(v2); v3 += dpp<XOR2>(v3);
      v0 += dpp<HMIR>(v0); v1 += dpp<HMIR>(v1); v2 += dpp<HMIR>(v2); v3 += dpp<HMIR>(v3);
      v2u o = {guard(cvtpk(v0, v1)), guard(cvtpk(v2, v3))};
      if (n16 == 0) GST2(o, qa_o, ws, "sc1");
    }
    STAMP(5);
    RAWBAR();
    {
      const char* vb = wuv + (size_t)(64 * w) * 8192 + h * 256 + j * 64;
#pragma unroll
      for (int i = 0; i < 4; ++i)
        dma16((unsigned)((16 * i + (lane >> 2)) * 8192 + (lane & 3) * 16), vb, (unsigned)(L_CKV + (64 * w + 16 * i) * 64));
    }
    v4u wor[8];
    if (w < 5) {
      const char* ob = wo + (size_t)(h * 128 + 32 * j) * 1280 + w * 256;
#pragma unroll
      for (int s = 0; s < 8; ++s) GLD4(wor[s], (unsigned)((8 * g4 + s) * 1280 + n16 * 16), ob, 0, "");
      VMWAIT(8);
    } else {
      VMWAIT(0);
    }
    v4u bv[2][2];
    {
      const int q = (lane & 15) >> 2, p = lane & 3;
      const unsigned a = L_CKV + (64 * w + 8 * g4 + q) * 64 + 8 * p;
#pragma unroll
      for (int ks = 0; ks < 2; ++ks)
#pragma unroll
        for (int dt = 0; dt < 2; ++dt) bv[ks][dt] = ldstr(a + ks * 2048 + dt * 32, a + ks * 2048 + 256 + dt * 32);
    }
    if (w == 7) { FLAG2_CHECK(1); }
    unsigned pvo = (unsigned)(OFF_PP + (h * 32 + n16) * 1024 + 128 * w + 16 * g4), pvo2 = pvo + 16384u;
    unsigned sto = (unsigned)(OFF_ST + h * 256 + (lane & 31) * 8);
    unsigned tabw = L_TAB + w * 512 + lane * 4, tabr = L_TAB + w * 512 + g4 * 16;
    unsigned crw = L_RED + lane * 32 + w * 4, crr = L_RED + (lane & 31) * 32;
    unsigned tabr2 = L_TAB + w * 512 + ((n16 >> 3) * 32 + 8 * g4) * 4;
    unsigned opo = (unsigned)(OFF_OP + (16 * w + lane) * 2048 + (h * 4 + j) * 16);
    TIE(pvo); TIE(pvo2); TIE(sto); TIE(tabw); TIE(tabr); TIE(crw); TIE(crr); TIE(tabr2); TIE(opo);
    VMWAIT(0);
    if (w < 5) {
#pragma unroll
      for (int s = 0; s < 8; ++s) TIE(wor[s]);
    }
    STAMP(6);
    BARRIER();
    v2u st;
    for (unsigned spin = 0;; ++spin) {
      GLD2(st, sto, ws, 0, "sc1");
      VMWAIT(0); TIE(st);
      if (allvalid(umax(st.x, st.y)) || spin > SPIN_CAP) break;
    }
    v4u pa[2][2];
    GLD4(pa[0][0], pvo, ws, 0, "sc1"); GLD4(pa[0][1], pvo, ws, 64, "sc1");
    GLD4(pa[1][0], pvo2, ws, 0, "sc1"); GLD4(pa[1][1], pvo2, ws, 64, "sc1");
    STAMP(7);
    f4 aw[2];
    {
      float m = asf(st.x), l = asf(st.y);
      float M = p16_max(row_max(m));
      float a = __builtin_amdgcn_exp2f((m - M) * SC);
      float Ls = p16_sum(row_sum(a * l));
      a = a * __builtin_amdgcn_rcpf(Ls);
      if (lane < 32) sts1(tabw, asu(a));
      LGWAIT();
      aw[0] = ldsf4(tabr); aw[1] = ldsf4(tabr + 64);
    }
    for (unsigned spin = 0;; ++spin) {
      VMWAIT(0); TIE(pa[0][0]); TIE(pa[0][1]); TIE(pa[1][0]); TIE(pa[1][1]);
      if (allvalid(umax(umax(mx4(pa[0][0]), mx4(pa[0][1])), umax(mx4(pa[1][0]), mx4(pa[1][1])))) || spin > SPIN_CAP) break;
      GLD4(pa[0][0], pvo, ws, 0, "sc1"); GLD4(pa[0][1], pvo, ws, 64, "sc1");
      GLD4(pa[1][0], pvo2, ws, 0, "sc1"); GLD4(pa[1][1], pvo2, ws, 64, "sc1");
    }
    STAMP(8);
    {
      f4 z = {0.f, 0.f, 0.f, 0.f};
      f4 t00 = mfma(pa[0][0], bv[0][0], z), t10 = mfma(pa[1][0], bv[0][0], z), t01 = mfma(pa[0][0], bv[0][1], z), t11 = mfma(pa[1][0], bv[0][1], z);
      t00 = mfma(pa[0][1], bv[1][0], t00); t10 = mfma(pa[1][1], bv[1][0], t10); t01 = mfma(pa[0][1], bv[1][1], t01); t11 = mfma(pa[1][1], bv[1][1], t11);
      f4 c0 = aw[0] * t00 + aw[1] * t10, c1 = aw[0] * t01 + aw[1] * t11;
      float u0 = (c0.x + c0.y) + (c0.z + c0.w), u1 = (c1.x + c1.y) + (c1.z + c1.w);
      auto t = __builtin_amdgcn_permlane16_swap(asu(u0), asu(u1), false, false); unsigned ta = t[0], tb = t[1];
      float u = p32_sum(asf(ta) + asf(tb));
      if (lane < 32) sts1(crw, asu(u));
    }
    BARRIER();
    STAMP(9);
    if (w < 5) {
      float c = sum8(ldsf4(crr), ldsf4(crr + 16));
      unsigned hi = cvtpk(c, c);
      float rs = c - asf(hi << 16);
      unsigned lo = cvtpk(rs, rs);
      sts1(tabw, lane < 32 ? hi : lo);
      LGWAIT();
      v4u t0 = lds4(tabr2), t1 = lds4(tabr2 + 16);
      unsigned dv[8] = {t0.x, t0.y, t0.z, t0.w, t1.x, t1.y, t1.z, t1.w};
      f4 acc = {0.f, 0.f, 0.f, 0.f};
#pragma unroll
      for (int s = 0; s < 8; ++s) { v4u a = {dv[s] & msk[0], dv[s] & msk[1], dv[s] & msk[2], dv[s] & msk[3]}; acc = mfma(a, wor[s], acc); }
      float o0 = p32_sum(acc.x), o1 = p32_sum(acc.y), o2 = p32_sum(acc.z), o3 = p32_sum(acc.w);
      unsigned a0 = guard(cvtpk(o0, o1)), a1 = guard(cvtpk(o2, o3));
      auto s0 = __builtin_amdgcn_permlane16_swap(a0, a0, false, false); auto s1 = __builtin_amdgcn_permlane16_swap(a1, a1, false, false);
      unsigned b0 = s0[1], b1 = s1[1];
      v4u o = {a0, a1, b0, b1};
      if (lane < 16) GST4(o, opo, ws, "sc1");
    }
    STAMP(10);
  } else {
    const int g = rr & 3, s = xcd * 4 + (rr >> 2), bi = xcd * 16 + rr;
    Q_ISSUE(false);
    STAMP(1);
    const char* kb = ckv + (size_t)(128 * s + 16 * w) * 1024;
    const unsigned kl = L_CKV + (16 * w) * 1024;
    if (w < 5) {
#pragma unroll
      for (int i = 0; i < NPRE; ++i) dma16((unsigned)((lane ^ i) * 16), kb + i * 1024, kl + i * 1024);
      Q_COMPUTE(NPRE, false);
#pragma unroll
      for (int i = NPRE; i < 16; ++i) dma16((unsigned)((lane ^ i) * 16), kb + i * 1024, kl + i * 1024);
    } else {
#pragma unroll
      for (int i = 0; i < NPRE2; ++i) dma16((unsigned)((lane ^ i) * 16), kb + i * 1024, kl + i * 1024);
      BARRIER();
#pragma unroll
      for (int i = NPRE2; i < 16; ++i) dma16((unsigned)((lane ^ i) * 16), kb + i * 1024, kl + i * 1024);
      v4u s_ = {SENT, SENT, SENT, SENT};
#pragma unroll
      for (int i = 0; i < 3; ++i) { const int hh = 3 * (w - 5) + i; if (hh < 8) GST4(s_, (unsigned)(OFF_PP + ((8 * g + hh) * 32 + s) * 1024 + lane * 16), ws, "sc1"); }
      if (w == 7) {
        v2u s2 = {SENT, SENT};
        if (lane < 8) GST2(s2, (unsigned)(OFF_ST + ((8 * g + lane) * 32 + s) * 8), ws, "sc1");
        GST4(s_, (unsigned)(OFF_MP + lane * 2048 + bi * 16), ws, "sc1");
        if (lane < 16) GST4(s_, (unsigned)(OFF_MP + (64 + lane) * 2048 + bi * 16), ws, "sc1");
      }
      if (w == 5) { dma16((unsigned)(lane * 16), x, L_QB); if (lane < 16) dma16((unsigned)(lane * 16), x + 1024, L_QB + 1024); }
    }
    const u64 epoch = EPOCH();
    if (w == 7) { const int hd = 8 * g + ((lane & 31) >> 2); const int blk = (hd >> 2) + 8 * ((hd & 3) * 4 + (lane & 3)); FLAG1_CHECK(blk); }
    else VMWAIT(0);
    STAMP(4);
    v4u kA[16];
    {
      const unsigned ka = L_CKV + (16 * w + n16) * 1024;
#pragma unroll
      for (int ks = 0; ks < 16; ++ks) kA[ks] = lds4(ka + (((4 * ks + g4) ^ n16) * 16));
    }
    unsigned qvo = (unsigned)(OFF_QA + (8 * g + w) * 1024 + lane * 16);
    unsigned qsw = L_QAS + w * 1056 + lane * 16, qsr = L_QAS + (n16 & 7) * 1056 + g4 * 16;
    unsigned mxw = L_MX + lane * 32 + w * 4, mxr = L_MX + (n16 & 7) * 32;
    unsigned pw = L_P + n16 * 288 + (16 * w + 4 * g4) * 2, pr = L_P + (n16 & 7) * 288 + g4 * 16;
    unsigned lsw = L_LS + lane * 32 + w * 4;
    unsigned ppo = (unsigned)(OFF_PP + ((8 * g + (n16 & 7)) * 32 + s) * 1024 + (64 * w + 32 * (g4 >> 1) + 8 * (2 * (n16 >> 3) + (g4 & 1))) * 2);
    TIE(qvo); TIE(qsw); TIE(qsr); TIE(mxw); TIE(mxr); TIE(pw); TIE(pr); TIE(lsw); TIE(ppo);
    BARRIER();
    if (w == 5 && lane == 0) { v2u f = {(unsigned)epoch, (unsigned)(epoch >> 32)}; GST2(f, (unsigned)(OFF_FLAG2 + b * 8), ws, "sc1"); }
    STAMP(5);
    v4u qa;
    GLD4(qa, qvo, ws, 0, "sc1");
    v4u vA[4][4];
    {
      const int q = (lane & 15) >> 2, p = lane & 3;
#pragma unroll
      for (int t = 0; t < 4; ++t) {
        const int ch = 8 * w + 4 * (p >> 1) + t;
        const int r0 = 8 * g4 + q, r1 = r0 + 4;
        const unsigned a0 = L_CKV + r0 * 1024 + ((ch ^ (r0 & 15)) * 16) + 8 * (p & 1), a1 = L_CKV + r1 * 1024 + ((ch ^ (r1 & 15)) * 16) + 8 * (p & 1);
        vA[0][t] = ldstr(a0, a1); vA[1][t] = ldstr(a0 + 32768, a1 + 32768);
        vA[2][t] = ldstr(a0 + 65536, a1 + 65536); vA[3][t] = ldstr(a0 + 98304, a1 + 98304);
      }
    }
    SCHED();
    for (unsigned spin = 0;; ++spin) {
      VMWAIT(0); TIE(qa);
      if (allvalid(mx4(qa)) || spin > SPIN_CAP) break;
      GLD4(qa, qvo, ws, 0, "sc1");
    }
    STAMP(6);
    sts4(qsw, qa);
    BARRIER();
    f4 sc = {0.f, 0.f, 0.f, 0.f};
    {
      v4u bq[16];
#pragma unroll
      for (int ks = 0; ks < 16; ++ks) bq[ks] = lds4(qsr + ks * 64);
      SCHED();
#pragma unroll
      for (int ks = 0; ks < 16; ++ks) sc = mfma(kA[ks], bq[ks], sc);
      float mw = p32_max(p16_max(fmx(fmx(sc.x, sc.y), fmx(sc.z, sc.w))));
      if (lane < 8) sts1(mxw, asu(mw));
    }
    BARRIER();
    STAMP(7);
    {
      const float Ms = -max8(ldsf4(mxr), ldsf4(mxr + 16)) * SC;
      float p0 = __builtin_amdgcn_exp2f(fmaf(sc.x, SC, Ms)), p1 = __builtin_amdgcn_exp2f(fmaf(sc.y, SC, Ms)), p2 = __builtin_amdgcn_exp2f(fmaf(sc.z, SC, Ms)), p3 = __builtin_amdgcn_exp2f(fmaf(sc.w, SC, Ms));
      v2u pk = {cvtpk(p0, p1), cvtpk(p2, p3)};
      if (n16 < 8) sts2(pw, pk);
      float ls = p32_sum(p16_sum((p0 + p1) + (p2 + p3)));
      if (lane < 8) sts1(lsw, asu(ls));
    }
    BARRIER();
    STAMP(8);
    {
      v4u pb[4];
#pragma unroll
      for (int ks = 0; ks < 4; ++ks) pb[ks] = lds4(pr + ks * 64);
      f4 lu = ldsf4(L_LS + w * 32), lv = ldsf4(L_LS + w * 32 + 16), mu = ldsf4(L_MX + w * 32), mv = ldsf4(L_MX + w * 32 + 16);
      SCHED();
      f4 ac[4];
#pragma unroll
      for (int t = 0; t < 4; ++t) ac[t] = mfma(vA[0][t], pb[0], f4{0.f, 0.f, 0.f, 0.f});
      {
        v2u o = {guard(asu(max8(mu, mv))), guard(asu(sum8(lu, lv)))};
        if (lane == 0) GST2(o, (unsigned)(OFF_ST + ((8 * g + w) * 32 + s) * 8), ws, "sc1");
      }
#pragma unroll
      for (int ks = 1; ks < 4; ++ks)
#pragma unroll
        for (int t = 0; t < 4; ++t) ac[t] = mfma(vA[ks][t], pb[ks], ac[t]);
      const bool up = n16 >= 8;
      f4 xa = up ? ac[2] : ac[0], ya = up ? ac[3] : ac[1];
      unsigned x0 = cvtpk(xa.x, xa.y), x1 = cvtpk(xa.z, xa.w), y0 = cvtpk(ya.x, ya.y), y1 = cvtpk(ya.z, ya.w);
      auto s0 = __builtin_amdgcn_permlane16_swap(x0, y0, false, false); auto s1 = __builtin_amdgcn_permlane16_swap(x1, y1, false, false);
      unsigned e0 = s0[0], e1 = s1[0], f0 = s0[1], f1 = s1[1];
      v4u o = {guard(e0), guard(e1), guard(f0), guard(f1)};
      GST4(o, ppo, ws, "sc1");
    }
    STAMP(9);
    RAWBAR();
    const int fb = bi < 108 ? bi : 107;
    v4u w1r[3], w2r[4];
    {
      const char* b1 = w1 + fb * 32;
      const unsigned vo = (unsigned)((lane >> 1) * 3456 + (lane & 1) * 16);
      GLD4(w1r[0], vo, b1 + (size_t)(32 * w) * 3456, 0, "");
      GLD4(w1r[1], vo, b1 + (size_t)(32 * (w + 8)) * 3456, 0, "");
      if (w < 4) GLD4(w1r[2], vo, b1 + (size_t)(32 * (w + 16)) * 3456, 0, "");
    }
    if (w < 5) {
      const char* b2 = w2 + (size_t)(16 * fb) * 1280 + w * 256;
#pragma unroll
      for (int i = 0; i < 4; ++i) GLD4(w2r[i], (unsigned)((4 * g4 + i) * 1280 + n16 * 16), b2, 0, "");
      VMWAIT(4);
    } else {
      VMWAIT(0);
    }
    TIE(w1r[0]); TIE(w1r[1]);
    if (w < 4) { TIE(w1r[2]); } else { w1r[2] = v4u{0u, 0u, 0u, 0u}; }
    {
      float ha[8];
#pragma unroll
      for (int k = 0; k < 8; ++k) ha[k] = 0.f;
#pragma unroll
      for (int i = 0; i < 3; ++i) {
        const int it = (i == 2 && w >= 4) ? 0 : w + 8 * i;
        const float xv = asf((unsigned)*(LAS unsigned short*)(size_t)(L_QB + (32 * it + (lane >> 1)) * 2) << 16);
        ha[0] += xv * asf(w1r[i].x << 16); ha[1] += xv * asf(w1r[i].x & 0xFFFF0000u);
        ha[2] += xv * asf(w1r[i].y << 16); ha[3] += xv * asf(w1r[i].y & 0xFFFF0000u);
        ha[4] += xv * asf(w1r[i].z << 16); ha[5] += xv * asf(w1r[i].z & 0xFFFF0000u);
        ha[6] += xv * asf(w1r[i].w << 16); ha[7] += xv * asf(w1r[i].w & 0xFFFF0000u);
      }
#pragma unroll
      for (int k = 0; k < 8; ++k) { float v = ha[k]; v += dpp<XOR2>(v); v += dpp<ROR4>(v); v += dpp<ROR8>(v); ha[k] = p32_sum(p16_sum(v)); }
      if (lane < 2) {
#pragma unroll
        for (int k = 0; k < 8; ++k) sts1(L_RED + (8 * lane + k) * 32 + w * 4, asu(ha[k]));
      }
    }
    BARRIER();
    if (w < 5) {
      VMWAIT(0);
#pragma unroll
      for (int i = 0; i < 4; ++i) TIE(w2r[i]);
      const int f = lane & 15;
      float hh = sum8(ldsf4(L_RED + f * 32), ldsf4(L_RED + f * 32 + 16));
      hh = hh * __builtin_amdgcn_rcpf(1.f + __expf(-hh));
      if (bi >= 108) hh = 0.f;
      unsigned hi = cvtpk(hh, hh);
      float rs = hh - asf(hi << 16);
      unsigned lo = cvtpk(rs, rs);
      if (lane < 32) sts1(L_TAB + w * 512 + lane * 4, lane < 16 ? hi : lo);
      LGWAIT();
      v4u t0 = lds4(L_TAB + w * 512 + ((n16 >> 3) * 16 + 4 * g4) * 4);
      unsigned dv[4] = {t0.x, t0.y, t0.z, t0.w};
      f4 acc = {0.f, 0.f, 0.f, 0.f};
#pragma unroll
      for (int i = 0; i < 4; ++i) { v4u a = {dv[i] & msk[0], dv[i] & msk[1], dv[i] & msk[2], dv[i] & msk[3]}; acc = mfma(a, w2r[i], acc); }
      float o0 = p32_sum(acc.x), o1 = p32_sum(acc.y), o2 = p32_sum(acc.z), o3 = p32_sum(acc.w);
      unsigned a0 = guard(cvtpk(o0, o1)), a1 = guard(cvtpk(o2, o3));
      auto s0 = __builtin_amdgcn_permlane16_swap(a0, a0, false, false); auto s1 = __builtin_amdgcn_permlane16_swap(a1, a1, false, false);
      unsigned b0 = s0[1], b1 = s1[1];
      v4u o = {a0, a1, b0, b1};
      if (lane < 16) GST4(o, (unsigned)(OFF_MP + (16 * w + lane) * 2048 + bi * 16), ws, "sc1");
    }
    STAMP(10);
    if (bi < 80 && w == 5) {
      FLAG2_CHECK(0);
      FLAG2_CHECK(1);
      unsigned gg[5];
#pragma unroll
      for (int i = 0; i < 5; ++i) GLD1(gg[i], (unsigned)(lane * 4), gw, i * 256, "");
      VMWAIT(0);
#pragma unroll
      for (int i = 0; i < 5; ++i) TIE(gg[i]);
      float gate;
      {
        float a = 0.f;
#pragma unroll
        for (int i = 0; i < 5; ++i) { const unsigned gx = *(LAS unsigned*)(size_t)(L_QB + i * 256 + lane * 4); a += asf(gx << 16) * asf(gg[i] << 16) + asf(gx & 0xFFFF0000u) * asf(gg[i] & 0xFFFF0000u); }
        a = p32_sum(p16_sum(row_sum(a)));
        gate = __builtin_amdgcn_rcpf(1.f + __expf(-a));
      }
      const unsigned one = 0x3F803F80u;
      v4u ones = {one & msk[0], one & msk[1], one & msk[2], one & msk[3]};
      v4u m0, m1;
      const unsigned mvo = (unsigned)(OFF_MP + bi * 2048 + lane * 16);
      for (unsigned spin = 0;; ++spin) {
        GLD4(m0, mvo, ws, 0, "sc1"); GLD4(m1, mvo, ws, 1024, "sc1");
        VMWAIT(0); TIE(m0); TIE(m1);
        if (allvalid(umax(mx4(m0), mx4(m1))) || spin > SPIN_CAP) break;
      }
      f4 bb = {0.f, 0.f, 0.f, 0.f};
      bb = mfma(ones, m0, bb); bb = mfma(ones, m1, bb);
      bb *= (1.f - gate);
      STAMP(11);
      const unsigned ovo = (unsigned)(OFF_OP + bi * 2048 + lane * 16);
      for (unsigned spin = 0;; ++spin) {
        GLD4(m0, ovo, ws, 0, "sc1"); GLD4(m1, ovo, ws, 1024, "sc1");
        VMWAIT(0); TIE(m0); TIE(m1);
        if (allvalid(umax(mx4(m0), mx4(m1))) || spin > SPIN_CAP) break;
      }
      STAMP(12);
      f4 aa = {0.f, 0.f, 0.f, 0.f};
      aa = mfma(ones, m0, aa); aa = mfma(ones, m1, aa);
      float r0 = row_sum(fmaf(gate, aa.x, bb.x)), r1 = row_sum(fmaf(gate, aa.y, bb.y));
      float r2 = row_sum(fmaf(gate, aa.z, bb.z)), r3 = row_sum(fmaf(gate, aa.w, bb.w));
      v2u o = {cvtpk(r0, r1), cvtpk(r2, r3)};
      if (n16 == 0 && g4 < 2) GST2(o, (unsigned)(bi * 16 + g4 * 8), out, "");
      if (bi == 0 && lane == 0) {
        v2u f = {(unsigned)epoch, (unsigned)(epoch >> 32)};
        GST2(f, (unsigned)OFF_E, ws, "sc1");
      }
      STAMP(13);
    }
  }
  if (lane == 0) {
    u64* sp = (u64*)(ws + OFF_STAMP) + ((size_t)b * 8 + w) * 16;
#pragma unroll
    for (int i = 0; i < 16; ++i) sp[i] = ts[i];
  }
}

__global__ void k_pre(u64* st) { if (threadIdx.x == 0) st[256 * 8 * 16] = __builtin_amdgcn_s_memrealtime(); }
__global__ void k_amp(const u64* st, int role, int idx, int stat, int amp) {
  const u64 tstart = __builtin_amdgcn_s_memrealtime();
  __shared__ u64 s0[256], s1[256];
  const int t = threadIdx.x;
  u64 mn = ~0ull, v = stat == 2 ? ~0ull : 0ull; bool any = false;
  for (int w = 0; w < 8; ++w) { const u64* q = st + ((size_t)t * 8 + w) * 16; mn = q[0] < mn ? q[0] : mn;
    const bool ok = role == 2 || ((t >> 3) >= 16) == (role == 1); const u64 x = q[idx];
    if (ok && x) { any = true; if (stat == 2) v = x < v ? x : v; else v = x > v ? x : v; } }
  s0[t] = mn; s1[t] = any ? v : 0ull;
  __syncthreads();
  if (t == 0) {
    mn = ~0ull; for (int i = 0; i < 256; ++i) mn = s0[i] < mn ? s0[i] : mn;
    u64 r = 0; int n = 0;
    if (stat == 0) { for (int i = 0; i < 256; ++i) r = s1[i] > r ? s1[i] : r; }
    else if (stat == 2) { r = ~0ull; for (int i = 0; i < 256; ++i) if (s1[i]) r = s1[i] < r ? s1[i] : r; }
    else if (stat == 1) {
      for (int i = 0; i < 256; ++i) if (s1[i]) n++;
      for (int i = 0; i < 256; ++i) if (s1[i]) { int c = 0; for (int k = 0; k < 256; ++k) if (s1[k] && (s1[k] < s1[i] || (s1[k] == s1[i] && k < i))) c++; if (c == n / 2) r = s1[i]; } }
    u64 q = r - mn;
    if (stat == 3) q = mn - st[256 * 8 * 16];
    if (stat == 4) { u64 mx = 0; for (int i = 0; i < 256; ++i) mx = s1[i] > mx ? s1[i] : mx; q = tstart - mx; }
    if (q > 5000) q = 5000;
    const u64 until = __builtin_amdgcn_s_memrealtime() + q * amp;
    while (__builtin_amdgcn_s_memrealtime() < until) __builtin_amdgcn_s_sleep(4);
  }
}

extern "C" void kernel_launch(void* const* d_in, const int* in_sizes, int n_in,
                              void* d_out, int out_size, void* d_ws, size_t ws_size,
                              hipStream_t stream) {
  gm_kernel<<<256, 512, 0, stream>>>((const char*)d_in[0], (const char*)d_in[1], (const char*)d_in[2], (const char*)d_in[3],
                                      (const char*)d_in[4], (const char*)d_in[5], (const char*)d_in[7], (const char*)d_in[8],
                                      (const char*)d_in[9], (char*)d_out, (char*)d_ws);
  k_amp<<<1, 256, 0, stream>>>((const u64*)((char*)d_ws + OFF_STAMP), 2, 14, 2, 20);
}
```

```cpp
#include <hip/hip_runtime.h>
#include <hip/hip_bf16.h>
#include <stdint.h>

typedef unsigned short u16;
static __device__ __forceinline__ float bf2f(u16 b) { return __uint_as_float(((unsigned)b) << 16); }
static __device__ __forceinline__ u16 f2bf(float f) { unsigned u = __float_as_uint(f); u += 0x7FFFu + ((u >> 16) & 1u); return (u16)(u >> 16); }

constexpr int H = 32, D = 128, SK = 4096, M = 640, DC = 512, F = 1728;

__global__ void k_q(const u16* x, const u16* wq, float* q) {
  int h = blockIdx.x, d = threadIdx.x;
  float acc = 0.f;
  for (int m = 0; m < M; ++m) acc += bf2f(x[m]) * bf2f(wq[(size_t)m * H * D + h * D + d]);
  q[h * D + d] = acc;
}
__global__ void k_qabs(const float* q, const u16* wuk, float* qa) {
  int h = blockIdx.x, c = threadIdx.x;
  float acc = 0.f;
  for (int d = 0; d < D; ++d) acc += q[h * D + d] * bf2f(wuk[(size_t)c * H * D + h * D + d]);
  qa[h * DC + c] = acc * 0.08838834764831845f;
}
__global__ void k_scores(const float* qa, const u16* ckv, float* s) {
  int h = blockIdx.x, k = blockIdx.y * 256 + threadIdx.x;
  float acc = 0.f;
  for (int c = 0; c < DC; ++c) acc += qa[h * DC + c] * bf2f(ckv[(size_t)k * DC + c]);
  s[h * SK + k] = acc;
}
__global__ void k_softmax(float* s) {
  __shared__ float red[256];
  int h = blockIdx.x, t = threadIdx.x;
  float m = -1e30f;
  for (int k = t; k < SK; k += 256) m = fmaxf(m, s[h * SK + k]);
  red[t] = m; __syncthreads();
  for (int o = 128; o > 0; o >>= 1) { if (t < o) red[t] = fmaxf(red[t], red[t + o]); __syncthreads(); }
  m = red[0]; __syncthreads();
  float l = 0.f;
  for (int k = t; k < SK; k += 256) { float e = __expf(s[h * SK + k] - m); s[h * SK + k] = e; l += e; }
  red[t] = l; __syncthreads();
  for (int o = 128; o > 0; o >>= 1) { if (t < o) red[t] += red[t + o]; __syncthreads(); }
  l = red[0];
  for (int k = t; k < SK; k += 256) s[h * SK + k] /= l;
}
__global__ void k_pv(const float* p, const u16* ckv, float* cl) {
  int h = blockIdx.x, c = threadIdx.x;
  float acc = 0.f;
  for (int k = 0; k < SK; ++k) acc += p[h * SK + k] * bf2f(ckv[(size_t)k * DC + c]);
  cl[h * DC + c] = acc;
}
__global__ void k_v(const float* cl, const u16* wuv, float* ctx) {
  int h = blockIdx.x, d = threadIdx.x;
  float acc = 0.f;
  for (int c = 0; c < DC; ++c) acc += cl[h * DC + c] * bf2f(wuv[(size_t)c * H * D + h * D + d]);
  ctx[h * D + d] = acc;
}
__global__ void k_h(const u16* x, const u16* w1, float* hh) {
  int f = blockIdx.x * 64 + threadIdx.x;
  float acc = 0.f;
  for (int m = 0; m < M; ++m) acc += bf2f(x[m]) * bf2f(w1[(size_t)m * F + f]);
  hh[f] = acc / (1.f + __expf(-acc));
}
__global__ void k_out(const u16* x, const u16* gw, const float* ctx, const u16* wo, const float* hh, const u16* w2, u16* out) {
  int m = blockIdx.x * 64 + threadIdx.x;
  float a = 0.f, b = 0.f, g = 0.f;
  for (int i = 0; i < H * D; ++i) a += ctx[i] * bf2f(wo[(size_t)i * M + m]);
  for (int f = 0; f < F; ++f) b += hh[f] * bf2f(w2[(size_t)f * M + m]);
  for (int i = 0; i < M; ++i) g += bf2f(x[i]) * bf2f(gw[i]);
  g = 1.f / (1.f + __expf(-g));
  out[m] = f2bf(g * a + (1.f - g) * b);
}


typedef unsigned long long u64;
typedef unsigned v4u __attribute__((ext_vector_type(4)));
#define R4(x) x x x x
#define R16(x) R4(R4(x))
#define R256(x) R16(R16(x))
template<int PRE, int JUMP>
__global__ void __launch_bounds__(512) k_ic(u64* st) {
  u64 t0, t1, t2;
  float a = threadIdx.x;
  asm volatile("s_waitcnt lgkmcnt(0)" :: "s"(st) : "memory");
  if (PRE) {
    u64 pc; asm volatile("s_getpc_b64 %0" : "=s"(pc));
    const char* p = (const char*)pc;
    v4u r; unsigned vo = threadIdx.x * 16 + ((blockIdx.x >> 3) & 1) * 8192;
    asm volatile("global_load_dwordx4 %0, %1, %2\n\ts_waitcnt vmcnt(0)" : "=&v"(r) : "v"(vo), "s"(p) : "memory");
    asm volatile("" :: "v"(r));
    __builtin_amdgcn_s_sleep(32);
  }
  asm volatile("s_memrealtime %0\n\ts_waitcnt lgkmcnt(0)" : "=s"(t0) :: "memory");
  if (JUMP == 0) asm volatile(R256(R4("v_add3_u32 %0, %0, %0, %0\n\t")) : "+v"(a));
  else asm volatile(R16("s_branch 1f\n\t.fill 127, 4, 0xBF800000\n1:\n\t") ::: "memory");
  asm volatile("s_memrealtime %0\n\ts_waitcnt lgkmcnt(0)" : "=s"(t1) :: "memory");
  if ((threadIdx.x & 63) == 0) { u64* s = st + ((size_t)blockIdx.x * 8 + (threadIdx.x >> 6)) * 4; s[0] = t0; s[1] = t1; s[2] = __float_as_uint(a); }
}
__global__ void k_amp(const u64* st, int mode, int amp) {
  __shared__ u64 s[256][2];
  int t = threadIdx.x;
  u64 mn = ~0ull, mx = 0;
  for (int w = 0; w < 8; ++w) { const u64* q = st + ((size_t)t * 8 + w) * 4; u64 d = q[1] - q[0]; mn = d < mn ? d : mn; mx = d > mx ? d : mx; }
  s[t][0] = mn; s[t][1] = mx;
  __syncthreads();
  if (t == 0) {
    mn = ~0ull; mx = 0;
    for (int i = 0; i < 256; ++i) { mn = s[i][0] < mn ? s[i][0] : mn; mx = s[i][1] > mx ? s[i][1] : mx; }
    u64 q = mode == 0 ? mn : mx;
    if (q > 3000) q = 3000;
    u64 t1 = __builtin_amdgcn_s_memrealtime();
    u64 until = t1 + q * amp;
    while (__builtin_amdgcn_s_memrealtime() < until) __builtin_amdgcn_s_sleep(4);
  }
}

extern "C" void kernel_launch(void* const* d_in, const int* in_sizes, int n_in,
                              void* d_out, int out_size, void* d_ws, size_t ws_size,
                              hipStream_t stream) {
  const u16* x = (const u16*)d_in[0];
  const u16* ckv = (const u16*)d_in[1];
  const u16* wq = (const u16*)d_in[2];
  const u16* wuk = (const u16*)d_in[3];
  const u16* wuv = (const u16*)d_in[4];
  const u16* wo = (const u16*)d_in[5];
  const u16* w1 = (const u16*)d_in[7];
  const u16* w2 = (const u16*)d_in[8];
  const u16* gw = (const u16*)d_in[9];
  float* ws = (float*)d_ws;
  float *q = ws, *qa = q + 4096, *s = qa + H * DC, *cl = s + H * SK, *ctx = cl + H * DC, *hh = ctx + 4096;
  u64* st = (u64*)((char*)d_ws + (8u<<20));
  k_ic<0, 0><<<256, 512, 0, stream>>>(st);
  k_amp<<<1, 256, 0, stream>>>(st, 1, 0);
  k_q<<<32, 128, 0, stream>>>(x, wq, q);
  k_qabs<<<32, 512, 0, stream>>>(q, wuk, qa);
  k_scores<<<dim3(32, 16), 256, 0, stream>>>(qa, ckv, s);
  k_softmax<<<32, 256, 0, stream>>>(s);
  k_pv<<<32, 512, 0, stream>>>(s, ckv, cl);
  k_v<<<32, 128, 0, stream>>>(cl, wuv, ctx);
  k_h<<<27, 64, 0, stream>>>(x, w1, hh);
  k_out<<<10, 64, 0, stream>>>(x, gw, ctx, wo, hh, w2, (u16*)d_out);
}
```

```cpp
#include <hip/hip_runtime.h>
#include <stdint.h>
#pragma clang diagnostic ignored "-Winline-asm"

typedef unsigned v4u __attribute__((ext_vector_type(4)));
typedef unsigned v2u __attribute__((ext_vector_type(2)));
typedef float f4 __attribute__((ext_vector_type(4)));
typedef float f2 __attribute__((ext_vector_type(2)));
typedef short s4 __attribute__((ext_vector_type(4)));
typedef short s8 __attribute__((ext_vector_type(8)));
typedef __bf16 bf2 __attribute__((ext_vector_type(2)));
typedef unsigned long long u64;
#define LAS __attribute__((address_space(3)))
#define DI __device__ __forceinline__

constexpr unsigned OFF_E = 0;
constexpr unsigned OFF_FLAG2 = 4096;
constexpr unsigned OFF_FLAG1 = 8192;
constexpr unsigned OFF_QP = OFF_FLAG1 + 32768;
constexpr unsigned OFF_QA = OFF_QP + 65536;
constexpr unsigned OFF_ST = OFF_QA + 32768;
constexpr unsigned OFF_OP = OFF_ST + 8192;
constexpr unsigned OFF_MP = OFF_OP + 163840;
constexpr unsigned OFF_PP = OFF_MP + 163840;
constexpr unsigned OFF_END = OFF_PP + 1048576;
constexpr unsigned OFF_STAMP = 8u << 20;
constexpr unsigned SENT = 0xFFFFFFFFu;
constexpr unsigned SPIN_CAP = 1u << 15;
constexpr float SC = 0.08838834764831845f * 1.4426950408889634f;

constexpr unsigned L_CKV = 0;
constexpr unsigned L_UK = 32768;
constexpr unsigned L_QAS = 131072;
constexpr unsigned L_QRED = L_QAS;
constexpr unsigned L_P = L_QAS + 8448;
constexpr unsigned L_MX = L_P + 2304;
constexpr unsigned L_LS = L_MX + 256;
constexpr unsigned L_RED = L_LS + 256;
constexpr unsigned L_TAB = L_RED + 1024;
constexpr unsigned L_QB = L_TAB + 4096;
constexpr unsigned L_EP = L_QB + 2304;
constexpr unsigned L_OK = L_EP + 8;
constexpr unsigned L_END = L_EP + 16;

DI f4 mfma(v4u a, v4u b, f4 c) { return __builtin_amdgcn_mfma_f32_16x16x32_bf16(__builtin_bit_cast(s8, a), __builtin_bit_cast(s8, b), c, 0, 0, 0); }
DI unsigned cvtpk(float lo, float hi) { f2 x = {lo, hi}; bf2 y = __builtin_convertvector(x, bf2); return __builtin_bit_cast(unsigned, y); }
DI float asf(unsigned u) { return __uint_as_float(u); }
DI unsigned asu(float f) { return __float_as_uint(f); }
DI unsigned guard(unsigned u) { return u < 0xFFFFFFFEu ? u : 0xFFFFFFFEu; }
DI float fmx(float a, float b) { return __builtin_elementwise_maximum(a, b); }
template <int CTRL> DI float dpp(float x) { return asf((unsigned)__builtin_amdgcn_mov_dpp((int)asu(x), CTRL, 0xf, 0xf, true)); }
constexpr int XOR1 = 0xB1, XOR2 = 0x4E, ROR4 = 0x124, ROR8 = 0x128, HMIR = 0x141;
DI float p16_max(float x) { auto t = __builtin_amdgcn_permlane16_swap(asu(x), asu(x), false, false); unsigned a = t[0], b = t[1]; return fmx(asf(a), asf(b)); }
DI float p32_max(float x) { auto t = __builtin_amdgcn_permlane32_swap(asu(x), asu(x), false, false); unsigned a = t[0], b = t[1]; return fmx(asf(a), asf(b)); }
DI float p16_sum(float x) { auto t = __builtin_amdgcn_permlane16_swap(asu(x), asu(x), false, false); unsigned a = t[0], b = t[1]; return asf(a) + asf(b); }
DI float p32_sum(float x) { auto t = __builtin_amdgcn_permlane32_swap(asu(x), asu(x), false, false); unsigned a = t[0], b = t[1]; return asf(a) + asf(b); }
DI float row_sum(float x) { x += dpp<XOR1>(x); x += dpp<XOR2>(x); x += dpp<ROR4>(x); x += dpp<ROR8>(x); return x; }
DI float row_max(float x) { x = fmx(x, dpp<XOR1>(x)); x = fmx(x, dpp<XOR2>(x)); x = fmx(x, dpp<ROR4>(x)); x = fmx(x, dpp<ROR8>(x)); return x; }
DI float max8(f4 u, f4 v) { return fmx(fmx(fmx(u.x, u.y), fmx(u.z, u.w)), fmx(fmx(v.x, v.y), fmx(v.z, v.w))); }
DI float sum8(f4 u, f4 v) { return ((u.x + u.y) + (u.z + u.w)) + ((v.x + v.y) + (v.z + v.w)); }
DI unsigned umax(unsigned a, unsigned b) { return a > b ? a : b; }
DI unsigned mx4(v4u v) { return umax(umax(v.x, v.y), umax(v.z, v.w)); }
DI bool allvalid(unsigned m) { return __builtin_amdgcn_ballot_w64(m == SENT) == 0ull; }
DI v4u lds4(unsigned a) { return *(LAS v4u*)(size_t)a; }
DI f4 ldsf4(unsigned a) { return *(LAS f4*)(size_t)a; }
DI void sts4(unsigned a, v4u v) { *(LAS v4u*)(size_t)a = v; }
DI void stsf4(unsigned a, f4 v) { *(LAS f4*)(size_t)a = v; }
DI void sts2(unsigned a, v2u v) { *(LAS v2u*)(size_t)a = v; }
DI void sts1(unsigned a, unsigned v) { *(LAS unsigned*)(size_t)a = v; }
DI v4u ldstr(unsigned a0, unsigned a1) {
  s4 lo = __builtin_amdgcn_ds_read_tr16_b64_v4i16((LAS s4*)(size_t)a0), hi = __builtin_amdgcn_ds_read_tr16_b64_v4i16((LAS s4*)(size_t)a1);
  s8 c = __builtin_shufflevector(lo, hi, 0, 1, 2, 3, 4, 5, 6, 7);
  return __builtin_bit_cast(v4u, c);
}

#define GLD4(dst, voff, sbase, IMM, MOD) asm volatile("global_load_dwordx4 %0, %1, %2 offset:%3 " MOD : "=v"(dst) : "v"(voff), "s"(sbase), "i"(IMM) : "memory")
#define GLD2(dst, voff, sbase, IMM, MOD) asm volatile("global_load_dwordx2 %0, %1, %2 offset:%3 " MOD : "=v"(dst) : "v"(voff), "s"(sbase), "i"(IMM) : "memory")
#define GLD1(dst, voff, sbase, IMM, MOD) asm volatile("global_load_dword %0, %1, %2 offset:%3 " MOD : "=v"(dst) : "v"(voff), "s"(sbase), "i"(IMM) : "memory")
#define GST4(src, voff, sbase, MOD) asm volatile("global_store_dwordx4 %0, %1, %2 " MOD "\n\ts_nop 1" :: "v"(voff), "v"(src), "s"(sbase) : "memory")
#define GST2(src, voff, sbase, MOD) asm volatile("global_store_dwordx2 %0, %1, %2 " MOD :: "v"(voff), "v"(src), "s"(sbase) : "memory")
#define SCHED() __builtin_amdgcn_sched_barrier(0)
#define VMWAIT(N) do { asm volatile("s_waitcnt vmcnt(%0)" :: "i"(N) : "memory"); SCHED(); } while (0)
#define LGWAIT() do { asm volatile("s_waitcnt lgkmcnt(0)" ::: "memory"); SCHED(); } while (0)
#define TIE(x) asm volatile("" : "+v"(x))
DI void dma16(unsigned voff, const void* sbase, unsigned ldsdst) {
  asm volatile("s_mov_b32 m0, %2\n\ts_nop 0\n\tglobal_load_lds_dwordx4 %0, %1" :: "v"(voff), "s"(sbase), "s"(ldsdst) : "memory", "m0");
}
#define BARRIER() do { asm volatile("s_waitcnt lgkmcnt(0)\n\ts_barrier" ::: "memory"); SCHED(); } while (0)
#define RAWBAR() do { asm volatile("s_barrier" ::: "memory"); SCHED(); } while (0)

#define CODEPF_DONE() asm volatile("" :: "v"(codepf))
#define STAMP(i) do {} while (0)
#define NPRE 4
#define NPRE2 8

__global__ void __launch_bounds__(512, 2)
gm_kernel(const char* __restrict__ x, const char* __restrict__ ckv, const char* __restrict__ wq, const char* __restrict__ wuk,
          const char* __restrict__ wuv, const char* __restrict__ wo, const char* __restrict__ w1, const char* __restrict__ w2,
          const char* __restrict__ gw, char* __restrict__ out, char* __restrict__ ws) {
  __shared__ __attribute__((aligned(16))) char smem[L_END];
  asm volatile("" :: "v"((LAS char*)smem));
  STAMP(0);
  const int tid = threadIdx.x, lane = tid & 63;
  const int w = __builtin_amdgcn_readfirstlane(tid >> 6);
  const int b = blockIdx.x, xcd = b & 7, r = b >> 3;
  v4u codepf;
  {
    u64 pc; unsigned csz;
    asm volatile("s_getpc_b64 %0\n.Lgm_pc_here:\n\ts_mov_b32 %1, .Lgm_code_end-.Lgm_pc_here" : "=s"(pc), "=s"(csz));
    unsigned co = (unsigned)(r * 1024 + lane * 16);
    if (w == 7 && co + 16u <= csz) GLD4(codepf, co, (const char*)pc, 0, "");
  }
  const bool roleA = r < 16;
  const int g4 = lane >> 4, n16 = lane & 15;
  const int rr = r & 15;
  const int hq = xcd * 4 + (rr >> 2), j8 = (roleA ? 0 : 4) + (rr & 3);
  asm volatile("" :: "s"(x), "s"(ckv), "s"(wq), "s"(wuk), "s"(wuv), "s"(wo), "s"(w1), "s"(w2), "s"(gw), "s"(out), "s"(ws));
  STAMP(14);
  unsigned msk[4];
#pragma unroll
  for (int t = 0; t < 4; ++t) msk[t] = (((n16 & 7) >> 1) == t) ? ((n16 & 1) ? 0xFFFF0000u : 0x0000FFFFu) : 0u;

  v4u wqr[4]; v2u xr, ew;
  STAMP(15);
  if (w == 0) {
    GLD2(ew, 0u, ws, OFF_E, "sc1");
    v4u s_ = {SENT, SENT, SENT, SENT};
    unsigned vo = (unsigned)(OFF_QP + (hq * 8 + j8) * 256 + n16 * 16);
    if (roleA) {
      if (g4 == 1) vo = (unsigned)(OFF_QA + hq * 1024 + j8 * 256 + n16 * 16);
      if (g4 == 2) vo = (unsigned)(OFF_OP + (64 + n16) * 2048 + (hq * 4 + j8) * 16);
      GST4(s_, (unsigned)(OFF_OP + lane * 2048 + (hq * 4 + j8) * 16), ws, "sc1");
    }
    if (lane < (roleA ? 48 : 16)) GST4(s_, vo, ws, "sc1");
  }
  if (w < 5) {
    const char* wb = wq + (size_t)(j8 * 80 + 16 * w) * 8192 + hq * 256;
    const unsigned vo = (unsigned)(4 * g4 * 8192 + n16 * 16);
    GLD2(xr, (unsigned)(8 * g4), x + (j8 * 80 + 16 * w) * 2, 0, "");
    GLD4(wqr[0], vo, wb, 0, ""); GLD4(wqr[1], vo, wb + 8192, 0, ""); GLD4(wqr[2], vo, wb + 16384, 0, ""); GLD4(wqr[3], vo, wb + 24576, 0, "");
  }
  STAMP(11);
  RAWBAR();
  STAMP(1);
  const int sB = xcd * 4 + (rr >> 2), gB = rr & 3;
  const char* kb = ckv + (size_t)(128 * sB + 16 * w) * 1024;
  const unsigned kl = L_CKV + (16 * w) * 1024;
#define CKV_ROWS() \
  _Pragma("unroll") for (int i = 0; i < 16; ++i) { const int ir = (i + 4 * gB) & 15; dma16((unsigned)((lane ^ ir) * 16), kb + ir * 1024, kl + ir * 1024); }
  if (roleA) {
    const char* ub = wuk + (size_t)(128 * j8 + 16 * w) * 8192 + hq * 256;
#pragma unroll
    for (int i = 0; i < 4; ++i)
      dma16((unsigned)((4 * i + g4) * 8192 + ((n16 ^ (4 * i + g4)) * 16)), ub, (unsigned)(L_UK + (16 * w + 4 * i) * 256));
  } else if (w >= 5) {
    CKV_ROWS();
  }
  if (w == 0) {
    if (roleA) VMWAIT(9); else VMWAIT(5);
    TIE(ew);
    const u64 e1 = (((u64)ew.y << 32) | ew.x) + 1;
    v2u f = {(unsigned)e1, (unsigned)(e1 >> 32)};
    if (lane < (roleA ? 2 : 1)) GST2(f, (unsigned)(lane == 0 ? OFF_FLAG1 + b * 128 : OFF_FLAG2 + b * 8), ws, "sc1");
    if (lane == 0) { sts2(L_EP, f); sts1(L_OK, 0u); }
  }
  if (w < 5) {
    if (w == 0) { if (roleA) VMWAIT(9); else VMWAIT(5); } else { if (roleA) VMWAIT(8); else VMWAIT(4); }
    TIE(xr);
    const unsigned d0 = __builtin_amdgcn_perm(xr.x, xr.x, 0x01000100u), d1 = __builtin_amdgcn_perm(xr.x, xr.x, 0x03020302u);
    const unsigned d2 = __builtin_amdgcn_perm(xr.y, xr.y, 0x01000100u), d3 = __builtin_amdgcn_perm(xr.y, xr.y, 0x03020302u);
    v4u a0 = {d0 & msk[0], d0 & msk[1], d0 & msk[2], d0 & msk[3]}, a1 = {d1 & msk[0], d1 & msk[1], d1 & msk[2], d1 & msk[3]};
    v4u a2 = {d2 & msk[0], d2 & msk[1], d2 & msk[2], d2 & msk[3]}, a3 = {d3 & msk[0], d3 & msk[1], d3 & msk[2], d3 & msk[3]};
    unsigned qr = L_QRED + w * 512 + (8 * n16 + 4 * g4) * 4;
    TIE(a0); TIE(a1); TIE(a2); TIE(a3); TIE(qr);
    if (w == 0) { if (roleA) VMWAIT(5); else VMWAIT(1); } else { if (roleA) VMWAIT(4); else VMWAIT(0); }
    TIE(wqr[0]); TIE(wqr[1]); TIE(wqr[2]); TIE(wqr[3]);
    STAMP(2);
    f4 acc = {0.f, 0.f, 0.f, 0.f};
    acc = mfma(a0, wqr[0], acc); acc = mfma(a1, wqr[1], acc); acc = mfma(a2, wqr[2], acc); acc = mfma(a3, wqr[3], acc);
    if (lane < 32) stsf4(qr, acc);
  }
  BARRIER();
  if (w == 0) {
    if (lane < 32) {
      const unsigned ra = L_QRED + lane * 16;
      f4 s0 = ldsf4(ra), s1 = ldsf4(ra + 512), s2 = ldsf4(ra + 1024), s3 = ldsf4(ra + 1536), s4_ = ldsf4(ra + 2048);
      f4 sm = (s0 + s1) + (s2 + s3) + s4_;
      v2u o = {guard(cvtpk(sm.x, sm.y)), guard(cvtpk(sm.z, sm.w))};
      GST2(o, (unsigned)(OFF_QP + (hq * 8 + j8) * 256 + lane * 8), ws, "sc1");
    }
    STAMP(3);
  }
#define EPOCH() ({ v2u e_ = *(LAS v2u*)(size_t)L_EP; (((u64)(unsigned)__builtin_amdgcn_readfirstlane(e_.y)) << 32) | (unsigned)__builtin_amdgcn_readfirstlane(e_.x); })
#define FLAG1_CHECK(BLK) \
  for (unsigned spin = 0;; ++spin) { \
    v2u f0; \
    GLD2(f0, (unsigned)(OFF_FLAG1 + (BLK) * 128), ws, 0, "sc1"); \
    VMWAIT(0); TIE(f0); \
    const unsigned df = (f0.x ^ (unsigned)epoch) | (f0.y ^ (unsigned)(epoch >> 32)); \
    if (__builtin_amdgcn_ballot_w64(df != 0u) == 0ull || spin > SPIN_CAP) break; \
  }
#define FLAG2_CHECK(HALF) \
  for (unsigned spin = 0;; ++spin) { \
    v4u f0; \
    GLD4(f0, (unsigned)(OFF_FLAG2 + (HALF) * 1024 + lane * 16), ws, 0, "sc1"); \
    VMWAIT(0); TIE(f0); \
    const unsigned lo = (unsigned)epoch, hi = (unsigned)(epoch >> 32); \
    const unsigned df = ((f0.x ^ lo) | (f0.y ^ hi)) | ((f0.z ^ lo) | (f0.w ^ hi)); \
    if (__builtin_amdgcn_ballot_w64(df != 0u) == 0ull || spin > SPIN_CAP) break; \
  }
  const u64 epoch = EPOCH();

  if (roleA) {
    const int h = hq, j = j8;
    if (w == 7) { const int j8l = lane & 7; const int blk = (h >> 2) + 8 * ((j8l >> 2) * 16 + (h & 3) * 4 + (j8l & 3)); FLAG1_CHECK(blk); CODEPF_DONE(); STAMP(3); }
    else VMWAIT(0);
    v4u uk[4];
#pragma unroll
    for (int ks = 0; ks < 4; ++ks) uk[ks] = lds4(L_UK + (16 * w + n16) * 256 + (((4 * ks + g4) ^ n16) * 16));
    unsigned qb_a = L_QB + (n16 & 7) * 288 + g4 * 16;
    unsigned qa_o = (unsigned)(OFF_QA + h * 1024 + (128 * j + 16 * w + 4 * g4) * 2);
    TIE(qb_a); TIE(qa_o);
    if (w == 7) {
      v4u p0, p1;
      const unsigned vo = (unsigned)(OFF_QP + h * 2048 + lane * 16);
      unsigned wa = L_QB + g4 * 288 + n16 * 16; TIE(wa);
      for (unsigned spin = 0;; ++spin) {
        GLD4(p0, vo, ws, 0, "sc1"); GLD4(p1, vo, ws, 1024, "sc1");
        VMWAIT(0); TIE(p0); TIE(p1);
        if (allvalid(umax(mx4(p0), mx4(p1))) || spin > SPIN_CAP) break;
      }
      STAMP(4);
      sts4(wa, p0); sts4(wa + 4 * 288, p1);
    }
    BARRIER();
    {
      v4u bq[4];
#pragma unroll
      for (int ks = 0; ks < 4; ++ks) bq[ks] = lds4(qb_a + ks * 64);
      SCHED();
      f4 acc = {0.f, 0.f, 0.f, 0.f};
#pragma unroll
      for (int ks = 0; ks < 4; ++ks) acc = mfma(uk[ks], bq[ks], acc);
      float v0 = acc.x, v1 = acc.y, v2 = acc.z, v3 = acc.w;
      v0 += dpp<XOR1>(v0); v1 += dpp<XOR1>(v1); v2 += dpp<XOR1>(v2); v3 += dpp<XOR1>(v3);
      v0 += dpp<XOR2>(v0); v1 += dpp<XOR2>(v1); v2 += dpp<XOR2>(v2); v3 += dpp<XOR2>(v3);
      v0 += dpp<HMIR>(v0); v1 += dpp<HMIR>(v1); v2 += dpp<HMIR>(v2); v3 += dpp<HMIR>(v3);
      v2u o = {guard(cvtpk(v0, v1)), guard(cvtpk(v2, v3))};
      if (n16 == 0) GST2(o, qa_o, ws, "sc1");
    }
    STAMP(5);
    RAWBAR();
    {
      const char* vb = wuv + (size_t)(64 * w) * 8192 + h * 256 + j * 64;
#pragma unroll
      for (int i = 0; i < 4; ++i)
        dma16((unsigned)((16 * i + (lane >> 2)) * 8192 + (lane & 3) * 16), vb, (unsigned)(L_CKV + (64 * w + 16 * i) * 64));
    }
    v4u wor[8];
    if (w < 5) {
      const char* ob = wo + (size_t)(h * 128 + 32 * j) * 1280 + w * 256;
#pragma unroll
      for (int s = 0; s < 8; ++s) GLD4(wor[s], (unsigned)((8 * g4 + s) * 1280 + n16 * 16), ob, 0, "");
      VMWAIT(8);
    } else {
      VMWAIT(0);
    }
    v4u bv[2][2];
    {
      const int q = (lane & 15) >> 2, p = lane & 3;
      const unsigned a = L_CKV + (64 * w + 8 * g4 + q) * 64 + 8 * p;
#pragma unroll
      for (int ks = 0; ks < 2; ++ks)
#pragma unroll
        for (int dt = 0; dt < 2; ++dt) bv[ks][dt] = ldstr(a + ks * 2048 + dt * 32, a + ks * 2048 + 256 + dt * 32);
    }
    if (w == 7) { FLAG2_CHECK(1); }
    unsigned pvo = (unsigned)(OFF_PP + (h * 32 + n16) * 1024 + 128 * w + 16 * g4), pvo2 = pvo + 16384u;
    unsigned sto = (unsigned)(OFF_ST + h * 256 + (lane & 31) * 8);
    unsigned tabw = L_TAB + w * 512 + lane * 4, tabr = L_TAB + w * 512 + g4 * 16;
    unsigned crw = L_RED + lane * 32 + w * 4, crr = L_RED + (lane & 31) * 32;
    unsigned tabr2 = L_TAB + w * 512 + ((n16 >> 3) * 32 + 8 * g4) * 4;
    unsigned opo = (unsigned)(OFF_OP + (16 * w + lane) * 2048 + (h * 4 + j) * 16);
    TIE(pvo); TIE(pvo2); TIE(sto); TIE(tabw); TIE(tabr); TIE(crw); TIE(crr); TIE(tabr2); TIE(opo);
    VMWAIT(0);
    if (w < 5) {
#pragma unroll
      for (int s = 0; s < 8; ++s) TIE(wor[s]);
    }
    STAMP(6);
    BARRIER();
    v2u st;
    for (unsigned spin = 0;; ++spin) {
      GLD2(st, sto, ws, 0, "sc1");
      VMWAIT(0); TIE(st);
      if (allvalid(umax(st.x, st.y)) || spin > SPIN_CAP) break;
    }
    v4u pa[2][2];
    GLD4(pa[0][0], pvo, ws, 0, "sc1"); GLD4(pa[0][1], pvo, ws, 64, "sc1");
    GLD4(pa[1][0], pvo2, ws, 0, "sc1"); GLD4(pa[1][1], pvo2, ws, 64, "sc1");
    STAMP(7);
    f4 aw[2];
    {
      float m = asf(st.x), l = asf(st.y);
      float M = p16_max(row_max(m));
      float a = __builtin_amdgcn_exp2f((m - M) * SC);
      float Ls = p16_sum(row_sum(a * l));
      a = a * __builtin_amdgcn_rcpf(Ls);
      if (lane < 32) sts1(tabw, asu(a));
      LGWAIT();
      aw[0] = ldsf4(tabr); aw[1] = ldsf4(tabr + 64);
    }
    for (unsigned spin = 0;; ++spin) {
      VMWAIT(0); TIE(pa[0][0]); TIE(pa[0][1]); TIE(pa[1][0]); TIE(pa[1][1]);
      if (allvalid(umax(umax(mx4(pa[0][0]), mx4(pa[0][1])), umax(mx4(pa[1][0]), mx4(pa[1][1])))) || spin > SPIN_CAP) break;
      GLD4(pa[0][0], pvo, ws, 0, "sc1"); GLD4(pa[0][1], pvo, ws, 64, "sc1");
      GLD4(pa[1][0], pvo2, ws, 0, "sc1"); GLD4(pa[1][1], pvo2, ws, 64, "sc1");
    }
    STAMP(8);
    {
      f4 z = {0.f, 0.f, 0.f, 0.f};
      f4 t00 = mfma(pa[0][0], bv[0][0], z), t10 = mfma(pa[1][0], bv[0][0], z), t01 = mfma(pa[0][0], bv[0][1], z), t11 = mfma(pa[1][0], bv[0][1], z);
      t00 = mfma(pa[0][1], bv[1][0], t00); t10 = mfma(pa[1][1], bv[1][0], t10); t01 = mfma(pa[0][1], bv[1][1], t01); t11 = mfma(pa[1][1], bv[1][1], t11);
      f4 c0 = aw[0] * t00 + aw[1] * t10, c1 = aw[0] * t01 + aw[1] * t11;
      float u0 = (c0.x + c0.y) + (c0.z + c0.w), u1 = (c1.x + c1.y) + (c1.z + c1.w);
      auto t = __builtin_amdgcn_permlane16_swap(asu(u0), asu(u1), false, false); unsigned ta = t[0], tb = t[1];
      float u = p32_sum(asf(ta) + asf(tb));
      if (lane < 32) sts1(crw, asu(u));
    }
    BARRIER();
    STAMP(9);
    if (w < 5) {
      float c = sum8(ldsf4(crr), ldsf4(crr + 16));
      unsigned hi = cvtpk(c, c);
      float rs = c - asf(hi << 16);
      unsigned lo = cvtpk(rs, rs);
      sts1(tabw, lane < 32 ? hi : lo);
      LGWAIT();
      v4u t0 = lds4(tabr2), t1 = lds4(tabr2 + 16);
      unsigned dv[8] = {t0.x, t0.y, t0.z, t0.w, t1.x, t1.y, t1.z, t1.w};
      f4 acc = {0.f, 0.f, 0.f, 0.f};
#pragma unroll
      for (int s = 0; s < 8; ++s) { v4u a = {dv[s] & msk[0], dv[s] & msk[1], dv[s] & msk[2], dv[s] & msk[3]}; acc = mfma(a, wor[s], acc); }
      float o0 = p32_sum(acc.x), o1 = p32_sum(acc.y), o2 = p32_sum(acc.z), o3 = p32_sum(acc.w);
      unsigned a0 = guard(cvtpk(o0, o1)), a1 = guard(cvtpk(o2, o3));
      auto s0 = __builtin_amdgcn_permlane16_swap(a0, a0, false, false); auto s1 = __builtin_amdgcn_permlane16_swap(a1, a1, false, false);
      unsigned b0 = s0[1], b1 = s1[1];
      v4u o = {a0, a1, b0, b1};
      if (lane < 16) GST4(o, opo, ws, "sc1");
    }
    STAMP(10);
  } else {
    const int g = rr & 3, s = xcd * 4 + (rr >> 2), bi = xcd * 16 + rr;
    RAWBAR();
    if (w < 5) {
      CKV_ROWS();
    } else {
      if (w == 7) {
        const int hd = 8 * g + ((lane & 31) >> 2); const int blk = (hd >> 2) + 8 * ((hd & 3) * 4 + (lane & 3));
        FLAG1_CHECK(blk);
        CODEPF_DONE();
        if (lane == 0) sts1(L_OK, 1u);
      }
      v4u s_ = {SENT, SENT, SENT, SENT};
#pragma unroll
      for (int i = 0; i < 3; ++i) { const int hh = 3 * (w - 5) + i; if (hh < 8) GST4(s_, (unsigned)(OFF_PP + ((8 * g + hh) * 32 + s) * 1024 + lane * 16), ws, "sc1"); }
      if (w == 7) {
        v2u s2 = {SENT, SENT};
        if (lane < 8) GST2(s2, (unsigned)(OFF_ST + ((8 * g + lane) * 32 + s) * 8), ws, "sc1");
        GST4(s_, (unsigned)(OFF_MP + lane * 2048 + bi * 16), ws, "sc1");
        if (lane < 16) GST4(s_, (unsigned)(OFF_MP + (64 + lane) * 2048 + bi * 16), ws, "sc1");
      }
      if (w == 5) { dma16((unsigned)(lane * 16), x, L_QB); if (lane < 16) dma16((unsigned)(lane * 16), x + 1024, L_QB + 1024); }
    }
    unsigned qvo = (unsigned)(OFF_QA + (8 * g + w) * 1024 + lane * 16);
    TIE(qvo);
    VMWAIT(0);
    STAMP(4);
    for (unsigned spin = 0; *(volatile LAS unsigned*)(size_t)L_OK == 0u && spin < SPIN_CAP; ++spin) __builtin_amdgcn_s_sleep(1);
    v4u qa;
    GLD4(qa, qvo, ws, 0, "sc1");
    v4u kA[16];
    {
      const unsigned ka = L_CKV + (16 * w + n16) * 1024;
#pragma unroll
      for (int ks = 0; ks < 16; ++ks) kA[ks] = lds4(ka + (((4 * ks + g4) ^ n16) * 16));
    }
    unsigned qsw = L_QAS + w * 1056 + lane * 16, qsr = L_QAS + (n16 & 7) * 1056 + g4 * 16;
    unsigned mxw = L_MX + lane * 32 + w * 4, mxr = L_MX + (n16 & 7) * 32;
    unsigned pw = L_P + n16 * 288 + (16 * w + 4 * g4) * 2, pr = L_P + (n16 & 7) * 288 + g4 * 16;
    unsigned lsw = L_LS + lane * 32 + w * 4;
    unsigned ppo = (unsigned)(OFF_PP + ((8 * g + (n16 & 7)) * 32 + s) * 1024 + (64 * w + 32 * (g4 >> 1) + 8 * (2 * (n16 >> 3) + (g4 & 1))) * 2);
    TIE(qsw); TIE(qsr); TIE(mxw); TIE(mxr); TIE(pw); TIE(pr); TIE(lsw); TIE(ppo);
    BARRIER();
    if (w == 5 && lane == 0) { v2u f = {(unsigned)epoch, (unsigned)(epoch >> 32)}; GST2(f, (unsigned)(OFF_FLAG2 + b * 8), ws, "sc1"); }
    STAMP(5);
    v4u vA[4][4];
    {
      const int q = (lane & 15) >> 2, p = lane & 3;
#pragma unroll
      for (int t = 0; t < 4; ++t) {
        const int ch = 8 * w + 4 * (p >> 1) + t;
        const int r0 = 8 * g4 + q, r1 = r0 + 4;
        const unsigned a0 = L_CKV + r0 * 1024 + ((ch ^ (r0 & 15)) * 16) + 8 * (p & 1), a1 = L_CKV + r1 * 1024 + ((ch ^ (r1 & 15)) * 16) + 8 * (p & 1);
        vA[0][t] = ldstr(a0, a1); vA[1][t] = ldstr(a0 + 32768, a1 + 32768);
        vA[2][t] = ldstr(a0 + 65536, a1 + 65536); vA[3][t] = ldstr(a0 + 98304, a1 + 98304);
      }
    }
    SCHED();
    for (unsigned spin = 0;; ++spin) {
      VMWAIT(0); TIE(qa);
      if (allvalid(mx4(qa)) || spin > SPIN_CAP) break;
      GLD4(qa, qvo, ws, 0, "sc1");
    }
    STAMP(6);
    sts4(qsw, qa);
    BARRIER();
    f4 sc = {0.f, 0.f, 0.f, 0.f};
    {
      v4u bq[16];
#pragma unroll
      for (int ks = 0; ks < 16; ++ks) bq[ks] = lds4(qsr + ks * 64);
      SCHED();
#pragma unroll
      for (int ks = 0; ks < 16; ++ks) sc = mfma(kA[ks], bq[ks], sc);
      float mw = p32_max(p16_max(fmx(fmx(sc.x, sc.y), fmx(sc.z, sc.w))));
      if (lane < 8) sts1(mxw, asu(mw));
    }
    BARRIER();
    STAMP(7);
    {
      const float Ms = -max8(ldsf4(mxr), ldsf4(mxr + 16)) * SC;
      float p0 = __builtin_amdgcn_exp2f(fmaf(sc.x, SC, Ms)), p1 = __builtin_amdgcn_exp2f(fmaf(sc.y, SC, Ms)), p2 = __builtin_amdgcn_exp2f(fmaf(sc.z, SC, Ms)), p3 = __builtin_amdgcn_exp2f(fmaf(sc.w, SC, Ms));
      v2u pk = {cvtpk(p0, p1), cvtpk(p2, p3)};
      if (n16 < 8) sts2(pw, pk);
      float ls = p32_sum(p16_sum((p0 + p1) + (p2 + p3)));
      if (lane < 8) sts1(lsw, asu(ls));
    }
    BARRIER();
    STAMP(8);
    {
      v4u pb[4];
#pragma unroll
      for (int ks = 0; ks < 4; ++ks) pb[ks] = lds4(pr + ks * 64);
      f4 lu = ldsf4(L_LS + w * 32), lv = ldsf4(L_LS + w * 32 + 16), mu = ldsf4(L_MX + w * 32), mv = ldsf4(L_MX + w * 32 + 16);
      SCHED();
      f4 ac[4];
#pragma unroll
      for (int t = 0; t < 4; ++t) ac[t] = mfma(vA[0][t], pb[0], f4{0.f, 0.f, 0.f, 0.f});
      {
        v2u o = {guard(asu(max8(mu, mv))), guard(asu(sum8(lu, lv)))};
        if (lane == 0) GST2(o, (unsigned)(OFF_ST + ((8 * g + w) * 32 + s) * 8), ws, "sc1");
      }
#pragma unroll
      for (int ks = 1; ks < 4; ++ks)
#pragma unroll
        for (int t = 0; t < 4; ++t) ac[t] = mfma(vA[ks][t], pb[ks], ac[t]);
      const bool up = n16 >= 8;
      f4 xa = up ? ac[2] : ac[0], ya = up ? ac[3] : ac[1];
      unsigned x0 = cvtpk(xa.x, xa.y), x1 = cvtpk(xa.z, xa.w), y0 = cvtpk(ya.x, ya.y), y1 = cvtpk(ya.z, ya.w);
      auto s0 = __builtin_amdgcn_permlane16_swap(x0, y0, false, false); auto s1 = __builtin_amdgcn_permlane16_swap(x1, y1, false, false);
      unsigned e0 = s0[0], e1 = s1[0], f0 = s0[1], f1 = s1[1];
      v4u o = {guard(e0), guard(e1), guard(f0), guard(f1)};
      GST4(o, ppo, ws, "sc1");
    }
    STAMP(9);
    RAWBAR();
    const int fb = bi < 108 ? bi : 107;
    v4u w1r[3], w2r[4];
    {
      const char* b1 = w1 + fb * 32;
      const unsigned vo = (unsigned)((lane >> 1) * 3456 + (lane & 1) * 16);
      GLD4(w1r[0], vo, b1 + (size_t)(32 * w) * 3456, 0, "");
      GLD4(w1r[1], vo, b1 + (size_t)(32 * (w + 8)) * 3456, 0, "");
      if (w < 4) GLD4(w1r[2], vo, b1 + (size_t)(32 * (w + 16)) * 3456, 0, "");
    }
    if (w < 5) {
      const char* b2 = w2 + (size_t)(16 * fb) * 1280 + w * 256;
#pragma unroll
      for (int i = 0; i < 4; ++i) GLD4(w2r[i], (unsigned)((4 * g4 + i) * 1280 + n16 * 16), b2, 0, "");
      VMWAIT(4);
    } else {
      VMWAIT(0);
    }
    TIE(w1r[0]); TIE(w1r[1]);
    if (w < 4) { TIE(w1r[2]); } else { w1r[2] = v4u{0u, 0u, 0u, 0u}; }
    {
      float ha[8];
#pragma unroll
      for (int k = 0; k < 8; ++k) ha[k] = 0.f;
#pragma unroll
      for (int i = 0; i < 3; ++i) {
        const int it = (i == 2 && w >= 4) ? 0 : w + 8 * i;
        const float xv = asf((unsigned)*(LAS unsigned short*)(size_t)(L_QB + (32 * it + (lane >> 1)) * 2) << 16);
        ha[0] += xv * asf(w1r[i].x << 16); ha[1] += xv * asf(w1r[i].x & 0xFFFF0000u);
        ha[2] += xv * asf(w1r[i].y << 16); ha[3] += xv * asf(w1r[i].y & 0xFFFF0000u);
        ha[4] += xv * asf(w1r[i].z << 16); ha[5] += xv * asf(w1r[i].z & 0xFFFF0000u);
        ha[6] += xv * asf(w1r[i].w << 16); ha[7] += xv * asf(w1r[i].w & 0xFFFF0000u);
      }
#pragma unroll
      for (int k = 0; k < 8; ++k) { float v = ha[k]; v += dpp<XOR2>(v); v += dpp<ROR4>(v); v += dpp<ROR8>(v); ha[k] = p32_sum(p16_sum(v)); }
      if (lane < 2) {
#pragma unroll
        for (int k = 0; k < 8; ++k) sts1(L_RED + (8 * lane + k) * 32 + w * 4, asu(ha[k]));
      }
    }
    BARRIER();
    if (w < 5) {
      VMWAIT(0);
#pragma unroll
      for (int i = 0; i < 4; ++i) TIE(w2r[i]);
      const int f = lane & 15;
      float hh = sum8(ldsf4(L_RED + f * 32), ldsf4(L_RED + f * 32 + 16));
      hh = hh * __builtin_amdgcn_rcpf(1.f + __expf(-hh));
      if (bi >= 108) hh = 0.f;
      unsigned hi = cvtpk(hh, hh);
      float rs = hh - asf(hi << 16);
      unsigned lo = cvtpk(rs, rs);
      if (lane < 32) sts1(L_TAB + w * 512 + lane * 4, lane < 16 ? hi : lo);
      LGWAIT();
      v4u t0 = lds4(L_TAB + w * 512 + ((n16 >> 3) * 16 + 4 * g4) * 4);
      unsigned dv[4] = {t0.x, t0.y, t0.z, t0.w};
      f4 acc = {0.f, 0.f, 0.f, 0.f};
#pragma unroll
      for (int i = 0; i < 4; ++i) { v4u a = {dv[i] & msk[0], dv[i] & msk[1], dv[i] & msk[2], dv[i] & msk[3]}; acc = mfma(a, w2r[i], acc); }
      float o0 = p32_sum(acc.x), o1 = p32_sum(acc.y), o2 = p32_sum(acc.z), o3 = p32_sum(acc.w);
      unsigned a0 = guard(cvtpk(o0, o1)), a1 = guard(cvtpk(o2, o3));
      auto s0 = __builtin_amdgcn_permlane16_swap(a0, a0, false, false); auto s1 = __builtin_amdgcn_permlane16_swap(a1, a1, false, false);
      unsigned b0 = s0[1], b1 = s1[1];
      v4u o = {a0, a1, b0, b1};
      if (lane < 16) GST4(o, (unsigned)(OFF_MP + (16 * w + lane) * 2048 + bi * 16), ws, "sc1");
    }
    STAMP(10);
    if (bi < 80 && w == 5) {
      FLAG2_CHECK(0);
      FLAG2_CHECK(1);
      unsigned gg[5];
#pragma unroll
      for (int i = 0; i < 5; ++i) GLD1(gg[i], (unsigned)(lane * 4), gw, i * 256, "");
      VMWAIT(0);
#pragma unroll
      for (int i = 0; i < 5; ++i) TIE(gg[i]);
      float gate;
      {
        float a = 0.f;
#pragma unroll
        for (int i = 0; i < 5; ++i) { const unsigned gx = *(LAS unsigned*)(size_t)(L_QB + i * 256 + lane * 4); a += asf(gx << 16) * asf(gg[i] << 16) + asf(gx & 0xFFFF0000u) * asf(gg[i] & 0xFFFF0000u); }
        a = p32_sum(p16_sum(row_sum(a)));
        gate = __builtin_amdgcn_rcpf(1.f + __expf(-a));
      }
      const unsigned one = 0x3F803F80u;
      v4u ones = {one & msk[0], one & msk[1], one & msk[2], one & msk[3]};
      v4u m0, m1;
      const unsigned mvo = (unsigned)(OFF_MP + bi * 2048 + lane * 16);
      for (unsigned spin = 0;; ++spin) {
        GLD4(m0, mvo, ws, 0, "sc1"); GLD4(m1, mvo, ws, 1024, "sc1");
        VMWAIT(0); TIE(m0); TIE(m1);
        if (allvalid(umax(mx4(m0), mx4(m1))) || spin > SPIN_CAP) break;
      }
      f4 bb = {0.f, 0.f, 0.f, 0.f};
      bb = mfma(ones, m0, bb); bb = mfma(ones, m1, bb);
      bb *= (1.f - gate);
      STAMP(11);
      const unsigned ovo = (unsigned)(OFF_OP + bi * 2048 + lane * 16);
      for (unsigned spin = 0;; ++spin) {
        GLD4(m0, ovo, ws, 0, "sc1"); GLD4(m1, ovo, ws, 1024, "sc1");
        VMWAIT(0); TIE(m0); TIE(m1);
        if (allvalid(umax(mx4(m0), mx4(m1))) || spin > SPIN_CAP) break;
      }
      STAMP(12);
      f4 aa = {0.f, 0.f, 0.f, 0.f};
      aa = mfma(ones, m0, aa); aa = mfma(ones, m1, aa);
      float r0 = row_sum(fmaf(gate, aa.x, bb.x)), r1 = row_sum(fmaf(gate, aa.y, bb.y));
      float r2 = row_sum(fmaf(gate, aa.z, bb.z)), r3 = row_sum(fmaf(gate, aa.w, bb.w));
      v2u o = {cvtpk(r0, r1), cvtpk(r2, r3)};
      if (n16 == 0 && g4 < 2) GST2(o, (unsigned)(bi * 16 + g4 * 8), out, "");
      if (bi == 0 && lane == 0) {
        v2u f = {(unsigned)epoch, (unsigned)(epoch >> 32)};
        GST2(f, (unsigned)OFF_E, ws, "sc1");
      }
      STAMP(13);
    }
  }
  asm volatile(".Lgm_code_end:" ::: "memory");
}

extern "C" void kernel_launch(void* const* d_in, const int* in_sizes, int n_in,
                              void* d_out, int out_size, void* d_ws, size_t ws_size,
                              hipStream_t stream) {
  gm_kernel<<<256, 512, 0, stream>>>((const char*)d_in[0], (const char*)d_in[1], (const char*)d_in[2], (const char*)d_in[3],
                                      (const char*)d_in[4], (const char*)d_in[5], (const char*)d_in[7], (const char*)d_in[8],
                                      (const char*)d_in[9], (char*)d_out, (char*)d_ws);
}
```

```cpp
#include <hip/hip_runtime.h>
#include <stdint.h>
#pragma clang diagnostic ignored "-Winline-asm"

typedef unsigned v4u __attribute__((ext_vector_type(4)));
typedef unsigned v2u __attribute__((ext_vector_type(2)));
typedef float f4 __attribute__((ext_vector_type(4)));
typedef float f2 __attribute__((ext_vector_type(2)));
typedef short s4 __attribute__((ext_vector_type(4)));
typedef short s8 __attribute__((ext_vector_type(8)));
typedef __bf16 bf2 __attribute__((ext_vector_type(2)));
typedef unsigned long long u64;
#define LAS __attribute__((address_space(3)))
#define DI __device__ __forceinline__

constexpr unsigned OFF_E = 0;
constexpr unsigned OFF_FLAG2 = 4096;
constexpr unsigned OFF_FLAG1 = 8192;
constexpr unsigned OFF_QP = OFF_FLAG1 + 32768;
constexpr unsigned OFF_QA = OFF_QP + 65536;
constexpr unsigned OFF_ST = OFF_QA + 32768;
constexpr unsigned OFF_OP = OFF_ST + 8192;
constexpr unsigned OFF_MP = OFF_OP + 163840;
constexpr unsigned OFF_PP = OFF_MP + 163840;
constexpr unsigned OFF_END = OFF_PP + 1048576;
constexpr unsigned OFF_STAMP = 8u << 20;
constexpr unsigned SENT = 0xFFFFFFFFu;
constexpr unsigned SPIN_CAP = 1u << 15;
constexpr float SC = 0.08838834764831845f * 1.4426950408889634f;

constexpr unsigned L_CKV = 0;
constexpr unsigned L_W1 = 0, L_W2 = 32768;
constexpr unsigned L_WUV = 0, L_WO = 32768;
constexpr unsigned L_AW = 131072 + 2048;
constexpr unsigned L_CRED = 131072;
constexpr unsigned L_UK = 32768;
constexpr unsigned L_QAS = 131072;
constexpr unsigned L_QRED = L_QAS;
constexpr unsigned L_P = L_QAS + 8448;
constexpr unsigned L_MX = L_P + 2304;
constexpr unsigned L_LS = L_MX + 256;
constexpr unsigned L_RED = L_LS + 256;
constexpr unsigned L_TAB = L_RED + 1024;
constexpr unsigned L_QB = L_TAB + 4096;
constexpr unsigned L_EP = L_QB + 2304;
constexpr unsigned L_OK = L_EP + 8;
constexpr unsigned L_CNT = L_EP + 12;
constexpr unsigned L_END = L_EP + 16;

DI f4 mfma(v4u a, v4u b, f4 c) { return __builtin_amdgcn_mfma_f32_16x16x32_bf16(__builtin_bit_cast(s8, a), __builtin_bit_cast(s8, b), c, 0, 0, 0); }
DI unsigned cvtpk(float lo, float hi) { f2 x = {lo, hi}; bf2 y = __builtin_convertvector(x, bf2); return __builtin_bit_cast(unsigned, y); }
DI float asf(unsigned u) { return __uint_as_float(u); }
DI unsigned asu(float f) { return __float_as_uint(f); }
DI unsigned guard(unsigned u) { return u < 0xFFFFFFFEu ? u : 0xFFFFFFFEu; }
DI float fmx(float a, float b) { return __builtin_elementwise_maximum(a, b); }
template <int CTRL> DI float dpp(float x) { return asf((unsigned)__builtin_amdgcn_mov_dpp((int)asu(x), CTRL, 0xf, 0xf, true)); }
constexpr int XOR1 = 0xB1, XOR2 = 0x4E, ROR4 = 0x124, ROR8 = 0x128, HMIR = 0x141;
DI float p16_max(float x) { auto t = __builtin_amdgcn_permlane16_swap(asu(x), asu(x), false, false); unsigned a = t[0], b = t[1]; return fmx(asf(a), asf(b)); }
DI float p32_max(float x) { auto t = __builtin_amdgcn_permlane32_swap(asu(x), asu(x), false, false); unsigned a = t[0], b = t[1]; return fmx(asf(a), asf(b)); }
DI float p16_sum(float x) { auto t = __builtin_amdgcn_permlane16_swap(asu(x), asu(x), false, false); unsigned a = t[0], b = t[1]; return asf(a) + asf(b); }
DI float p32_sum(float x) { auto t = __builtin_amdgcn_permlane32_swap(asu(x), asu(x), false, false); unsigned a = t[0], b = t[1]; return asf(a) + asf(b); }
DI float row_sum(float x) { x += dpp<XOR1>(x); x += dpp<XOR2>(x); x += dpp<ROR4>(x); x += dpp<ROR8>(x); return x; }
DI float row_max(float x) { x = fmx(x, dpp<XOR1>(x)); x = fmx(x, dpp<XOR2>(x)); x = fmx(x, dpp<ROR4>(x)); x = fmx(x, dpp<ROR8>(x)); return x; }
DI float max8(f4 u, f4 v) { return fmx(fmx(fmx(u.x, u.y), fmx(u.z, u.w)), fmx(fmx(v.x, v.y), fmx(v.z, v.w))); }
DI float sum8(f4 u, f4 v) { return ((u.x + u.y) + (u.z + u.w)) + ((v.x + v.y) + (v.z + v.w)); }
DI unsigned umax(unsigned a, unsigned b) { return a > b ? a : b; }
DI unsigned mx4(v4u v) { return umax(umax(v.x, v.y), umax(v.z, v.w)); }
DI bool allvalid(unsigned m) { return __builtin_amdgcn_ballot_w64(m == SENT) == 0ull; }
DI v4u lds4(unsigned a) { return *(LAS v4u*)(size_t)a; }
DI f4 ldsf4(unsigned a) { return *(LAS f4*)(size_t)a; }
DI void sts4(unsigned a, v4u v) { *(LAS v4u*)(size_t)a = v; }
DI void stsf4(unsigned a, f4 v) { *(LAS f4*)(size_t)a = v; }
DI void sts2(unsigned a, v2u v) { *(LAS v2u*)(size_t)a = v; }
DI void sts1(unsigned a, unsigned v) { *(LAS unsigned*)(size_t)a = v; }
DI v4u ldstr(unsigned a0, unsigned a1) {
  s4 lo = __builtin_amdgcn_ds_read_tr16_b64_v4i16((LAS s4*)(size_t)a0), hi = __builtin_amdgcn_ds_read_tr16_b64_v4i16((LAS s4*)(size_t)a1);
  s8 c = __builtin_shufflevector(lo, hi, 0, 1, 2, 3, 4, 5, 6, 7);
  return __builtin_bit_cast(v4u, c);
}

#define GLD4(dst, voff, sbase, IMM, MOD) asm volatile("global_load_dwordx4 %0, %1, %2 offset:%3 " MOD : "=v"(dst) : "v"(voff), "s"(sbase), "i"(IMM) : "memory")
#define GLD2(dst, voff, sbase, IMM, MOD) asm volatile("global_load_dwordx2 %0, %1, %2 offset:%3 " MOD : "=v"(dst) : "v"(voff), "s"(sbase), "i"(IMM) : "memory")
#define GLD1(dst, voff, sbase, IMM, MOD) asm volatile("global_load_dword %0, %1, %2 offset:%3 " MOD : "=v"(dst) : "v"(voff), "s"(sbase), "i"(IMM) : "memory")
#define GST4(src, voff, sbase, MOD) asm volatile("global_store_dwordx4 %0, %1, %2 " MOD "\n\ts_nop 1" :: "v"(voff), "v"(src), "s"(sbase) : "memory")
#define GST2(src, voff, sbase, MOD) asm volatile("global_store_dwordx2 %0, %1, %2 " MOD :: "v"(voff), "v"(src), "s"(sbase) : "memory")
#define SCHED() __builtin_amdgcn_sched_barrier(0)
#define VMWAIT(N) do { asm volatile("s_waitcnt vmcnt(%0)" :: "i"(N) : "memory"); SCHED(); } while (0)
#define LGWAIT() do { asm volatile("s_waitcnt lgkmcnt(0)" ::: "memory"); SCHED(); } while (0)
#define TIE(x) asm volatile("" : "+v"(x))
DI void dma16(unsigned voff, const void* sbase, unsigned ldsdst) {
  asm volatile("s_mov_b32 m0, %2\n\ts_nop 0\n\tglobal_load_lds_dwordx4 %0, %1" :: "v"(voff), "s"(sbase), "s"(ldsdst) : "memory", "m0");
}
#define BARRIER() do { asm volatile("s_waitcnt lgkmcnt(0)\n\ts_barrier" ::: "memory"); SCHED(); } while (0)
#define RAWBAR() do { asm volatile("s_barrier" ::: "memory"); SCHED(); } while (0)

#define CODEPF_DONE() asm volatile("" :: "v"(codepf))
#define STAMP(i) do {} while (0)
#define NPRE 4
#define MLP1 0
#define NPRE2 10

__global__ void __launch_bounds__(512, 2)
gm_kernel(const char* __restrict__ x, const char* __restrict__ ckv, const char* __restrict__ wq, const char* __restrict__ wuk,
          const char* __restrict__ wuv, const char* __restrict__ wo, const char* __restrict__ w1, const char* __restrict__ w2,
          const char* __restrict__ gw, char* __restrict__ out, char* __restrict__ ws) {
  __shared__ __attribute__((aligned(16))) char smem[L_END];
  asm volatile("" :: "v"((LAS char*)smem));
  STAMP(0);
  const int tid = threadIdx.x, lane = tid & 63;
  const int w = __builtin_amdgcn_readfirstlane(tid >> 6);
  const int b = blockIdx.x, xcd = b & 7, r = b >> 3;
  v4u codepf;
  {
    u64 pc; unsigned csz;
    asm volatile("s_getpc_b64 %0\n.Lgm_pc_here:\n\ts_mov_b32 %1, .Lgm_code_end-.Lgm_pc_here" : "=s"(pc), "=s"(csz));
    unsigned co = (unsigned)(r * 1024 + lane * 16);
    if (w == 7 && co + 16u <= csz) GLD4(codepf, co, (const char*)pc, 0, "");
  }
  const bool roleA = r < 16;
  const int g4 = lane >> 4, n16 = lane & 15;
  const int rr = r & 15;
  const int hq = xcd * 4 + (rr >> 2), j8 = (roleA ? 0 : 4) + (rr & 3);
  asm volatile("" :: "s"(x), "s"(ckv), "s"(wq), "s"(wuk), "s"(wuv), "s"(wo), "s"(w1), "s"(w2), "s"(gw), "s"(out), "s"(ws));
  STAMP(14);
  unsigned msk[4];
#pragma unroll
  for (int t = 0; t < 4; ++t) msk[t] = (((n16 & 7) >> 1) == t) ? ((n16 & 1) ? 0xFFFF0000u : 0x0000FFFFu) : 0u;

#define B_CLEARS(G_, S_, BI_) \
    { v4u s_ = {SENT, SENT, SENT, SENT}; \
      _Pragma("unroll") for (int i = 0; i < 3; ++i) { const int hh = 3 * (w - 5) + i; if (hh < 8) GST4(s_, (unsigned)(OFF_PP + ((8 * (G_) + hh) * 32 + (S_)) * 1024 + lane * 16), ws, "sc1"); } \
      if (w == 7) { \
        GST4(s_, (unsigned)(OFF_MP + (BI_) * 1280 + lane * 16), ws, "sc1"); \
        unsigned vo = (unsigned)(OFF_MP + (BI_) * 1280 + 1024 + lane * 16); \
        if (lane >= 16) vo = (unsigned)(OFF_ST + ((G_) * 32 + (S_)) * 64 + (lane - 16) * 16); \
        if (lane < 20) GST4(s_, vo, ws, "sc1"); } }
#define CLR_EARLY 2
  const int sB = xcd * 4 + (rr >> 2), gB = rr & 3;
  v4u wqr[4]; v2u xr, ew;
  STAMP(15);
  if (w == 0) {
    GLD2(ew, 0u, ws, OFF_E, "sc1");
    v4u s_ = {SENT, SENT, SENT, SENT};
    unsigned vo = (unsigned)(OFF_QP + (hq * 8 + j8) * 256 + n16 * 16);
    if (roleA && g4 == 1) vo = (unsigned)(OFF_QA + hq * 1024 + j8 * 256 + n16 * 16);
    if (lane < (roleA ? 32 : 16)) GST4(s_, vo, ws, "sc1");
  }
  if (w < 5) {
    const char* wb = wq + (size_t)(j8 * 80 + 16 * w) * 8192 + hq * 256;
    const unsigned vo = (unsigned)(4 * g4 * 8192 + n16 * 16);
    GLD2(xr, (unsigned)(8 * g4), x + (j8 * 80 + 16 * w) * 2, 0, "");
    GLD4(wqr[0], vo, wb, 0, ""); GLD4(wqr[1], vo, wb + 8192, 0, ""); GLD4(wqr[2], vo, wb + 16384, 0, ""); GLD4(wqr[3], vo, wb + 24576, 0, "");
  }
  if (CLR_EARLY == 2 && !roleA && w >= 5) B_CLEARS(gB, sB, xcd * 16 + rr);
  STAMP(11);
  RAWBAR();
  STAMP(1);
  const int kt = (w + 2 * gB) & 7;
  const char* kb = ckv + (size_t)(128 * sB + 16 * kt) * 1024;
  const unsigned kl = L_CKV + (16 * kt) * 1024;
#define CKV_ROWS(I0, I1) \
  _Pragma("unroll") for (int i = I0; i < I1; ++i) dma16((unsigned)((lane ^ i) * 16), kb + i * 1024, kl + i * 1024);
  if (roleA) {
    const char* ub = wuk + (size_t)(128 * j8 + 16 * w) * 8192 + hq * 256;
#pragma unroll
    for (int i = 0; i < 4; ++i)
      dma16((unsigned)((4 * i + g4) * 8192 + ((n16 ^ (4 * i + g4)) * 16)), ub, (unsigned)(L_UK + (16 * w + 4 * i) * 256));
  } else if (w >= 5) {
    if (CLR_EARLY == 1) B_CLEARS(gB, sB, xcd * 16 + rr);
    CKV_ROWS(0, NPRE2);
  }
  if (w == 0) {
    if (roleA) VMWAIT(9); else VMWAIT(5);
    TIE(ew);
    const u64 e1 = (((u64)ew.y << 32) | ew.x) + 1;
    v2u f = {(unsigned)e1, (unsigned)(e1 >> 32)};
    if (lane == 0) GST2(f, (unsigned)(OFF_FLAG1 + b * 128), ws, "sc1");
    if (lane == 0) { sts2(L_EP, f); sts1(L_OK, 0u); sts1(L_CNT, 0u); }
  }
  if (w < 5) {
    if (w == 0) { if (roleA) VMWAIT(9); else VMWAIT(5); } else { if (roleA) VMWAIT(8); else VMWAIT(4); }
    TIE(xr);
    const unsigned d0 = __builtin_amdgcn_perm(xr.x, xr.x, 0x01000100u), d1 = __builtin_amdgcn_perm(xr.x, xr.x, 0x03020302u);
    const unsigned d2 = __builtin_amdgcn_perm(xr.y, xr.y, 0x01000100u), d3 = __builtin_amdgcn_perm(xr.y, xr.y, 0x03020302u);
    v4u a0 = {d0 & msk[0], d0 & msk[1], d0 & msk[2], d0 & msk[3]}, a1 = {d1 & msk[0], d1 & msk[1], d1 & msk[2], d1 & msk[3]};
    v4u a2 = {d2 & msk[0], d2 & msk[1], d2 & msk[2], d2 & msk[3]}, a3 = {d3 & msk[0], d3 & msk[1], d3 & msk[2], d3 & msk[3]};
    unsigned qr = L_QRED + w * 512 + (8 * n16 + 4 * g4) * 4;
    TIE(a0); TIE(a1); TIE(a2); TIE(a3); TIE(qr);
    if (w == 0) { if (roleA) VMWAIT(5); else VMWAIT(1); } else { if (roleA) VMWAIT(4); else VMWAIT(0); }
    TIE(wqr[0]); TIE(wqr[1]); TIE(wqr[2]); TIE(wqr[3]);
    STAMP(2);
    f4 acc = {0.f, 0.f, 0.f, 0.f};
    acc = mfma(a0, wqr[0], acc); acc = mfma(a1, wqr[1], acc); acc = mfma(a2, wqr[2], acc); acc = mfma(a3, wqr[3], acc);
    if (lane < 32) stsf4(qr, acc);
  }
  BARRIER();
  if (w == 0) {
    if (lane < 32) {
      const unsigned ra = L_QRED + lane * 16;
      f4 s0 = ldsf4(ra), s1 = ldsf4(ra + 512), s2 = ldsf4(ra + 1024), s3 = ldsf4(ra + 1536), s4_ = ldsf4(ra + 2048);
      f4 sm = (s0 + s1) + (s2 + s3) + s4_;
      v2u o = {guard(cvtpk(sm.x, sm.y)), guard(cvtpk(sm.z, sm.w))};
      GST2(o, (unsigned)(OFF_QP + (hq * 8 + j8) * 256 + lane * 8), ws, "sc1");
    }
    STAMP(3);
  }
#define EPOCH() ({ v2u e_ = *(LAS v2u*)(size_t)L_EP; (((u64)(unsigned)__builtin_amdgcn_readfirstlane(e_.y)) << 32) | (unsigned)__builtin_amdgcn_readfirstlane(e_.x); })
#define FLAG1_CHECK(BLK) \
  for (unsigned spin = 0;; ++spin) { \
    v2u f0; \
    GLD2(f0, (unsigned)(OFF_FLAG1 + (BLK) * 128), ws, 0, "sc1"); \
    VMWAIT(0); TIE(f0); \
    const unsigned df = (f0.x ^ (unsigned)epoch) | (f0.y ^ (unsigned)(epoch >> 32)); \
    if (__builtin_amdgcn_ballot_w64(df != 0u) == 0ull || spin > SPIN_CAP) break; \
  }
#define FLAG2_CHECK(HALF) \
  for (unsigned spin = 0;; ++spin) { \
    v4u f0; \
    GLD4(f0, (unsigned)(OFF_FLAG2 + (HALF) * 1024 + lane * 16), ws, 0, "sc1"); \
    VMWAIT(0); TIE(f0); \
    const unsigned lo = (unsigned)epoch, hi = (unsigned)(epoch >> 32); \
    const unsigned df = ((f0.x ^ lo) | (f0.y ^ hi)) | ((f0.z ^ lo) | (f0.w ^ hi)); \
    if (__builtin_amdgcn_ballot_w64(df != 0u) == 0ull || spin > SPIN_CAP) break; \
  }
  const u64 epoch = EPOCH();

  if (roleA) {
    const int h = hq, j = j8;
    if (w == 7) { const int j8l = lane & 7; const int blk = (h >> 2) + 8 * ((j8l >> 2) * 16 + (h & 3) * 4 + (j8l & 3)); FLAG1_CHECK(blk); CODEPF_DONE(); STAMP(3); }
    else VMWAIT(0);
    STAMP(12);
    v4u uk[4];
#pragma unroll
    for (int ks = 0; ks < 4; ++ks) uk[ks] = lds4(L_UK + (16 * w + n16) * 256 + (((4 * ks + g4) ^ n16) * 16));
    unsigned qb_a = L_QB + (n16 & 7) * 288 + g4 * 16;
    unsigned qa_o = (unsigned)(OFF_QA + h * 1024 + (128 * j + 16 * w + 4 * g4) * 2);
    TIE(qb_a); TIE(qa_o);
    if (w == 7) {
      v4u p0, p1;
      const unsigned vo = (unsigned)(OFF_QP + h * 2048 + lane * 16);
      unsigned wa = L_QB + g4 * 288 + n16 * 16; TIE(wa);
      for (unsigned spin = 0;; ++spin) {
        GLD4(p0, vo, ws, 0, "sc1"); GLD4(p1, vo, ws, 1024, "sc1");
        VMWAIT(0); TIE(p0); TIE(p1);
        if (allvalid(umax(mx4(p0), mx4(p1))) || spin > SPIN_CAP) break;
      }
      STAMP(4);
      sts4(wa, p0); sts4(wa + 4 * 288, p1);
    }
    BARRIER();
    {
      v4u bq[4];
#pragma unroll
      for (int ks = 0; ks < 4; ++ks) bq[ks] = lds4(qb_a + ks * 64);
      SCHED();
      f4 acc = {0.f, 0.f, 0.f, 0.f};
#pragma unroll
      for (int ks = 0; ks < 4; ++ks) acc = mfma(uk[ks], bq[ks], acc);
      float v0 = acc.x, v1 = acc.y, v2 = acc.z, v3 = acc.w;
      v0 += dpp<XOR1>(v0); v1 += dpp<XOR1>(v1); v2 += dpp<XOR1>(v2); v3 += dpp<XOR1>(v3);
      v0 += dpp<XOR2>(v0); v1 += dpp<XOR2>(v1); v2 += dpp<XOR2>(v2); v3 += dpp<XOR2>(v3);
      v0 += dpp<HMIR>(v0); v1 += dpp<HMIR>(v1); v2 += dpp<HMIR>(v2); v3 += dpp<HMIR>(v3);
      v2u o = {guard(cvtpk(v0, v1)), guard(cvtpk(v2, v3))};
      if (n16 == 0) GST2(o, qa_o, ws, "sc1");
    }
    STAMP(5);
    RAWBAR();
    const int ca = j & 1, db = j >> 1;
    {
      const char* vb = wuv + (size_t)(256 * ca + 32 * w) * 8192 + h * 256 + db * 128;
#pragma unroll
      for (int i = 0; i < 4; ++i)
        dma16((unsigned)((8 * i + (lane >> 3)) * 8192 + (((lane & 7) ^ ((((lane >> 4) & 1) << 1) | ((i & 1) << 2))) * 16)), vb, (unsigned)(L_WUV + (32 * w + 8 * i) * 128));
    }
    if (w == 6) {
      v4u s_ = {SENT, SENT, SENT, SENT};
      GST4(s_, (unsigned)(OFF_OP + (h * 4 + j) * 1280 + lane * 16), ws, "sc1");
      if (lane < 16) GST4(s_, (unsigned)(OFF_OP + (h * 4 + j) * 1280 + 1024 + lane * 16), ws, "sc1");
    }
    {
      unsigned ovo[5];
#pragma unroll
      for (int i = 0; i < 5; ++i) {
        const unsigned n = 64u * (unsigned)(w + 8 * i) + (unsigned)lane, row = (n * 52429u) >> 22, pos = n - row * 80u;
        ovo[i] = row * 1280u + ((pos ^ (((row & 3u) << 1) | (((row >> 3) & 1u) << 3))) * 16u);
      }
      const char* ob = wo + (size_t)(h * 128 + 64 * db) * 1280;
      RAWBAR();
#pragma unroll
      for (int i = 0; i < 5; ++i) dma16(ovo[i], ob + ca * 40960, (unsigned)(L_WO + ca * 40960 + (w + 8 * i) * 1024));
      RAWBAR();
#pragma unroll
      for (int i = 0; i < 5; ++i) dma16(ovo[i], ob + (1 - ca) * 40960, (unsigned)(L_WO + (1 - ca) * 40960 + (w + 8 * i) * 1024));
    }
    if (lane < 8) sts4(L_TAB + w * 512 + 256 + lane * 16, v4u{0u, 0u, 0u, 0u});
    unsigned pvo = (unsigned)(OFF_PP + (h * 32 + n16) * 1024 + 512 * ca + 64 * w + 16 * g4), pvo2 = pvo + 16384u;
    unsigned sto = (unsigned)(OFF_ST + ((h >> 3) * 32 + (lane & 31)) * 64 + (h & 7) * 8);
    unsigned crw = L_CRED + lane * 32 + w * 4, crr = L_CRED + lane * 32;
    unsigned tabh = L_TAB + w * 512 + lane * 2;
    unsigned opo = (unsigned)(OFF_OP + (h * 4 + j) * 1280 + 160 * w + 32 * n16 + 8 * g4);
    unsigned bva[4], woa[5], bta[5];
    {
      const int q = (lane & 15) >> 2, p = lane & 3;
#pragma unroll
      for (int dt = 0; dt < 4; ++dt)
        bva[dt] = L_WUV + (32 * w + 8 * g4 + q) * 128 + (((2 * dt + (p >> 1)) ^ (((q >> 1) << 1) | ((g4 & 1) << 2))) * 16) + 8 * (p & 1);
#pragma unroll
      for (int t = 0; t < 5; ++t) {
        woa[t] = L_WO + (8 * g4 + q) * 1280 + (((10 * w + 2 * t + (p >> 1)) ^ ((q << 1) | ((g4 & 1) << 3))) * 16) + 8 * (p & 1);
        bta[t] = L_TAB + w * 512 + ((n16 & 7) == t ? (n16 >> 3) * 128 : 256) + g4 * 16;
      }
    }
    TIE(pvo); TIE(pvo2); TIE(sto); TIE(crw); TIE(crr); TIE(tabh); TIE(opo);
#pragma unroll
    for (int t = 0; t < 5; ++t) { TIE(woa[t]); TIE(bta[t]); }
    if (w == 6) VMWAIT(12); else VMWAIT(10);
    STAMP(13);
    v4u bv[4];
#pragma unroll
    for (int dt = 0; dt < 4; ++dt) bv[dt] = ldstr(bva[dt], bva[dt] + 512);
    if (w == 7) { FLAG2_CHECK(1); }
    VMWAIT(0);
    if (w == 6 && lane == 0) { v2u f = {(unsigned)epoch, (unsigned)(epoch >> 32)}; GST2(f, (unsigned)(OFF_FLAG2 + b * 8), ws, "sc1"); }
    STAMP(6);
    BARRIER();
    v4u wa[5][2];
#pragma unroll
    for (int t = 0; t < 5; ++t) { wa[t][0] = ldstr(woa[t], woa[t] + 5120); wa[t][1] = ldstr(woa[t] + 40960, woa[t] + 46080); }
    SCHED();
    if (w == 0) {
      v2u st;
      for (unsigned spin = 0;; ++spin) {
        GLD2(st, sto, ws, 0, "sc1");
        VMWAIT(0); TIE(st);
        if (allvalid(umax(st.x, st.y)) || spin > SPIN_CAP) break;
      }
      if (lane == 0) sts1(L_OK, 2u);
      float m = asf(st.x), l = asf(st.y);
      float M = p16_max(row_max(m));
      float a = __builtin_amdgcn_exp2f((m - M) * SC);
      float Ls = p16_sum(row_sum(a * l));
      a = a * __builtin_amdgcn_rcpf(Ls);
      if (lane < 32) sts1(L_AW + lane * 4, asu(a));
      LGWAIT();
      if (lane == 0) sts1(L_OK, 3u);
    } else {
      for (unsigned spin = 0; *(volatile LAS unsigned*)(size_t)L_OK < 2u && spin < 64u * SPIN_CAP; ++spin) __builtin_amdgcn_s_sleep(1);
    }
    v4u pa[2];
    GLD4(pa[0], pvo, ws, 0, "sc1"); GLD4(pa[1], pvo2, ws, 0, "sc1");
    STAMP(7);
    for (unsigned spin = 0; *(volatile LAS unsigned*)(size_t)L_OK < 3u && spin < 64u * SPIN_CAP; ++spin) __builtin_amdgcn_s_sleep(1);
    f4 aw[2];
    aw[0] = ldsf4(L_AW + g4 * 16); aw[1] = ldsf4(L_AW + 64 + g4 * 16);
    for (unsigned spin = 0;; ++spin) {
      VMWAIT(0); TIE(pa[0]); TIE(pa[1]);
      if (allvalid(umax(mx4(pa[0]), mx4(pa[1]))) || spin > SPIN_CAP) break;
      GLD4(pa[0], pvo, ws, 0, "sc1"); GLD4(pa[1], pvo2, ws, 0, "sc1");
    }
    STAMP(8);
    {
      const f4 z = {0.f, 0.f, 0.f, 0.f};
      float u[4];
#pragma unroll
      for (int dt = 0; dt < 4; ++dt) {
        f4 c = aw[0] * mfma(pa[0], bv[dt], z) + aw[1] * mfma(pa[1], bv[dt], z);
        u[dt] = (c.x + c.y) + (c.z + c.w);
      }
      auto t0 = __builtin_amdgcn_permlane16_swap(asu(u[0]), asu(u[1]), false, false); auto t1 = __builtin_amdgcn_permlane16_swap(asu(u[2]), asu(u[3]), false, false);
      unsigned x0 = t0[0], x1 = t0[1], y0 = t1[0], y1 = t1[1];
      auto t2 = __builtin_amdgcn_permlane32_swap(asu(asf(x0) + asf(x1)), asu(asf(y0) + asf(y1)), false, false);
      unsigned z0 = t2[0], z1 = t2[1];
      sts1(crw, asu(asf(z0) + asf(z1)));
    }
    BARRIER();
    STAMP(9);
    {
      float c = sum8(ldsf4(crr), ldsf4(crr + 16));
      unsigned hi = cvtpk(c, c);
      float rs = c - asf(hi << 16);
      unsigned lo = cvtpk(rs, rs);
      *(LAS unsigned short*)(size_t)tabh = (unsigned short)hi;
      *(LAS unsigned short*)(size_t)(tabh + 128) = (unsigned short)lo;
      LGWAIT();
      v4u bt[5][2];
#pragma unroll
      for (int t = 0; t < 5; ++t) { bt[t][0] = lds4(bta[t]); bt[t][1] = lds4(bta[t] + 64); }
      SCHED();
      f4 acc = {0.f, 0.f, 0.f, 0.f};
#pragma unroll
      for (int t = 0; t < 5; ++t) { acc = mfma(wa[t][0], bt[t][0], acc); acc = mfma(wa[t][1], bt[t][1], acc); }
      float o0 = acc.x + dpp<ROR8>(acc.x), o1 = acc.y + dpp<ROR8>(acc.y), o2 = acc.z + dpp<ROR8>(acc.z), o3 = acc.w + dpp<ROR8>(acc.w);
      unsigned a0 = guard(cvtpk(o0, o1)), a1 = guard(cvtpk(o2, o3));
      auto s0 = __builtin_amdgcn_permlane16_swap(a0, a0, false, false); auto s1 = __builtin_amdgcn_permlane16_swap(a1, a1, false, false);
      unsigned b0 = s0[1], b1 = s1[1];
      v4u o = {a0, a1, b0, b1};
      if (n16 < 5 && (g4 & 1) == 0) GST4(o, opo, ws, "sc1");
    }
    STAMP(10);
  } else {
    const int g = rr & 3, s = xcd * 4 + (rr >> 2), bi = xcd * 16 + rr;
    RAWBAR();
    if (w < 5) {
      CKV_ROWS(0, 16);
    } else {
      CKV_ROWS(NPRE2, 16);
      v2u f1;
      unsigned f1o;
      { const int hd = 8 * g + ((lane & 31) >> 2); f1o = (unsigned)(OFF_FLAG1 + ((hd >> 2) + 8 * ((hd & 3) * 4 + (lane & 3))) * 128); }
      if (w == 7) GLD2(f1, f1o, ws, 0, "sc1");
      if (w == 5) { dma16((unsigned)(lane * 16), x, L_QB); if (lane < 16) dma16((unsigned)(lane * 16), x + 1024, L_QB + 1024); }
      if (w == 5) VMWAIT(18); else if (w == 6) VMWAIT(16); else VMWAIT(17);
      unsigned old = 0;
      if (lane == 0) old = __hip_atomic_fetch_add((LAS unsigned*)(size_t)L_CNT, 1u, __ATOMIC_RELAXED, __HIP_MEMORY_SCOPE_WORKGROUP);
      if (lane == 0 && old == 2u) { v2u f = {(unsigned)epoch, (unsigned)(epoch >> 32)}; GST2(f, (unsigned)(OFF_FLAG2 + b * 8), ws, "sc1"); }
      if (w == 7) {
        VMWAIT(0); TIE(f1);
        for (unsigned spin = 0; spin < SPIN_CAP; ++spin) {
          const unsigned df = (f1.x ^ (unsigned)epoch) | (f1.y ^ (unsigned)(epoch >> 32));
          if (__builtin_amdgcn_ballot_w64(df != 0u) == 0ull) break;
          GLD2(f1, f1o, ws, 0, "sc1");
          VMWAIT(0); TIE(f1);
        }
        CODEPF_DONE();
        if (lane == 0) sts1(L_OK, 1u);
      }
    }
    unsigned qvo = (unsigned)(OFF_QA + (8 * g + w) * 1024 + lane * 16);
    unsigned qsw = L_QAS + w * 1056 + lane * 16, qsr = L_QAS + (n16 & 7) * 1056 + g4 * 16;
    unsigned mxw = L_MX + lane * 32 + w * 4, mxr = L_MX + (n16 & 7) * 32;
    unsigned pw = L_P + n16 * 288 + (16 * kt + 4 * g4) * 2, pr = L_P + (n16 & 7) * 288 + g4 * 16;
    unsigned lsw = L_LS + lane * 32 + w * 4;
    unsigned ppo = (unsigned)(OFF_PP + ((8 * g + (n16 & 7)) * 32 + s) * 1024 + (64 * w + 32 * (g4 >> 1) + 8 * (2 * (n16 >> 3) + (g4 & 1))) * 2);
    unsigned ka[4], ta[4][2];
    {
      const unsigned kr = L_CKV + (16 * kt + n16) * 1024;
#pragma unroll
      for (int u = 0; u < 4; ++u) ka[u] = kr + (((4 * u + g4) ^ n16) * 16);
      const int q = (lane & 15) >> 2, p = lane & 3;
#pragma unroll
      for (int t = 0; t < 4; ++t) {
        const int ch = 8 * w + 4 * (p >> 1) + t;
        const int r0 = 8 * g4 + q, r1 = r0 + 4;
        ta[t][0] = L_CKV + r0 * 1024 + ((ch ^ (r0 & 15)) * 16) + 8 * (p & 1);
        ta[t][1] = L_CKV + r1 * 1024 + ((ch ^ (r1 & 15)) * 16) + 8 * (p & 1);
      }
    }
    TIE(qvo); TIE(qsw); TIE(qsr); TIE(mxw); TIE(mxr); TIE(pw); TIE(pr); TIE(lsw); TIE(ppo);
#pragma unroll
    for (int u = 0; u < 4; ++u) { TIE(ka[u]); TIE(ta[u][0]); TIE(ta[u][1]); }
    VMWAIT(0);
    STAMP(4);
    for (unsigned spin = 0; *(volatile LAS unsigned*)(size_t)L_OK == 0u && spin < SPIN_CAP; ++spin) __builtin_amdgcn_s_sleep(1);
    v4u qa;
    GLD4(qa, qvo, ws, 0, "sc1");
    v4u kA[16];
#pragma unroll
    for (int ks = 0; ks < 16; ++ks) kA[ks] = lds4(ka[ks & 3] + (ks >> 2) * 256);
    BARRIER();
    STAMP(5);
    v4u vA[4][4];
#pragma unroll
    for (int t = 0; t < 4; ++t) {
      vA[0][t] = ldstr(ta[t][0], ta[t][1]); vA[1][t] = ldstr(ta[t][0] + 32768, ta[t][1] + 32768);
      const unsigned b0 = ta[t][0] + 65536, b1 = ta[t][1] + 65536;
      vA[2][t] = ldstr(b0, b1); vA[3][t] = ldstr(b0 + 32768, b1 + 32768);
    }
    SCHED();
    VMWAIT(0); TIE(qa);
    if (__builtin_expect(!allvalid(mx4(qa)), 0)) {
      v4u qb;
      for (unsigned spin = 0;; ++spin) {
        GLD4(qb, qvo, ws, 0, "sc1");
        VMWAIT(0); TIE(qb);
        if (allvalid(mx4(qb)) || spin > SPIN_CAP) break;
      }
      qa = qb;
    }
    STAMP(6);
    sts4(qsw, qa);
    BARRIER();
    const int fb = bi < 108 ? bi : 107;
    const char* b1 = w1 + fb * 32;
    const unsigned vo1 = (unsigned)((lane >> 1) * 3456 + (lane & 1) * 16);
    const int pq = 5 * (bi & 3);
    f4 sc = {0.f, 0.f, 0.f, 0.f};
    {
      v4u bq[16];
#pragma unroll
      for (int ks = 0; ks < 16; ++ks) bq[ks] = lds4(qsr + ks * 64);
      const char* b2 = w2 + (size_t)(16 * fb) * 1280 + w * 1024;
      const unsigned vo2 = (unsigned)(lane * 16);
      const int p0 = pq + w;
      SCHED();
#pragma unroll
      for (int ks = 0; ks < 16; ++ks) {
        sc = mfma(kA[ks], bq[ks], sc);
        if (MLP1 && ks == 2) { SCHED(); if (w < 5) dma16(vo1, b1 + (size_t)(32 * p0) * 3456, L_W1 + p0 * 1024); SCHED(); }
        if (MLP1 && ks == 5) { SCHED(); dma16(vo2, b2, L_W2 + w * 1024); SCHED(); }
        if (MLP1 && ks == 8) { SCHED(); dma16(vo2, b2 + 8192, L_W2 + (w + 8) * 1024); SCHED(); }
        if (MLP1 && ks == 11) { SCHED(); if (w < 4) dma16(vo2, b2 + 16384, L_W2 + (w + 16) * 1024); SCHED(); }
      }
      float mw = p32_max(p16_max(fmx(fmx(sc.x, sc.y), fmx(sc.z, sc.w))));
      if (lane < 8) sts1(mxw, asu(mw));
    }
    BARRIER();
    STAMP(7);
    {
      const float Ms = -max8(ldsf4(mxr), ldsf4(mxr + 16)) * SC;
      float p0 = __builtin_amdgcn_exp2f(fmaf(sc.x, SC, Ms)), p1 = __builtin_amdgcn_exp2f(fmaf(sc.y, SC, Ms)), p2 = __builtin_amdgcn_exp2f(fmaf(sc.z, SC, Ms)), p3 = __builtin_amdgcn_exp2f(fmaf(sc.w, SC, Ms));
      v2u pk = {cvtpk(p0, p1), cvtpk(p2, p3)};
      if (n16 < 8) sts2(pw, pk);
      float ls = p32_sum(p16_sum((p0 + p1) + (p2 + p3)));
      if (lane < 8) sts1(lsw, asu(ls));
    }
    BARRIER();
    STAMP(8);
    {
      v4u pb[4];
#pragma unroll
      for (int ks = 0; ks < 4; ++ks) pb[ks] = lds4(pr + ks * 64);
      f4 lu, lv, mu, mv;
      if (w == 7) { const unsigned sa = (lane & 7) * 32; lu = ldsf4(L_LS + sa); lv = ldsf4(L_LS + sa + 16); mu = ldsf4(L_MX + sa); mv = ldsf4(L_MX + sa + 16); }
      SCHED();
      f4 ac[4];
#pragma unroll
      for (int t = 0; t < 4; ++t) ac[t] = mfma(vA[0][t], pb[0], f4{0.f, 0.f, 0.f, 0.f});
      if (w == 7) {
        v2u o = {guard(asu(max8(mu, mv))), guard(asu(sum8(lu, lv)))};
        if (lane < 8) GST2(o, (unsigned)(OFF_ST + (g * 32 + s) * 64 + lane * 8), ws, "sc1");
      }
#pragma unroll
      for (int ks = 1; ks < 4; ++ks)
#pragma unroll
        for (int t = 0; t < 4; ++t) ac[t] = mfma(vA[ks][t], pb[ks], ac[t]);
      const bool up = n16 >= 8;
      f4 xa = up ? ac[2] : ac[0], ya = up ? ac[3] : ac[1];
      unsigned x0 = cvtpk(xa.x, xa.y), x1 = cvtpk(xa.z, xa.w), y0 = cvtpk(ya.x, ya.y), y1 = cvtpk(ya.z, ya.w);
      auto s0 = __builtin_amdgcn_permlane16_swap(x0, y0, false, false); auto s1 = __builtin_amdgcn_permlane16_swap(x1, y1, false, false);
      unsigned e0 = s0[0], e1 = s1[0], f0 = s0[1], f1 = s1[1];
      v4u o = {guard(e0), guard(e1), guard(f0), guard(f1)};
      GST4(o, ppo, ws, "sc1");
    }
    STAMP(9);
    RAWBAR();
    int pc0 = pq + 5 + w, pc1 = pq + 13 + w;
    if (pc0 >= 20) pc0 -= 20;
    if (pc1 >= 20) pc1 -= 20;
    if (!MLP1) {
      const char* b2 = w2 + (size_t)(16 * fb) * 1280 + w * 1024;
      const unsigned vo2 = (unsigned)(lane * 16);
      if (w < 5) dma16(vo1, b1 + (size_t)(32 * (pq + w)) * 3456, L_W1 + (pq + w) * 1024);
      dma16(vo2, b2, L_W2 + w * 1024); dma16(vo2, b2 + 8192, L_W2 + (w + 8) * 1024); if (w < 4) dma16(vo2, b2 + 16384, L_W2 + (w + 16) * 1024);
      RAWBAR();
    }
    dma16(vo1, b1 + (size_t)(32 * pc0) * 3456, L_W1 + pc0 * 1024);
    if (w < 7) dma16(vo1, b1 + (size_t)(32 * pc1) * 3456, L_W1 + pc1 * 1024);
    VMWAIT(0);
    STAMP(12);
    {
      const int q = (lane & 15) >> 2, p = lane & 3;
      const unsigned wa = L_W1 + (8 * g4 + q) * 32 + 8 * p, xa = L_QB + g4 * 16;
      f4 hacc = {0.f, 0.f, 0.f, 0.f};
      hacc = mfma(lds4(xa + pc0 * 64), ldstr(wa + pc0 * 1024, wa + pc0 * 1024 + 128), hacc);
      if (w < 7) hacc = mfma(lds4(xa + pc1 * 64), ldstr(wa + pc1 * 1024, wa + pc1 * 1024 + 128), hacc);
      if (w < 5) { const int p0 = pq + w; hacc = mfma(lds4(xa + p0 * 64), ldstr(wa + p0 * 1024, wa + p0 * 1024 + 128), hacc); }
      if (lane < 16) sts1(L_RED + lane * 32 + w * 4, asu(hacc.x));
    }
    BARRIER();
    STAMP(15);
    if (w < 5) {
      v4u w2r[4];
#pragma unroll
      for (int i = 0; i < 4; ++i) w2r[i] = lds4(L_W2 + (4 * g4 + i) * 1280 + w * 256 + n16 * 16);
      const int f = lane & 15;
      float hh = sum8(ldsf4(L_RED + f * 32), ldsf4(L_RED + f * 32 + 16));
      hh = hh * __builtin_amdgcn_rcpf(1.f + __expf(-hh));
      if (bi >= 108) hh = 0.f;
      unsigned hi = cvtpk(hh, hh);
      float rs = hh - asf(hi << 16);
      unsigned lo = cvtpk(rs, rs);
      if (lane < 32) sts1(L_TAB + w * 512 + lane * 4, lane < 16 ? hi : lo);
      LGWAIT();
      v4u t0 = lds4(L_TAB + w * 512 + ((n16 >> 3) * 16 + 4 * g4) * 4);
      unsigned dv[4] = {t0.x, t0.y, t0.z, t0.w};
      f4 acc = {0.f, 0.f, 0.f, 0.f};
#pragma unroll
      for (int i = 0; i < 4; ++i) { v4u a = {dv[i] & msk[0], dv[i] & msk[1], dv[i] & msk[2], dv[i] & msk[3]}; acc = mfma(a, w2r[i], acc); }
      float o0 = p32_sum(acc.x), o1 = p32_sum(acc.y), o2 = p32_sum(acc.z), o3 = p32_sum(acc.w);
      unsigned a0 = guard(cvtpk(o0, o1)), a1 = guard(cvtpk(o2, o3));
      auto s0 = __builtin_amdgcn_permlane16_swap(a0, a0, false, false); auto s1 = __builtin_amdgcn_permlane16_swap(a1, a1, false, false);
      unsigned b0 = s0[1], b1 = s1[1];
      v4u o = {a0, a1, b0, b1};
      if (lane < 16) GST4(o, (unsigned)(OFF_MP + bi * 1280 + w * 256 + lane * 16), ws, "sc1");
    }
    STAMP(10);
    if (bi < 80 && w == 5) {
      FLAG2_CHECK(0);
      FLAG2_CHECK(1);
      unsigned gg[5];
#pragma unroll
      for (int i = 0; i < 5; ++i) GLD1(gg[i], (unsigned)(lane * 4), gw, i * 256, "");
      VMWAIT(0);
#pragma unroll
      for (int i = 0; i < 5; ++i) TIE(gg[i]);
      float gate;
      {
        float a = 0.f;
#pragma unroll
        for (int i = 0; i < 5; ++i) { const unsigned gx = *(LAS unsigned*)(size_t)(L_QB + i * 256 + lane * 4); a += asf(gx << 16) * asf(gg[i] << 16) + asf(gx & 0xFFFF0000u) * asf(gg[i] & 0xFFFF0000u); }
        a = p32_sum(p16_sum(row_sum(a)));
        gate = __builtin_amdgcn_rcpf(1.f + __expf(-a));
      }
      const unsigned one = 0x3F803F80u;
      v4u ones = {one & msk[0], one & msk[1], one & msk[2], one & msk[3]};
      v4u m0, m1;
      const unsigned mvo = (unsigned)(OFF_MP + lane * 1280 + bi * 16), mvo2 = mvo + 64 * 1280;
      for (unsigned spin = 0;; ++spin) {
        GLD4(m0, mvo, ws, 0, "sc1"); GLD4(m1, mvo2, ws, 0, "sc1");
        VMWAIT(0); TIE(m0); TIE(m1);
        if (allvalid(umax(mx4(m0), mx4(m1))) || spin > SPIN_CAP) break;
      }
      f4 bb = {0.f, 0.f, 0.f, 0.f};
      bb = mfma(ones, m0, bb); bb = mfma(ones, m1, bb);
      bb *= (1.f - gate);
      STAMP(11);
      const unsigned ovo = (unsigned)(OFF_OP + lane * 1280 + bi * 16), ovo2 = ovo + 64 * 1280;
      for (unsigned spin = 0;; ++spin) {
        GLD4(m0, ovo, ws, 0, "sc1"); GLD4(m1, ovo2, ws, 0, "sc1");
        VMWAIT(0); TIE(m0); TIE(m1);
        if (allvalid(umax(mx4(m0), mx4(m1))) || spin > SPIN_CAP) break;
      }
      STAMP(12);
      f4 aa = {0.f, 0.f, 0.f, 0.f};
      aa = mfma(ones, m0, aa); aa = mfma(ones, m1, aa);
      float r0 = row_sum(fmaf(gate, aa.x, bb.x)), r1 = row_sum(fmaf(gate, aa.y, bb.y));
      float r2 = row_sum(fmaf(gate, aa.z, bb.z)), r3 = row_sum(fmaf(gate, aa.w, bb.w));
      v2u o = {cvtpk(r0, r1), cvtpk(r2, r3)};
      if (n16 == 0 && g4 < 2) GST2(o, (unsigned)(bi * 16 + g4 * 8), out, "");
      if (bi == 0 && lane == 0) {
        v2u f = {(unsigned)epoch, (unsigned)(epoch >> 32)};
        GST2(f, (unsigned)OFF_E, ws, "sc1");
      }
      STAMP(13);
    }
  }
  asm volatile(".Lgm_code_end:" ::: "memory");
}

extern "C" void kernel_launch(void* const* d_in, const int* in_sizes, int n_in,
                              void* d_out, int out_size, void* d_ws, size_t ws_size,
                              hipStream_t stream) {
  gm_kernel<<<256, 512, 0, stream>>>((const char*)d_in[0], (const char*)d_in[1], (const char*)d_in[2], (const char*)d_in[3],
                                      (const char*)d_in[4], (const char*)d_in[5], (const char*)d_in[7], (const char*)d_in[8],
                                      (const char*)d_in[9], (char*)d_out, (char*)d_ws);
}
```

```cpp
#define STAMPS 1
#include <hip/hip_runtime.h>
#include <stdint.h>
#pragma clang diagnostic ignored "-Winline-asm"

typedef unsigned v4u __attribute__((ext_vector_type(4)));
typedef unsigned v2u __attribute__((ext_vector_type(2)));
typedef float f4 __attribute__((ext_vector_type(4)));
typedef float f2 __attribute__((ext_vector_type(2)));
typedef short s4 __attribute__((ext_vector_type(4)));
typedef short s8 __attribute__((ext_vector_type(8)));
typedef __bf16 bf2 __attribute__((ext_vector_type(2)));
typedef unsigned long long u64;
#define LAS __attribute__((address_space(3)))
#define DI __device__ __forceinline__

constexpr unsigned OFF_E = 0;
constexpr unsigned OFF_FLAG2 = 4096;
constexpr unsigned OFF_FLAG1 = 8192;
constexpr unsigned OFF_QP = OFF_FLAG1 + 32768;
constexpr unsigned OFF_QA = OFF_QP + 65536;
constexpr unsigned OFF_ST = OFF_QA + 32768;
constexpr unsigned OFF_OP = OFF_ST + 8192;
constexpr unsigned OFF_MP = OFF_OP + 163840;
constexpr unsigned OFF_PP = OFF_MP + 163840;
constexpr unsigned OFF_END = OFF_PP + 1048576;
constexpr unsigned OFF_STAMP = 8u << 20;
constexpr unsigned SENT = 0xFFFFFFFFu;
constexpr unsigned SPIN_CAP = 1u << 15;
constexpr float SC = 0.08838834764831845f * 1.4426950408889634f;

constexpr unsigned L_CKV = 0;
constexpr unsigned L_W1 = 0, L_W2 = 32768;
constexpr unsigned L_WUV = 0, L_WO = 32768;
constexpr unsigned L_AW = 131072 + 2048;
constexpr unsigned L_CRED = 131072;
constexpr unsigned L_UK = 32768;
constexpr unsigned L_QAS = 131072;
constexpr unsigned L_QRED = L_QAS;
constexpr unsigned L_P = L_QAS + 8448;
constexpr unsigned L_MX = L_P + 2304;
constexpr unsigned L_LS = L_MX + 256;
constexpr unsigned L_RED = L_LS + 256;
constexpr unsigned L_TAB = L_RED + 1024;
constexpr unsigned L_QB = L_TAB + 4096;
constexpr unsigned L_EP = L_QB + 2304;
constexpr unsigned L_OK = L_EP + 8;
constexpr unsigned L_CNT = L_EP + 12;
constexpr unsigned L_END = L_EP + 16;

DI f4 mfma(v4u a, v4u b, f4 c) { return __builtin_amdgcn_mfma_f32_16x16x32_bf16(__builtin_bit_cast(s8, a), __builtin_bit_cast(s8, b), c, 0, 0, 0); }
DI unsigned cvtpk(float lo, float hi) { f2 x = {lo, hi}; bf2 y = __builtin_convertvector(x, bf2); return __builtin_bit_cast(unsigned, y); }
DI float asf(unsigned u) { return __uint_as_float(u); }
DI unsigned asu(float f) { return __float_as_uint(f); }
DI unsigned guard(unsigned u) { return u < 0xFFFFFFFEu ? u : 0xFFFFFFFEu; }
DI float fmx(float a, float b) { return __builtin_elementwise_maximum(a, b); }
template <int CTRL> DI float dpp(float x) { return asf((unsigned)__builtin_amdgcn_mov_dpp((int)asu(x), CTRL, 0xf, 0xf, true)); }
constexpr int XOR1 = 0xB1, XOR2 = 0x4E, ROR4 = 0x124, ROR8 = 0x128, HMIR = 0x141;
DI float p16_max(float x) { auto t = __builtin_amdgcn_permlane16_swap(asu(x), asu(x), false, false); unsigned a = t[0], b = t[1]; return fmx(asf(a), asf(b)); }
DI float p32_max(float x) { auto t = __builtin_amdgcn_permlane32_swap(asu(x), asu(x), false, false); unsigned a = t[0], b = t[1]; return fmx(asf(a), asf(b)); }
DI float p16_sum(float x) { auto t = __builtin_amdgcn_permlane16_swap(asu(x), asu(x), false, false); unsigned a = t[0], b = t[1]; return asf(a) + asf(b); }
DI float p32_sum(float x) { auto t = __builtin_amdgcn_permlane32_swap(asu(x), asu(x), false, false); unsigned a = t[0], b = t[1]; return asf(a) + asf(b); }
DI float row_sum(float x) { x += dpp<XOR1>(x); x += dpp<XOR2>(x); x += dpp<ROR4>(x); x += dpp<ROR8>(x); return x; }
DI float row_max(float x) { x = fmx(x, dpp<XOR1>(x)); x = fmx(x, dpp<XOR2>(x)); x = fmx(x, dpp<ROR4>(x)); x = fmx(x, dpp<ROR8>(x)); return x; }
DI float max8(f4 u, f4 v) { return fmx(fmx(fmx(u.x, u.y), fmx(u.z, u.w)), fmx(fmx(v.x, v.y), fmx(v.z, v.w))); }
DI float sum8(f4 u, f4 v) { return ((u.x + u.y) + (u.z + u.w)) + ((v.x + v.y) + (v.z + v.w)); }
DI unsigned umax(unsigned a, unsigned b) { return a > b ? a : b; }
DI unsigned mx4(v4u v) { return umax(umax(v.x, v.y), umax(v.z, v.w)); }
DI bool allvalid(unsigned m) { return __builtin_amdgcn_ballot_w64(m == SENT) == 0ull; }
DI v4u lds4(unsigned a) { return *(LAS v4u*)(size_t)a; }
DI f4 ldsf4(unsigned a) { return *(LAS f4*)(size_t)a; }
DI void sts4(unsigned a, v4u v) { *(LAS v4u*)(size_t)a = v; }
DI void stsf4(unsigned a, f4 v) { *(LAS f4*)(size_t)a = v; }
DI void sts2(unsigned a, v2u v) { *(LAS v2u*)(size_t)a = v; }
DI void sts1(unsigned a, unsigned v) { *(LAS unsigned*)(size_t)a = v; }
DI v4u ldstr(unsigned a0, unsigned a1) {
  s4 lo = __builtin_amdgcn_ds_read_tr16_b64_v4i16((LAS s4*)(size_t)a0), hi = __builtin_amdgcn_ds_read_tr16_b64_v4i16((LAS s4*)(size_t)a1);
  s8 c = __builtin_shufflevector(lo, hi, 0, 1, 2, 3, 4, 5, 6, 7);
  return __builtin_bit_cast(v4u, c);
}

#define GLD4(dst, voff, sbase, IMM, MOD) asm volatile("global_load_dwordx4 %0, %1, %2 offset:%3 " MOD : "=v"(dst) : "v"(voff), "s"(sbase), "i"(IMM) : "memory")
#define GLD2(dst, voff, sbase, IMM, MOD) asm volatile("global_load_dwordx2 %0, %1, %2 offset:%3 " MOD : "=v"(dst) : "v"(voff), "s"(sbase), "i"(IMM) : "memory")
#define GLD1(dst, voff, sbase, IMM, MOD) asm volatile("global_load_dword %0, %1, %2 offset:%3 " MOD : "=v"(dst) : "v"(voff), "s"(sbase), "i"(IMM) : "memory")
#define GST4(src, voff, sbase, MOD) asm volatile("global_store_dwordx4 %0, %1, %2 " MOD "\n\ts_nop 1" :: "v"(voff), "v"(src), "s"(sbase) : "memory")
#define GST2(src, voff, sbase, MOD) asm volatile("global_store_dwordx2 %0, %1, %2 " MOD :: "v"(voff), "v"(src), "s"(sbase) : "memory")
#define SCHED() __builtin_amdgcn_sched_barrier(0)
#define VMWAIT(N) do { asm volatile("s_waitcnt vmcnt(%0)" :: "i"(N) : "memory"); SCHED(); } while (0)
#define LGWAIT() do { asm volatile("s_waitcnt lgkmcnt(0)" ::: "memory"); SCHED(); } while (0)
#define TIE(x) asm volatile("" : "+v"(x))
DI void dma16(unsigned voff, const void* sbase, unsigned ldsdst) {
  asm volatile("s_mov_b32 m0, %2\n\ts_nop 0\n\tglobal_load_lds_dwordx4 %0, %1" :: "v"(voff), "s"(sbase), "s"(ldsdst) : "memory", "m0");
}
#define BARRIER() do { asm volatile("s_waitcnt lgkmcnt(0)\n\ts_barrier" ::: "memory"); SCHED(); } while (0)
#define RAWBAR() do { asm volatile("s_barrier" ::: "memory"); SCHED(); } while (0)

#define CODEPF_DONE() asm volatile("" :: "v"(codepf))
#define STAMP(i) do { u64 t_; asm volatile("s_memrealtime %0\n\ts_waitcnt lgkmcnt(0)" : "=s"(t_) :: "memory"); const unsigned l_ = (unsigned)t_ | 1u; asm volatile("v_writelane_b32 %0, %1, %2" : "+v"(tsv) : "s"(l_), "i"(i)); } while (0)
#define NPRE 4
#define MLP1 0
#define NPRE2 10

__global__ void __launch_bounds__(512, 2)
gm_kernel(const char* __restrict__ x, const char* __restrict__ ckv, const char* __restrict__ wq, const char* __restrict__ wuk,
          const char* __restrict__ wuv, const char* __restrict__ wo, const char* __restrict__ w1, const char* __restrict__ w2,
          const char* __restrict__ gw, char* __restrict__ out, char* __restrict__ ws) {
  __shared__ __attribute__((aligned(16))) char smem[L_END];
  asm volatile("" :: "v"((LAS char*)smem));
  unsigned tsv = 0;
  STAMP(0);
  const int tid = threadIdx.x, lane = tid & 63;
  const int w = __builtin_amdgcn_readfirstlane(tid >> 6);
  const int b = blockIdx.x, xcd = b & 7, r = b >> 3;
  v4u codepf;
  {
    u64 pc; unsigned csz;
    asm volatile("s_getpc_b64 %0\n.Lgm_pc_here:\n\ts_mov_b32 %1, .Lgm_code_end-.Lgm_pc_here" : "=s"(pc), "=s"(csz));
    unsigned co = (unsigned)(r * 1024 + lane * 16);
    if (w == 7 && co + 16u <= csz) GLD4(codepf, co, (const char*)pc, 0, "");
  }
  const bool roleA = r < 16;
  const int g4 = lane >> 4, n16 = lane & 15;
  const int rr = r & 15;
  const int hq = xcd * 4 + (rr >> 2), j8 = (roleA ? 0 : 4) + (rr & 3);
  asm volatile("" :: "s"(x), "s"(ckv), "s"(wq), "s"(wuk), "s"(wuv), "s"(wo), "s"(w1), "s"(w2), "s"(gw), "s"(out), "s"(ws));
  STAMP(14);
  unsigned msk[4];
#pragma unroll
  for (int t = 0; t < 4; ++t) msk[t] = (((n16 & 7) >> 1) == t) ? ((n16 & 1) ? 0xFFFF0000u : 0x0000FFFFu) : 0u;

#define B_CLEARS(G_, S_, BI_) \
    { v4u s_ = {SENT, SENT, SENT, SENT}; \
      _Pragma("unroll") for (int i = 0; i < 3; ++i) { const int hh = 3 * (w - 5) + i; if (hh < 8) GST4(s_, (unsigned)(OFF_PP + ((8 * (G_) + hh) * 32 + (S_)) * 1024 + lane * 16), ws, "sc1"); } \
      if (w == 7) { \
        GST4(s_, (unsigned)(OFF_MP + (BI_) * 1280 + lane * 16), ws, "sc1"); \
        unsigned vo = (unsigned)(OFF_MP + (BI_) * 1280 + 1024 + lane * 16); \
        if (lane >= 16) vo = (unsigned)(OFF_ST + ((G_) * 32 + (S_)) * 64 + (lane - 16) * 16); \
        if (lane < 20) GST4(s_, vo, ws, "sc1"); } }
#define CLR_EARLY 2
  const int sB = xcd * 4 + (rr >> 2), gB = rr & 3;
  v4u wqr[4]; v2u xr, ew;
  STAMP(15);
  if (w == 0) {
    GLD2(ew, 0u, ws, OFF_E, "sc1");
    v4u s_ = {SENT, SENT, SENT, SENT};
    unsigned vo = (unsigned)(OFF_QP + (hq * 8 + j8) * 256 + n16 * 16);
    if (roleA && g4 == 1) vo = (unsigned)(OFF_QA + hq * 1024 + j8 * 256 + n16 * 16);
    if (lane < (roleA ? 32 : 16)) GST4(s_, vo, ws, "sc1");
  }
  if (w < 5) {
    const char* wb = wq + (size_t)(j8 * 80 + 16 * w) * 8192 + hq * 256;
    const unsigned vo = (unsigned)(4 * g4 * 8192 + n16 * 16);
    GLD2(xr, (unsigned)(8 * g4), x + (j8 * 80 + 16 * w) * 2, 0, "");
    GLD4(wqr[0], vo, wb, 0, ""); GLD4(wqr[1], vo, wb + 8192, 0, ""); GLD4(wqr[2], vo, wb + 16384, 0, ""); GLD4(wqr[3], vo, wb + 24576, 0, "");
  }
  if (CLR_EARLY == 2 && !roleA && w >= 5) B_CLEARS(gB, sB, xcd * 16 + rr);
  STAMP(11);
  RAWBAR();
  STAMP(1);
  const int kt = (w + 2 * gB) & 7;
  const char* kb = ckv + (size_t)(128 * sB + 16 * kt) * 1024;
  const unsigned kl = L_CKV + (16 * kt) * 1024;
#define CKV_ROWS(I0, I1) \
  _Pragma("unroll") for (int i = I0; i < I1; ++i) dma16((unsigned)((lane ^ i) * 16), kb + i * 1024, kl + i * 1024);
  if (roleA) {
    const char* ub = wuk + (size_t)(128 * j8 + 16 * w) * 8192 + hq * 256;
#pragma unroll
    for (int i = 0; i < 4; ++i)
      dma16((unsigned)((4 * i + g4) * 8192 + ((n16 ^ (4 * i + g4)) * 16)), ub, (unsigned)(L_UK + (16 * w + 4 * i) * 256));
  } else if (w >= 5) {
    if (CLR_EARLY == 1) B_CLEARS(gB, sB, xcd * 16 + rr);
    CKV_ROWS(0, NPRE2);
  }
  if (w == 0) {
    if (roleA) VMWAIT(9); else VMWAIT(5);
    TIE(ew);
    const u64 e1 = (((u64)ew.y << 32) | ew.x) + 1;
    v2u f = {(unsigned)e1, (unsigned)(e1 >> 32)};
    if (lane == 0) GST2(f, (unsigned)(OFF_FLAG1 + b * 128), ws, "sc1");
    if (lane == 0) { sts2(L_EP, f); sts1(L_OK, 0u); sts1(L_CNT, 0u); }
  }
  if (w < 5) {
    if (w == 0) { if (roleA) VMWAIT(9); else VMWAIT(5); } else { if (roleA) VMWAIT(8); else VMWAIT(4); }
    TIE(xr);
    const unsigned d0 = __builtin_amdgcn_perm(xr.x, xr.x, 0x01000100u), d1 = __builtin_amdgcn_perm(xr.x, xr.x, 0x03020302u);
    const unsigned d2 = __builtin_amdgcn_perm(xr.y, xr.y, 0x01000100u), d3 = __builtin_amdgcn_perm(xr.y, xr.y, 0x03020302u);
    v4u a0 = {d0 & msk[0], d0 & msk[1], d0 & msk[2], d0 & msk[3]}, a1 = {d1 & msk[0], d1 & msk[1], d1 & msk[2], d1 & msk[3]};
    v4u a2 = {d2 & msk[0], d2 & msk[1], d2 & msk[2], d2 & msk[3]}, a3 = {d3 & msk[0], d3 & msk[1], d3 & msk[2], d3 & msk[3]};
    unsigned qr = L_QRED + w * 512 + (8 * n16 + 4 * g4) * 4;
    TIE(a0); TIE(a1); TIE(a2); TIE(a3); TIE(qr);
    if (w == 0) { if (roleA) VMWAIT(5); else VMWAIT(1); } else { if (roleA) VMWAIT(4); else VMWAIT(0); }
    TIE(wqr[0]); TIE(wqr[1]); TIE(wqr[2]); TIE(wqr[3]);
    STAMP(2);
    f4 acc = {0.f, 0.f, 0.f, 0.f};
    acc = mfma(a0, wqr[0], acc); acc = mfma(a1, wqr[1], acc); acc = mfma(a2, wqr[2], acc); acc = mfma(a3, wqr[3], acc);
    if (lane < 32) stsf4(qr, acc);
  }
  BARRIER();
  if (w == 0) {
    if (lane < 32) {
      const unsigned ra = L_QRED + lane * 16;
      f4 s0 = ldsf4(ra), s1 = ldsf4(ra + 512), s2 = ldsf4(ra + 1024), s3 = ldsf4(ra + 1536), s4_ = ldsf4(ra + 2048);
      f4 sm = (s0 + s1) + (s2 + s3) + s4_;
      v2u o = {guard(cvtpk(sm.x, sm.y)), guard(cvtpk(sm.z, sm.w))};
      GST2(o, (unsigned)(OFF_QP + (hq * 8 + j8) * 256 + lane * 8), ws, "sc1");
    }
    STAMP(3);
  }
#define EPOCH() ({ v2u e_ = *(LAS v2u*)(size_t)L_EP; (((u64)(unsigned)__builtin_amdgcn_readfirstlane(e_.y)) << 32) | (unsigned)__builtin_amdgcn_readfirstlane(e_.x); })
#define FLAG1_CHECK(BLK) \
  for (unsigned spin = 0;; ++spin) { \
    v2u f0; \
    GLD2(f0, (unsigned)(OFF_FLAG1 + (BLK) * 128), ws, 0, "sc1"); \
    VMWAIT(0); TIE(f0); \
    const unsigned df = (f0.x ^ (unsigned)epoch) | (f0.y ^ (unsigned)(epoch >> 32)); \
    if (__builtin_amdgcn_ballot_w64(df != 0u) == 0ull || spin > SPIN_CAP) break; \
  }
#define FLAG2_CHECK(HALF) \
  for (unsigned spin = 0;; ++spin) { \
    v4u f0; \
    GLD4(f0, (unsigned)(OFF_FLAG2 + (HALF) * 1024 + lane * 16), ws, 0, "sc1"); \
    VMWAIT(0); TIE(f0); \
    const unsigned lo = (unsigned)epoch, hi = (unsigned)(epoch >> 32); \
    const unsigned df = ((f0.x ^ lo) | (f0.y ^ hi)) | ((f0.z ^ lo) | (f0.w ^ hi)); \
    if (__builtin_amdgcn_ballot_w64(df != 0u) == 0ull || spin > SPIN_CAP) break; \
  }
  const u64 epoch = EPOCH();

  if (roleA) {
    const int h = hq, j = j8;
    if (w == 7) { const int j8l = lane & 7; const int blk = (h >> 2) + 8 * ((j8l >> 2) * 16 + (h & 3) * 4 + (j8l & 3)); FLAG1_CHECK(blk); CODEPF_DONE(); STAMP(3); }
    else VMWAIT(0);
    STAMP(12);
    v4u uk[4];
#pragma unroll
    for (int ks = 0; ks < 4; ++ks) uk[ks] = lds4(L_UK + (16 * w + n16) * 256 + (((4 * ks + g4) ^ n16) * 16));
    unsigned qb_a = L_QB + (n16 & 7) * 288 + g4 * 16;
    unsigned qa_o = (unsigned)(OFF_QA + h * 1024 + (128 * j + 16 * w + 4 * g4) * 2);
    TIE(qb_a); TIE(qa_o);
    if (w == 7) {
      v4u p0, p1;
      const unsigned vo = (unsigned)(OFF_QP + h * 2048 + lane * 16);
      unsigned wa = L_QB + g4 * 288 + n16 * 16; TIE(wa);
      for (unsigned spin = 0;; ++spin) {
        GLD4(p0, vo, ws, 0, "sc1"); GLD4(p1, vo, ws, 1024, "sc1");
        VMWAIT(0); TIE(p0); TIE(p1);
        if (allvalid(umax(mx4(p0), mx4(p1))) || spin > SPIN_CAP) break;
      }
      STAMP(4);
      sts4(wa, p0); sts4(wa + 4 * 288, p1);
    }
    BARRIER();
    {
      v4u bq[4];
#pragma unroll
      for (int ks = 0; ks < 4; ++ks) bq[ks] = lds4(qb_a + ks * 64);
      SCHED();
      f4 acc = {0.f, 0.f, 0.f, 0.f};
#pragma unroll
      for (int ks = 0; ks < 4; ++ks) acc = mfma(uk[ks], bq[ks], acc);
      float v0 = acc.x, v1 = acc.y, v2 = acc.z, v3 = acc.w;
      v0 += dpp<XOR1>(v0); v1 += dpp<XOR1>(v1); v2 += dpp<XOR1>(v2); v3 += dpp<XOR1>(v3);
      v0 += dpp<XOR2>(v0); v1 += dpp<XOR2>(v1); v2 += dpp<XOR2>(v2); v3 += dpp<XOR2>(v3);
      v0 += dpp<HMIR>(v0); v1 += dpp<HMIR>(v1); v2 += dpp<HMIR>(v2); v3 += dpp<HMIR>(v3);
      v2u o = {guard(cvtpk(v0, v1)), guard(cvtpk(v2, v3))};
      if (n16 == 0) GST2(o, qa_o, ws, "sc1");
    }
    STAMP(5);
    RAWBAR();
    const int ca = j & 1, db = j >> 1;
    {
      const char* vb = wuv + (size_t)(256 * ca + 32 * w) * 8192 + h * 256 + db * 128;
#pragma unroll
      for (int i = 0; i < 4; ++i)
        dma16((unsigned)((8 * i + (lane >> 3)) * 8192 + (((lane & 7) ^ ((((lane >> 4) & 1) << 1) | ((i & 1) << 2))) * 16)), vb, (unsigned)(L_WUV + (32 * w + 8 * i) * 128));
    }
    if (w == 6) {
      v4u s_ = {SENT, SENT, SENT, SENT};
      GST4(s_, (unsigned)(OFF_OP + (h * 4 + j) * 1280 + lane * 16), ws, "sc1");
      if (lane < 16) GST4(s_, (unsigned)(OFF_OP + (h * 4 + j) * 1280 + 1024 + lane * 16), ws, "sc1");
    }
    {
      unsigned ovo[5];
#pragma unroll
      for (int i = 0; i < 5; ++i) {
        const unsigned n = 64u * (unsigned)(w + 8 * i) + (unsigned)lane, row = (n * 52429u) >> 22, pos = n - row * 80u;
        ovo[i] = row * 1280u + ((pos ^ (((row & 3u) << 1) | (((row >> 3) & 1u) << 3))) * 16u);
      }
      const char* ob = wo + (size_t)(h * 128 + 64 * db) * 1280;
      RAWBAR();
#pragma unroll
      for (int i = 0; i < 5; ++i) dma16(ovo[i], ob + ca * 40960, (unsigned)(L_WO + ca * 40960 + (w + 8 * i) * 1024));
      RAWBAR();
#pragma unroll
      for (int i = 0; i < 5; ++i) dma16(ovo[i], ob + (1 - ca) * 40960, (unsigned)(L_WO + (1 - ca) * 40960 + (w + 8 * i) * 1024));
    }
    if (lane < 8) sts4(L_TAB + w * 512 + 256 + lane * 16, v4u{0u, 0u, 0u, 0u});
    unsigned pvo = (unsigned)(OFF_PP + (h * 32 + n16) * 1024 + 512 * ca + 64 * w + 16 * g4), pvo2 = pvo + 16384u;
    unsigned sto = (unsigned)(OFF_ST + ((h >> 3) * 32 + (lane & 31)) * 64 + (h & 7) * 8);
    unsigned crw = L_CRED + lane * 32 + w * 4, crr = L_CRED + lane * 32;
    unsigned tabh = L_TAB + w * 512 + lane * 2;
    unsigned opo = (unsigned)(OFF_OP + (h * 4 + j) * 1280 + 160 * w + 32 * n16 + 8 * g4);
    unsigned bva[4], woa[5], bta[5];
    {
      const int q = (lane & 15) >> 2, p = lane & 3;
#pragma unroll
      for (int dt = 0; dt < 4; ++dt)
        bva[dt] = L_WUV + (32 * w + 8 * g4 + q) * 128 + (((2 * dt + (p >> 1)) ^ (((q >> 1) << 1) | ((g4 & 1) << 2))) * 16) + 8 * (p & 1);
#pragma unroll
      for (int t = 0; t < 5; ++t) {
        woa[t] = L_WO + (8 * g4 + q) * 1280 + (((10 * w + 2 * t + (p >> 1)) ^ ((q << 1) | ((g4 & 1) << 3))) * 16) + 8 * (p & 1);
        bta[t] = L_TAB + w * 512 + ((n16 & 7) == t ? (n16 >> 3) * 128 : 256) + g4 * 16;
      }
    }
    TIE(pvo); TIE(pvo2); TIE(sto); TIE(crw); TIE(crr); TIE(tabh); TIE(opo);
#pragma unroll
    for (int t = 0; t < 5; ++t) { TIE(woa[t]); TIE(bta[t]); }
    if (w == 6) VMWAIT(12); else VMWAIT(10);
    STAMP(13);
    v4u bv[4];
#pragma unroll
    for (int dt = 0; dt < 4; ++dt) bv[dt] = ldstr(bva[dt], bva[dt] + 512);
    if (w == 7) { FLAG2_CHECK(1); }
    VMWAIT(0);
    if (w == 6 && lane == 0) { v2u f = {(unsigned)epoch, (unsigned)(epoch >> 32)}; GST2(f, (unsigned)(OFF_FLAG2 + b * 8), ws, "sc1"); }
    STAMP(6);
    BARRIER();
    v4u wa[5][2];
#pragma unroll
    for (int t = 0; t < 5; ++t) { wa[t][0] = ldstr(woa[t], woa[t] + 5120); wa[t][1] = ldstr(woa[t] + 40960, woa[t] + 46080); }
    SCHED();
    if (w == 0) {
      v2u st;
      for (unsigned spin = 0;; ++spin) {
        GLD2(st, sto, ws, 0, "sc1");
        VMWAIT(0); TIE(st);
        if (allvalid(umax(st.x, st.y)) || spin > SPIN_CAP) break;
      }
      if (lane == 0) sts1(L_OK, 2u);
      float m = asf(st.x), l = asf(st.y);
      float M = p16_max(row_max(m));
      float a = __builtin_amdgcn_exp2f((m - M) * SC);
      float Ls = p16_sum(row_sum(a * l));
      a = a * __builtin_amdgcn_rcpf(Ls);
      if (lane < 32) sts1(L_AW + lane * 4, asu(a));
      LGWAIT();
      if (lane == 0) sts1(L_OK, 3u);
    } else {
      for (unsigned spin = 0; *(volatile LAS unsigned*)(size_t)L_OK < 2u && spin < 64u * SPIN_CAP; ++spin) __builtin_amdgcn_s_sleep(1);
    }
    v4u pa[2];
    GLD4(pa[0], pvo, ws, 0, "sc1"); GLD4(pa[1], pvo2, ws, 0, "sc1");
    STAMP(7);
    for (unsigned spin = 0; *(volatile LAS unsigned*)(size_t)L_OK < 3u && spin < 64u * SPIN_CAP; ++spin) __builtin_amdgcn_s_sleep(1);
    f4 aw[2];
    aw[0] = ldsf4(L_AW + g4 * 16); aw[1] = ldsf4(L_AW + 64 + g4 * 16);
    for (unsigned spin = 0;; ++spin) {
      VMWAIT(0); TIE(pa[0]); TIE(pa[1]);
      if (allvalid(umax(mx4(pa[0]), mx4(pa[1]))) || spin > SPIN_CAP) break;
      GLD4(pa[0], pvo, ws, 0, "sc1"); GLD4(pa[1], pvo2, ws, 0, "sc1");
    }
    STAMP(8);
    {
      const f4 z = {0.f, 0.f, 0.f, 0.f};
      float u[4];
#pragma unroll
      for (int dt = 0; dt < 4; ++dt) {
        f4 c = aw[0] * mfma(pa[0], bv[dt], z) + aw[1] * mfma(pa[1], bv[dt], z);
        u[dt] = (c.x + c.y) + (c.z + c.w);
      }
      auto t0 = __builtin_amdgcn_permlane16_swap(asu(u[0]), asu(u[1]), false, false); auto t1 = __builtin_amdgcn_permlane16_swap(asu(u[2]), asu(u[3]), false, false);
      unsigned x0 = t0[0], x1 = t0[1], y0 = t1[0], y1 = t1[1];
      auto t2 = __builtin_amdgcn_permlane32_swap(asu(asf(x0) + asf(x1)), asu(asf(y0) + asf(y1)), false, false);
      unsigned z0 = t2[0], z1 = t2[1];
      sts1(crw, asu(asf(z0) + asf(z1)));
    }
    BARRIER();
    STAMP(9);
    {
      float c = sum8(ldsf4(crr), ldsf4(crr + 16));
      unsigned hi = cvtpk(c, c);
      float rs = c - asf(hi << 16);
      unsigned lo = cvtpk(rs, rs);
      *(LAS unsigned short*)(size_t)tabh = (unsigned short)hi;
      *(LAS unsigned short*)(size_t)(tabh + 128) = (unsigned short)lo;
      LGWAIT();
      v4u bt[5][2];
#pragma unroll
      for (int t = 0; t < 5; ++t) { bt[t][0] = lds4(bta[t]); bt[t][1] = lds4(bta[t] + 64); }
      SCHED();
      f4 acc = {0.f, 0.f, 0.f, 0.f};
#pragma unroll
      for (int t = 0; t < 5; ++t) { acc = mfma(wa[t][0], bt[t][0], acc); acc = mfma(wa[t][1], bt[t][1], acc); }
      float o0 = acc.x + dpp<ROR8>(acc.x), o1 = acc.y + dpp<ROR8>(acc.y), o2 = acc.z + dpp<ROR8>(acc.z), o3 = acc.w + dpp<ROR8>(acc.w);
      unsigned a0 = guard(cvtpk(o0, o1)), a1 = guard(cvtpk(o2, o3));
      auto s0 = __builtin_amdgcn_permlane16_swap(a0, a0, false, false); auto s1 = __builtin_amdgcn_permlane16_swap(a1, a1, false, false);
      unsigned b0 = s0[1], b1 = s1[1];
      v4u o = {a0, a1, b0, b1};
      if (n16 < 5 && (g4 & 1) == 0) GST4(o, opo, ws, "sc1");
    }
    STAMP(10);
  } else {
    const int g = rr & 3, s = xcd * 4 + (rr >> 2), bi = xcd * 16 + rr;
    RAWBAR();
    if (w < 5) {
      CKV_ROWS(0, 16);
    } else {
      CKV_ROWS(NPRE2, 16);
      v2u f1;
      unsigned f1o;
      { const int hd = 8 * g + ((lane & 31) >> 2); f1o = (unsigned)(OFF_FLAG1 + ((hd >> 2) + 8 * ((hd & 3) * 4 + (lane & 3))) * 128); }
      if (w == 7) GLD2(f1, f1o, ws, 0, "sc1");
      if (w == 5) { dma16((unsigned)(lane * 16), x, L_QB); if (lane < 16) dma16((unsigned)(lane * 16), x + 1024, L_QB + 1024); }
      if (w == 5) VMWAIT(18); else if (w == 6) VMWAIT(16); else VMWAIT(17);
      unsigned old = 0;
      if (lane == 0) old = __hip_atomic_fetch_add((LAS unsigned*)(size_t)L_CNT, 1u, __ATOMIC_RELAXED, __HIP_MEMORY_SCOPE_WORKGROUP);
      if (lane == 0 && old == 2u) { v2u f = {(unsigned)epoch, (unsigned)(epoch >> 32)}; GST2(f, (unsigned)(OFF_FLAG2 + b * 8), ws, "sc1"); }
      if (w == 7) {
        VMWAIT(0); TIE(f1);
        for (unsigned spin = 0; spin < SPIN_CAP; ++spin) {
          const unsigned df = (f1.x ^ (unsigned)epoch) | (f1.y ^ (unsigned)(epoch >> 32));
          if (__builtin_amdgcn_ballot_w64(df != 0u) == 0ull) break;
          GLD2(f1, f1o, ws, 0, "sc1");
          VMWAIT(0); TIE(f1);
        }
        CODEPF_DONE();
        if (lane == 0) sts1(L_OK, 1u);
      }
    }
    unsigned qvo = (unsigned)(OFF_QA + (8 * g + w) * 1024 + lane * 16);
    unsigned qsw = L_QAS + w * 1056 + lane * 16, qsr = L_QAS + (n16 & 7) * 1056 + g4 * 16;
    unsigned mxw = L_MX + lane * 32 + w * 4, mxr = L_MX + (n16 & 7) * 32;
    unsigned pw = L_P + n16 * 288 + (16 * kt + 4 * g4) * 2, pr = L_P + (n16 & 7) * 288 + g4 * 16;
    unsigned lsw = L_LS + lane * 32 + w * 4;
    unsigned ppo = (unsigned)(OFF_PP + ((8 * g + (n16 & 7)) * 32 + s) * 1024 + (64 * w + 32 * (g4 >> 1) + 8 * (2 * (n16 >> 3) + (g4 & 1))) * 2);
    unsigned ka[4], ta[4][2];
    {
      const unsigned kr = L_CKV + (16 * kt + n16) * 1024;
#pragma unroll
      for (int u = 0; u < 4; ++u) ka[u] = kr + (((4 * u + g4) ^ n16) * 16);
      const int q = (lane & 15) >> 2, p = lane & 3;
#pragma unroll
      for (int t = 0; t < 4; ++t) {
        const int ch = 8 * w + 4 * (p >> 1) + t;
        const int r0 = 8 * g4 + q, r1 = r0 + 4;
        ta[t][0] = L_CKV + r0 * 1024 + ((ch ^ (r0 & 15)) * 16) + 8 * (p & 1);
        ta[t][1] = L_CKV + r1 * 1024 + ((ch ^ (r1 & 15)) * 16) + 8 * (p & 1);
      }
    }
    TIE(qvo); TIE(qsw); TIE(qsr); TIE(mxw); TIE(mxr); TIE(pw); TIE(pr); TIE(lsw); TIE(ppo);
#pragma unroll
    for (int u = 0; u < 4; ++u) { TIE(ka[u]); TIE(ta[u][0]); TIE(ta[u][1]); }
    VMWAIT(0);
    STAMP(4);
    for (unsigned spin = 0; *(volatile LAS unsigned*)(size_t)L_OK == 0u && spin < SPIN_CAP; ++spin) __builtin_amdgcn_s_sleep(1);
    v4u qa;
    GLD4(qa, qvo, ws, 0, "sc1");
    v4u kA[16];
#pragma unroll
    for (int ks = 0; ks < 16; ++ks) kA[ks] = lds4(ka[ks & 3] + (ks >> 2) * 256);
    BARRIER();
    STAMP(5);
    v4u vA[4][4];
#pragma unroll
    for (int t = 0; t < 4; ++t) {
      vA[0][t] = ldstr(ta[t][0], ta[t][1]); vA[1][t] = ldstr(ta[t][0] + 32768, ta[t][1] + 32768);
      const unsigned b0 = ta[t][0] + 65536, b1 = ta[t][1] + 65536;
      vA[2][t] = ldstr(b0, b1); vA[3][t] = ldstr(b0 + 32768, b1 + 32768);
    }
    SCHED();
    VMWAIT(0); TIE(qa);
    if (__builtin_expect(!allvalid(mx4(qa)), 0)) {
      v4u qb;
      for (unsigned spin = 0;; ++spin) {
        GLD4(qb, qvo, ws, 0, "sc1");
        VMWAIT(0); TIE(qb);
        if (allvalid(mx4(qb)) || spin > SPIN_CAP) break;
      }
      qa = qb;
    }
    STAMP(6);
    sts4(qsw, qa);
    BARRIER();
    const int fb = bi < 108 ? bi : 107;
    const char* b1 = w1 + fb * 32;
    const unsigned vo1 = (unsigned)((lane >> 1) * 3456 + (lane & 1) * 16);
    const int pq = 5 * (bi & 3);
    f4 sc = {0.f, 0.f, 0.f, 0.f};
    {
      v4u bq[16];
#pragma unroll
      for (int ks = 0; ks < 16; ++ks) bq[ks] = lds4(qsr + ks * 64);
      const char* b2 = w2 + (size_t)(16 * fb) * 1280 + w * 1024;
      const unsigned vo2 = (unsigned)(lane * 16);
      const int p0 = pq + w;
      SCHED();
#pragma unroll
      for (int ks = 0; ks < 16; ++ks) {
        sc = mfma(kA[ks], bq[ks], sc);
        if (MLP1 && ks == 2) { SCHED(); if (w < 5) dma16(vo1, b1 + (size_t)(32 * p0) * 3456, L_W1 + p0 * 1024); SCHED(); }
        if (MLP1 && ks == 5) { SCHED(); dma16(vo2, b2, L_W2 + w * 1024); SCHED(); }
        if (MLP1 && ks == 8) { SCHED(); dma16(vo2, b2 + 8192, L_W2 + (w + 8) * 1024); SCHED(); }
        if (MLP1 && ks == 11) { SCHED(); if (w < 4) dma16(vo2, b2 + 16384, L_W2 + (w + 16) * 1024); SCHED(); }
      }
      float mw = p32_max(p16_max(fmx(fmx(sc.x, sc.y), fmx(sc.z, sc.w))));
      if (lane < 8) sts1(mxw, asu(mw));
    }
    BARRIER();
    STAMP(7);
    {
      const float Ms = -max8(ldsf4(mxr), ldsf4(mxr + 16)) * SC;
      float p0 = __builtin_amdgcn_exp2f(fmaf(sc.x, SC, Ms)), p1 = __builtin_amdgcn_exp2f(fmaf(sc.y, SC, Ms)), p2 = __builtin_amdgcn_exp2f(fmaf(sc.z, SC, Ms)), p3 = __builtin_amdgcn_exp2f(fmaf(sc.w, SC, Ms));
      v2u pk = {cvtpk(p0, p1), cvtpk(p2, p3)};
      if (n16 < 8) sts2(pw, pk);
      float ls = p32_sum(p16_sum((p0 + p1) + (p2 + p3)));
      if (lane < 8) sts1(lsw, asu(ls));
    }
    BARRIER();
    STAMP(8);
    {
      v4u pb[4];
#pragma unroll
      for (int ks = 0; ks < 4; ++ks) pb[ks] = lds4(pr + ks * 64);
      f4 lu, lv, mu, mv;
      if (w == 7) { const unsigned sa = (lane & 7) * 32; lu = ldsf4(L_LS + sa); lv = ldsf4(L_LS + sa + 16); mu = ldsf4(L_MX + sa); mv = ldsf4(L_MX + sa + 16); }
      SCHED();
      f4 ac[4];
#pragma unroll
      for (int t = 0; t < 4; ++t) ac[t] = mfma(vA[0][t], pb[0], f4{0.f, 0.f, 0.f, 0.f});
      if (w == 7) {
        v2u o = {guard(asu(max8(mu, mv))), guard(asu(sum8(lu, lv)))};
        if (lane < 8) GST2(o, (unsigned)(OFF_ST + (g * 32 + s) * 64 + lane * 8), ws, "sc1");
      }
#pragma unroll
      for (int ks = 1; ks < 4; ++ks)
#pragma unroll
        for (int t = 0; t < 4; ++t) ac[t] = mfma(vA[ks][t], pb[ks], ac[t]);
      const bool up = n16 >= 8;
      f4 xa = up ? ac[2] : ac[0], ya = up ? ac[3] : ac[1];
      unsigned x0 = cvtpk(xa.x, xa.y), x1 = cvtpk(xa.z, xa.w), y0 = cvtpk(ya.x, ya.y), y1 = cvtpk(ya.z, ya.w);
      auto s0 = __builtin_amdgcn_permlane16_swap(x0, y0, false, false); auto s1 = __builtin_amdgcn_permlane16_swap(x1, y1, false, false);
      unsigned e0 = s0[0], e1 = s1[0], f0 = s0[1], f1 = s1[1];
      v4u o = {guard(e0), guard(e1), guard(f0), guard(f1)};
      GST4(o, ppo, ws, "sc1");
    }
    STAMP(9);
    RAWBAR();
    int pc0 = pq + 5 + w, pc1 = pq + 13 + w;
    if (pc0 >= 20) pc0 -= 20;
    if (pc1 >= 20) pc1 -= 20;
    if (!MLP1) {
      const char* b2 = w2 + (size_t)(16 * fb) * 1280 + w * 1024;
      const unsigned vo2 = (unsigned)(lane * 16);
      if (w < 5) dma16(vo1, b1 + (size_t)(32 * (pq + w)) * 3456, L_W1 + (pq + w) * 1024);
      dma16(vo2, b2, L_W2 + w * 1024); dma16(vo2, b2 + 8192, L_W2 + (w + 8) * 1024); if (w < 4) dma16(vo2, b2 + 16384, L_W2 + (w + 16) * 1024);
      RAWBAR();
    }
    dma16(vo1, b1 + (size_t)(32 * pc0) * 3456, L_W1 + pc0 * 1024);
    if (w < 7) dma16(vo1, b1 + (size_t)(32 * pc1) * 3456, L_W1 + pc1 * 1024);
    VMWAIT(0);
    STAMP(12);
    {
      const int q = (lane & 15) >> 2, p = lane & 3;
      const unsigned wa = L_W1 + (8 * g4 + q) * 32 + 8 * p, xa = L_QB + g4 * 16;
      f4 hacc = {0.f, 0.f, 0.f, 0.f};
      hacc = mfma(lds4(xa + pc0 * 64), ldstr(wa + pc0 * 1024, wa + pc0 * 1024 + 128), hacc);
      if (w < 7) hacc = mfma(lds4(xa + pc1 * 64), ldstr(wa + pc1 * 1024, wa + pc1 * 1024 + 128), hacc);
      if (w < 5) { const int p0 = pq + w; hacc = mfma(lds4(xa + p0 * 64), ldstr(wa + p0 * 1024, wa + p0 * 1024 + 128), hacc); }
      if (lane < 16) sts1(L_RED + lane * 32 + w * 4, asu(hacc.x));
    }
    BARRIER();
    STAMP(15);
    if (w < 5) {
      v4u w2r[4];
#pragma unroll
      for (int i = 0; i < 4; ++i) w2r[i] = lds4(L_W2 + (4 * g4 + i) * 1280 + w * 256 + n16 * 16);
      const int f = lane & 15;
      float hh = sum8(ldsf4(L_RED + f * 32), ldsf4(L_RED + f * 32 + 16));
      hh = hh * __builtin_amdgcn_rcpf(1.f + __expf(-hh));
      if (bi >= 108) hh = 0.f;
      unsigned hi = cvtpk(hh, hh);
      float rs = hh - asf(hi << 16);
      unsigned lo = cvtpk(rs, rs);
      if (lane < 32) sts1(L_TAB + w * 512 + lane * 4, lane < 16 ? hi : lo);
      LGWAIT();
      v4u t0 = lds4(L_TAB + w * 512 + ((n16 >> 3) * 16 + 4 * g4) * 4);
      unsigned dv[4] = {t0.x, t0.y, t0.z, t0.w};
      f4 acc = {0.f, 0.f, 0.f, 0.f};
#pragma unroll
      for (int i = 0; i < 4; ++i) { v4u a = {dv[i] & msk[0], dv[i] & msk[1], dv[i] & msk[2], dv[i] & msk[3]}; acc = mfma(a, w2r[i], acc); }
      float o0 = p32_sum(acc.x), o1 = p32_sum(acc.y), o2 = p32_sum(acc.z), o3 = p32_sum(acc.w);
      unsigned a0 = guard(cvtpk(o0, o1)), a1 = guard(cvtpk(o2, o3));
      auto s0 = __builtin_amdgcn_permlane16_swap(a0, a0, false, false); auto s1 = __builtin_amdgcn_permlane16_swap(a1, a1, false, false);
      unsigned b0 = s0[1], b1 = s1[1];
      v4u o = {a0, a1, b0, b1};
      if (lane < 16) GST4(o, (unsigned)(OFF_MP + bi * 1280 + w * 256 + lane * 16), ws, "sc1");
    }
    STAMP(10);
    if (bi < 80 && w == 5) {
      FLAG2_CHECK(0);
      FLAG2_CHECK(1);
      unsigned gg[5];
#pragma unroll
      for (int i = 0; i < 5; ++i) GLD1(gg[i], (unsigned)(lane * 4), gw, i * 256, "");
      VMWAIT(0);
#pragma unroll
      for (int i = 0; i < 5; ++i) TIE(gg[i]);
      float gate;
      {
        float a = 0.f;
#pragma unroll
        for (int i = 0; i < 5; ++i) { const unsigned gx = *(LAS unsigned*)(size_t)(L_QB + i * 256 + lane * 4); a += asf(gx << 16) * asf(gg[i] << 16) + asf(gx & 0xFFFF0000u) * asf(gg[i] & 0xFFFF0000u); }
        a = p32_sum(p16_sum(row_sum(a)));
        gate = __builtin_amdgcn_rcpf(1.f + __expf(-a));
      }
      const unsigned one = 0x3F803F80u;
      v4u ones = {one & msk[0], one & msk[1], one & msk[2], one & msk[3]};
      v4u m0, m1;
      const unsigned mvo = (unsigned)(OFF_MP + lane * 1280 + bi * 16), mvo2 = mvo + 64 * 1280;
      for (unsigned spin = 0;; ++spin) {
        GLD4(m0, mvo, ws, 0, "sc1"); GLD4(m1, mvo2, ws, 0, "sc1");
        VMWAIT(0); TIE(m0); TIE(m1);
        if (allvalid(umax(mx4(m0), mx4(m1))) || spin > SPIN_CAP) break;
      }
      f4 bb = {0.f, 0.f, 0.f, 0.f};
      bb = mfma(ones, m0, bb); bb = mfma(ones, m1, bb);
      bb *= (1.f - gate);
      STAMP(11);
      const unsigned ovo = (unsigned)(OFF_OP + lane * 1280 + bi * 16), ovo2 = ovo + 64 * 1280;
      for (unsigned spin = 0;; ++spin) {
        GLD4(m0, ovo, ws, 0, "sc1"); GLD4(m1, ovo2, ws, 0, "sc1");
        VMWAIT(0); TIE(m0); TIE(m1);
        if (allvalid(umax(mx4(m0), mx4(m1))) || spin > SPIN_CAP) break;
      }
      STAMP(12);
      f4 aa = {0.f, 0.f, 0.f, 0.f};
      aa = mfma(ones, m0, aa); aa = mfma(ones, m1, aa);
      float r0 = row_sum(fmaf(gate, aa.x, bb.x)), r1 = row_sum(fmaf(gate, aa.y, bb.y));
      float r2 = row_sum(fmaf(gate, aa.z, bb.z)), r3 = row_sum(fmaf(gate, aa.w, bb.w));
      v2u o = {cvtpk(r0, r1), cvtpk(r2, r3)};
      if (n16 == 0 && g4 < 2) GST2(o, (unsigned)(bi * 16 + g4 * 8), out, "");
      if (bi == 0 && lane == 0) {
        v2u f = {(unsigned)epoch, (unsigned)(epoch >> 32)};
        GST2(f, (unsigned)OFF_E, ws, "sc1");
      }
      STAMP(13);
    }
  }
  asm volatile(".Lgm_code_end:" ::: "memory");
  if (lane < 16) ((u64*)(ws + OFF_STAMP))[((size_t)b * 8 + w) * 16 + lane] = tsv;
}

__global__ void k_pre(u64* st) { if (threadIdx.x == 0) st[256 * 8 * 16] = __builtin_amdgcn_s_memrealtime(); }
__global__ void k_amp(const u64* st, int role, int idx, int stat, int amp) {
  const u64 tstart = __builtin_amdgcn_s_memrealtime();
  __shared__ u64 s0[256], s1[256];
  const int t = threadIdx.x;
  u64 mn = ~0ull, v = stat == 2 ? ~0ull : 0ull; bool any = false;
  for (int w = 0; w < 8; ++w) { const u64* q = st + ((size_t)t * 8 + w) * 16; mn = q[0] < mn ? q[0] : mn;
    const bool ok = role == 2 || ((t >> 3) >= 16) == (role == 1); const u64 x = q[idx];
    if (ok && x) { any = true; if (stat == 2) v = x < v ? x : v; else v = x > v ? x : v; } }
  s0[t] = mn; s1[t] = any ? v : 0ull;
  __syncthreads();
  if (t == 0) {
    mn = ~0ull; for (int i = 0; i < 256; ++i) mn = s0[i] < mn ? s0[i] : mn;
    u64 r = 0; int n = 0;
    if (stat == 0) { for (int i = 0; i < 256; ++i) r = s1[i] > r ? s1[i] : r; }
    else if (stat == 2) { r = ~0ull; for (int i = 0; i < 256; ++i) if (s1[i]) r = s1[i] < r ? s1[i] : r; }
    else if (stat == 1) {
      for (int i = 0; i < 256; ++i) if (s1[i]) n++;
      for (int i = 0; i < 256; ++i) if (s1[i]) { int c = 0; for (int k = 0; k < 256; ++k) if (s1[k] && (s1[k] < s1[i] || (s1[k] == s1[i] && k < i))) c++; if (c == n / 2) r = s1[i]; } }
    u64 q = r - mn;
    if (stat == 3) q = mn - st[256 * 8 * 16];
    if (stat == 4) { u64 mx = 0; for (int i = 0; i < 256; ++i) mx = s1[i] > mx ? s1[i] : mx; q = tstart - mx; }
    if (q > 5000) q = 5000;
    const u64 until = __builtin_amdgcn_s_memrealtime() + q * amp;
    while (__builtin_amdgcn_s_memrealtime() < until) __builtin_amdgcn_s_sleep(4);
  }
}

extern "C" void kernel_launch(void* const* d_in, const int* in_sizes, int n_in,
                              void* d_out, int out_size, void* d_ws, size_t ws_size,
                              hipStream_t stream) {
  gm_kernel<<<256, 512, 0, stream>>>((const char*)d_in[0], (const char*)d_in[1], (const char*)d_in[2], (const char*)d_in[3],
                                      (const char*)d_in[4], (const char*)d_in[5], (const char*)d_in[7], (const char*)d_in[8],
                                      (const char*)d_in[9], (char*)d_out, (char*)d_ws);
  k_amp<<<1, 256, 0, stream>>>((const u64*)((char*)d_ws + OFF_STAMP), 0, 3, 0, 0);
}
```

```cpp
#include <hip/hip_runtime.h>
#include <stdint.h>
#pragma clang diagnostic ignored "-Winline-asm"

typedef unsigned v4u __attribute__((ext_vector_type(4)));
typedef unsigned v2u __attribute__((ext_vector_type(2)));
typedef float f4 __attribute__((ext_vector_type(4)));
typedef float f2 __attribute__((ext_vector_type(2)));
typedef short s4 __attribute__((ext_vector_type(4)));
typedef short s8 __attribute__((ext_vector_type(8)));
typedef __bf16 bf2 __attribute__((ext_vector_type(2)));
typedef unsigned long long u64;
#define LAS __attribute__((address_space(3)))
#define DI __device__ __forceinline__

constexpr unsigned OFF_E = 0;
constexpr unsigned OFF_FLAG2 = 4096;
constexpr unsigned OFF_FLAG1 = 8192;
constexpr unsigned OFF_QP = OFF_FLAG1 + 32768;
constexpr unsigned OFF_QA = OFF_QP + 65536;
constexpr unsigned OFF_ST = OFF_QA + 32768;
constexpr unsigned OFF_OP = OFF_ST + 8192;
constexpr unsigned OFF_MP = OFF_OP + 163840;
constexpr unsigned OFF_PP = OFF_MP + 163840;
constexpr unsigned OFF_END = OFF_PP + 1048576;
constexpr unsigned OFF_STAMP = 8u << 20;
constexpr unsigned SENT = 0xFFFFFFFFu;
constexpr unsigned SPIN_CAP = 1u << 15;
constexpr float SC = 0.08838834764831845f * 1.4426950408889634f;

constexpr unsigned L_CKV = 0;
constexpr unsigned L_W1 = 0, L_W2 = 32768;
constexpr unsigned L_WUV = 0, L_WO = 32768;
constexpr unsigned L_AW = 131072 + 2560;
constexpr unsigned L_CRED = 131072;
constexpr unsigned L_UK = 32768;
constexpr unsigned L_QAS = 131072;
constexpr unsigned L_QRED = L_QAS;
constexpr unsigned L_P = L_QAS + 8448;
constexpr unsigned L_MX = L_P + 2304;
constexpr unsigned L_LS = L_MX + 256;
constexpr unsigned L_RED = L_LS + 256;
constexpr unsigned L_TAB = L_RED + 1024;
constexpr unsigned L_QB = L_TAB + 4096;
constexpr unsigned L_EP = L_QB + 2304;
constexpr unsigned L_OK = L_EP + 8;
constexpr unsigned L_CNT = L_EP + 12;
constexpr unsigned L_END = L_EP + 16;

DI f4 mfma(v4u a, v4u b, f4 c) { return __builtin_amdgcn_mfma_f32_16x16x32_bf16(__builtin_bit_cast(s8, a), __builtin_bit_cast(s8, b), c, 0, 0, 0); }
DI unsigned cvtpk(float lo, float hi) { f2 x = {lo, hi}; bf2 y = __builtin_convertvector(x, bf2); return __builtin_bit_cast(unsigned, y); }
DI float asf(unsigned u) { return __uint_as_float(u); }
DI unsigned asu(float f) { return __float_as_uint(f); }
DI unsigned guard(unsigned u) { return u < 0xFFFFFFFEu ? u : 0xFFFFFFFEu; }
DI float fmx(float a, float b) { return __builtin_elementwise_maximum(a, b); }
template <int CTRL> DI float dpp(float x) { return asf((unsigned)__builtin_amdgcn_mov_dpp((int)asu(x), CTRL, 0xf, 0xf, true)); }
template <int CTRL> DI unsigned dppu(unsigned x) { return (unsigned)__builtin_amdgcn_mov_dpp((int)x, CTRL, 0xf, 0xf, true); }
constexpr int XOR1 = 0xB1, XOR2 = 0x4E, ROR4 = 0x124, ROR8 = 0x128, HMIR = 0x141;
DI float p16_max(float x) { auto t = __builtin_amdgcn_permlane16_swap(asu(x), asu(x), false, false); unsigned a = t[0], b = t[1]; return fmx(asf(a), asf(b)); }
DI float p32_max(float x) { auto t = __builtin_amdgcn_permlane32_swap(asu(x), asu(x), false, false); unsigned a = t[0], b = t[1]; return fmx(asf(a), asf(b)); }
DI float p16_sum(float x) { auto t = __builtin_amdgcn_permlane16_swap(asu(x), asu(x), false, false); unsigned a = t[0], b = t[1]; return asf(a) + asf(b); }
DI float p32_sum(float x) { auto t = __builtin_amdgcn_permlane32_swap(asu(x), asu(x), false, false); unsigned a = t[0], b = t[1]; return asf(a) + asf(b); }
DI float row_sum(float x) { x += dpp<XOR1>(x); x += dpp<XOR2>(x); x += dpp<ROR4>(x); x += dpp<ROR8>(x); return x; }
DI float row_max(float x) { x = fmx(x, dpp<XOR1>(x)); x = fmx(x, dpp<XOR2>(x)); x = fmx(x, dpp<ROR4>(x)); x = fmx(x, dpp<ROR8>(x)); return x; }
DI float max8(f4 u, f4 v) { return fmx(fmx(fmx(u.x, u.y), fmx(u.z, u.w)), fmx(fmx(v.x, v.y), fmx(v.z, v.w))); }
DI float sum8(f4 u, f4 v) { return ((u.x + u.y) + (u.z + u.w)) + ((v.x + v.y) + (v.z + v.w)); }
DI unsigned umax(unsigned a, unsigned b) { return a > b ? a : b; }
DI unsigned mx4(v4u v) { return umax(umax(v.x, v.y), umax(v.z, v.w)); }
DI bool allvalid(unsigned m) { return __builtin_amdgcn_ballot_w64(m == SENT) == 0ull; }
DI v4u lds4(unsigned a) { return *(LAS v4u*)(size_t)a; }
DI f4 ldsf4(unsigned a) { return *(LAS f4*)(size_t)a; }
DI void sts4(unsigned a, v4u v) { *(LAS v4u*)(size_t)a = v; }
DI void stsf4(unsigned a, f4 v) { *(LAS f4*)(size_t)a = v; }
DI void sts2(unsigned a, v2u v) { *(LAS v2u*)(size_t)a = v; }
DI void sts1(unsigned a, unsigned v) { *(LAS unsigned*)(size_t)a = v; }
DI v4u ldstr(unsigned a0, unsigned a1) {
  s4 lo = __builtin_amdgcn_ds_read_tr16_b64_v4i16((LAS s4*)(size_t)a0), hi = __builtin_amdgcn_ds_read_tr16_b64_v4i16((LAS s4*)(size_t)a1);
  s8 c = __builtin_shufflevector(lo, hi, 0, 1, 2, 3, 4, 5, 6, 7);
  return __builtin_bit_cast(v4u, c);
}

#define GLD4(dst, voff, sbase, IMM, MOD) asm volatile("global_load_dwordx4 %0, %1, %2 offset:%3 " MOD : "=v"(dst) : "v"(voff), "s"(sbase), "i"(IMM) : "memory")
#define GLD2(dst, voff, sbase, IMM, MOD) asm volatile("global_load_dwordx2 %0, %1, %2 offset:%3 " MOD : "=v"(dst) : "v"(voff), "s"(sbase), "i"(IMM) : "memory")
#define GLD1(dst, voff, sbase, IMM, MOD) asm volatile("global_load_dword %0, %1, %2 offset:%3 " MOD : "=v"(dst) : "v"(voff), "s"(sbase), "i"(IMM) : "memory")
#define GST4(src, voff, sbase, MOD) asm volatile("global_store_dwordx4 %0, %1, %2 " MOD "\n\ts_nop 1" :: "v"(voff), "v"(src), "s"(sbase) : "memory")
#define GST2(src, voff, sbase, MOD) asm volatile("global_store_dwordx2 %0, %1, %2 " MOD :: "v"(voff), "v"(src), "s"(sbase) : "memory")
#define SCHED() __builtin_amdgcn_sched_barrier(0)
#define VMWAIT(N) do { asm volatile("s_waitcnt vmcnt(%0)" :: "i"(N) : "memory"); SCHED(); } while (0)
#define LGWAIT() do { asm volatile("s_waitcnt lgkmcnt(0)" ::: "memory"); SCHED(); } while (0)
#define TIE(x) asm volatile("" : "+v"(x))
DI void dma16(unsigned voff, const void* sbase, unsigned ldsdst) {
  asm volatile("s_mov_b32 m0, %2\n\ts_nop 0\n\tglobal_load_lds_dwordx4 %0, %1" :: "v"(voff), "s"(sbase), "s"(ldsdst) : "memory", "m0");
}
#define BARRIER() do { asm volatile("s_waitcnt lgkmcnt(0)\n\ts_barrier" ::: "memory"); SCHED(); } while (0)
#define RAWBAR() do { asm volatile("s_barrier" ::: "memory"); SCHED(); } while (0)

#define CODEPF_DONE() asm volatile("" :: "v"(codepf))
#define STAMP(i) do {} while (0)
#define NPRE 4
#define EDELAY 12
#define STL 0
#define MDELAY 0
#define MLP1 0
#define NPRE2 10

__global__ void __launch_bounds__(512, 2)
gm_kernel(const char* __restrict__ x, const char* __restrict__ ckv, const char* __restrict__ wq, const char* __restrict__ wuk,
          const char* __restrict__ wuv, const char* __restrict__ wo, const char* __restrict__ w1, const char* __restrict__ w2,
          const char* __restrict__ gw, char* __restrict__ out, char* __restrict__ ws) {
  __shared__ __attribute__((aligned(16))) char smem[L_END];
  asm volatile("" :: "v"((LAS char*)smem));
  STAMP(0);
  const int tid = threadIdx.x, lane = tid & 63;
  const int w = __builtin_amdgcn_readfirstlane(tid >> 6);
  const int b = blockIdx.x, xcd = b & 7, r = b >> 3;
  v4u codepf;
  {
    u64 pc; unsigned csz;
    asm volatile("s_getpc_b64 %0\n.Lgm_pc_here:\n\ts_mov_b32 %1, .Lgm_code_end-.Lgm_pc_here" : "=s"(pc), "=s"(csz));
    unsigned co = (unsigned)(r * 1024 + lane * 16);
    if (w == 7 && co + 16u <= csz) GLD4(codepf, co, (const char*)pc, 0, "");
  }
  const bool roleA = r < 16;
  const int g4 = lane >> 4, n16 = lane & 15;
  const int rr = r & 15;
  const int hq = xcd * 4 + (rr >> 2), j8 = (roleA ? 0 : 4) + (rr & 3);
  asm volatile("" :: "s"(x), "s"(ckv), "s"(wq), "s"(wuk), "s"(wuv), "s"(wo), "s"(w1), "s"(w2), "s"(gw), "s"(out), "s"(ws));
  unsigned msk[4];
#pragma unroll
  for (int t = 0; t < 4; ++t) msk[t] = (((n16 & 7) >> 1) == t) ? ((n16 & 1) ? 0xFFFF0000u : 0x0000FFFFu) : 0u;

#define B_CLEARS(G_, S_, BI_) \
    { v4u s_ = {SENT, SENT, SENT, SENT}; \
      _Pragma("unroll") for (int i = 0; i < 3; ++i) { const int hh = 3 * (w - 5) + i; if (hh < 8) GST4(s_, (unsigned)(OFF_PP + ((8 * (G_) + hh) * 32 + (S_)) * 1024 + lane * 16), ws, "sc1"); } \
      if (w == 7) { \
        GST4(s_, (unsigned)(OFF_MP + (BI_) * 1280 + lane * 16), ws, "sc1"); \
        unsigned vo = (unsigned)(OFF_MP + (BI_) * 1280 + 1024 + lane * 16); \
        if (!STL && lane >= 16) vo = (unsigned)(OFF_ST + ((G_) * 32 + (S_)) * 64 + (lane - 16) * 16); \
        if (lane < (STL ? 16 : 20)) GST4(s_, vo, ws, "sc1"); \
        if (STL) { v2u s2_ = {SENT, SENT}; if (lane < 8) GST2(s2_, (unsigned)(OFF_ST + ((8 * (G_) + lane) * 32 + (S_)) * 8), ws, "sc1"); } } }
#define CLR_EARLY 1
  const int sB = xcd * 4 + (rr >> 2), gB = rr & 3;
  v4u wqr[4]; v2u xr, ew;
  STAMP(15);
  if (w == 0) {
    GLD2(ew, 0u, ws, OFF_E, "sc1");
    v4u s_ = {SENT, SENT, SENT, SENT};
    unsigned vo = (unsigned)(OFF_QP + (hq * 8 + j8) * 256 + n16 * 16);
    if (roleA && g4 == 1) vo = (unsigned)(OFF_QA + hq * 1024 + j8 * 256 + n16 * 16);
    if (lane < (roleA ? 32 : 16)) GST4(s_, vo, ws, "sc1");
  }
  if (w < 5) {
    const char* wb = wq + (size_t)(j8 * 80 + 16 * w) * 8192 + hq * 256;
    const unsigned vo = (unsigned)(4 * g4 * 8192 + n16 * 16);
    GLD2(xr, (unsigned)(8 * g4), x + (j8 * 80 + 16 * w) * 2, 0, "");
    GLD4(wqr[0], vo, wb, 0, ""); GLD4(wqr[1], vo, wb + 8192, 0, ""); GLD4(wqr[2], vo, wb + 16384, 0, ""); GLD4(wqr[3], vo, wb + 24576, 0, "");
  }
  if (CLR_EARLY == 2 && !roleA && w >= 5) B_CLEARS(gB, sB, xcd * 16 + rr);
  STAMP(11);
  RAWBAR();
  STAMP(1);
  const int kt = (w + 2 * gB) & 7;
  const char* kb = ckv + (size_t)(128 * sB + 16 * kt) * 1024;
  const unsigned kl = L_CKV + (16 * kt) * 1024;
#define CKV_ROWS(I0, I1) \
  _Pragma("unroll") for (int i = I0; i < I1; ++i) dma16((unsigned)((lane ^ i) * 16), kb + i * 1024, kl + i * 1024);
  if (roleA) {
    const char* ub = wuk + (size_t)(128 * j8 + 16 * w) * 8192 + hq * 256;
#pragma unroll
    for (int i = 0; i < 4; ++i)
      dma16((unsigned)((4 * i + g4) * 8192 + ((n16 ^ (4 * i + g4)) * 16)), ub, (unsigned)(L_UK + (16 * w + 4 * i) * 256));
  } else if (w >= 5) {
    if (EDELAY) __builtin_amdgcn_s_sleep(EDELAY);
    if (CLR_EARLY == 1) B_CLEARS(gB, sB, xcd * 16 + rr);
    CKV_ROWS(0, NPRE2);
  }
  if (w == 0) {
    if (roleA) VMWAIT(9); else VMWAIT(5);
    TIE(ew);
    const u64 e1 = (((u64)ew.y << 32) | ew.x) + 1;
    v2u f = {(unsigned)e1, (unsigned)(e1 >> 32)};
    if (lane == 0) GST2(f, (unsigned)(OFF_FLAG1 + b * 128), ws, "sc1");
    if (lane == 0) { sts2(L_EP, f); sts1(L_OK, 0u); sts1(L_CNT, 0u); }
  }
  if (w < 5) {
    if (w == 0) { if (roleA) VMWAIT(9); else VMWAIT(5); } else { if (roleA) VMWAIT(8); else VMWAIT(4); }
    TIE(xr);
    const unsigned d0 = __builtin_amdgcn_perm(xr.x, xr.x, 0x01000100u), d1 = __builtin_amdgcn_perm(xr.x, xr.x, 0x03020302u);
    const unsigned d2 = __builtin_amdgcn_perm(xr.y, xr.y, 0x01000100u), d3 = __builtin_amdgcn_perm(xr.y, xr.y, 0x03020302u);
    v4u a0 = {d0 & msk[0], d0 & msk[1], d0 & msk[2], d0 & msk[3]}, a1 = {d1 & msk[0], d1 & msk[1], d1 & msk[2], d1 & msk[3]};
    v4u a2 = {d2 & msk[0], d2 & msk[1], d2 & msk[2], d2 & msk[3]}, a3 = {d3 & msk[0], d3 & msk[1], d3 & msk[2], d3 & msk[3]};
    unsigned qr = L_QRED + w * 512 + (8 * n16 + 4 * g4) * 4;
    TIE(a0); TIE(a1); TIE(a2); TIE(a3); TIE(qr);
    if (w == 0) { if (roleA) VMWAIT(5); else VMWAIT(1); } else { if (roleA) VMWAIT(4); else VMWAIT(0); }
    TIE(wqr[0]); TIE(wqr[1]); TIE(wqr[2]); TIE(wqr[3]);
    STAMP(2);
    f4 acc = {0.f, 0.f, 0.f, 0.f};
    acc = mfma(a0, wqr[0], acc); acc = mfma(a1, wqr[1], acc); acc = mfma(a2, wqr[2], acc); acc = mfma(a3, wqr[3], acc);
    if (lane < 32) stsf4(qr, acc);
  }
  BARRIER();
  if (w == 0) {
    if (lane < 32) {
      const unsigned ra = L_QRED + lane * 16;
      f4 s0 = ldsf4(ra), s1 = ldsf4(ra + 512), s2 = ldsf4(ra + 1024), s3 = ldsf4(ra + 1536), s4_ = ldsf4(ra + 2048);
      f4 sm = (s0 + s1) + (s2 + s3) + s4_;
      v2u o = {guard(cvtpk(sm.x, sm.y)), guard(cvtpk(sm.z, sm.w))};
      GST2(o, (unsigned)(OFF_QP + (hq * 8 + j8) * 256 + lane * 8), ws, "sc1");
    }
    STAMP(3);
  }
#define EPOCH() ({ v2u e_ = *(LAS v2u*)(size_t)L_EP; (((u64)(unsigned)__builtin_amdgcn_readfirstlane(e_.y)) << 32) | (unsigned)__builtin_amdgcn_readfirstlane(e_.x); })
#define FLAG1_CHECK(BLK) \
  for (unsigned spin = 0;; ++spin) { \
    v2u f0; \
    GLD2(f0, (unsigned)(OFF_FLAG1 + (BLK) * 128), ws, 0, "sc1"); \
    VMWAIT(0); TIE(f0); \
    const unsigned df = (f0.x ^ (unsigned)epoch) | (f0.y ^ (unsigned)(epoch >> 32)); \
    if (__builtin_amdgcn_ballot_w64(df != 0u) == 0ull || spin > SPIN_CAP) break; \
  }
#define FLAG2_CHECK(HALF) \
  for (unsigned spin = 0;; ++spin) { \
    v4u f0; \
    GLD4(f0, (unsigned)(OFF_FLAG2 + (HALF) * 1024 + lane * 16), ws, 0, "sc1"); \
    VMWAIT(0); TIE(f0); \
    const unsigned lo = (unsigned)epoch, hi = (unsigned)(epoch >> 32); \
    const unsigned df = ((f0.x ^ lo) | (f0.y ^ hi)) | ((f0.z ^ lo) | (f0.w ^ hi)); \
    if (__builtin_amdgcn_ballot_w64(df != 0u) == 0ull || spin > SPIN_CAP) break; \
  }
  const u64 epoch = EPOCH();

  if (roleA) {
    const int h = hq, j = j8;
    if (w == 7) { const int j8l = lane & 7; const int blk = (h >> 2) + 8 * ((j8l >> 2) * 16 + (h & 3) * 4 + (j8l & 3)); FLAG1_CHECK(blk); CODEPF_DONE(); STAMP(3); }
    else VMWAIT(0);
    STAMP(12);
    v4u uk[4];
#pragma unroll
    for (int ks = 0; ks < 4; ++ks) uk[ks] = lds4(L_UK + (16 * w + n16) * 256 + (((4 * ks + g4) ^ n16) * 16));
    unsigned qb_a = L_QB + (n16 & 7) * 288 + g4 * 16;
    unsigned qa_o = (unsigned)(OFF_QA + h * 1024 + (128 * j + 16 * w + 4 * g4) * 2);
    TIE(qb_a); TIE(qa_o);
    if (w == 7) {
      v4u p0, p1;
      const unsigned vo = (unsigned)(OFF_QP + h * 2048 + lane * 16);
      unsigned wa = L_QB + g4 * 288 + n16 * 16; TIE(wa);
      for (unsigned spin = 0;; ++spin) {
        GLD4(p0, vo, ws, 0, "sc1"); GLD4(p1, vo, ws, 1024, "sc1");
        VMWAIT(0); TIE(p0); TIE(p1);
        if (allvalid(umax(mx4(p0), mx4(p1))) || spin > SPIN_CAP) break;
      }
      STAMP(4);
      sts4(wa, p0); sts4(wa + 4 * 288, p1);
    }
    BARRIER();
    {
      v4u bq[4];
#pragma unroll
      for (int ks = 0; ks < 4; ++ks) bq[ks] = lds4(qb_a + ks * 64);
      SCHED();
      f4 acc = {0.f, 0.f, 0.f, 0.f};
#pragma unroll
      for (int ks = 0; ks < 4; ++ks) acc = mfma(uk[ks], bq[ks], acc);
      float v0 = acc.x, v1 = acc.y, v2 = acc.z, v3 = acc.w;
      v0 += dpp<XOR1>(v0); v1 += dpp<XOR1>(v1); v2 += dpp<XOR1>(v2); v3 += dpp<XOR1>(v3);
      v0 += dpp<XOR2>(v0); v1 += dpp<XOR2>(v1); v2 += dpp<XOR2>(v2); v3 += dpp<XOR2>(v3);
      v0 += dpp<HMIR>(v0); v1 += dpp<HMIR>(v1); v2 += dpp<HMIR>(v2); v3 += dpp<HMIR>(v3);
      v2u o = {guard(cvtpk(v0, v1)), guard(cvtpk(v2, v3))};
      if (n16 == 0) GST2(o, qa_o, ws, "sc1");
    }
    STAMP(5);
    RAWBAR();
    const int ca = j & 1, db = j >> 1;
    if (w == 6) {
      v4u s_ = {SENT, SENT, SENT, SENT};
      GST4(s_, (unsigned)(OFF_OP + (h * 4 + j) * 1280 + lane * 16), ws, "sc1");
      if (lane < 16) GST4(s_, (unsigned)(OFF_OP + (h * 4 + j) * 1280 + 1024 + lane * 16), ws, "sc1");
    }
    {
      const char* vb = wuv + (size_t)(256 * ca + 32 * w) * 8192 + h * 256 + db * 128;
#pragma unroll
      for (int i = 0; i < 4; ++i)
        dma16((unsigned)((8 * i + (lane >> 3)) * 8192 + (((lane & 7) ^ ((((lane >> 4) & 1) << 1) | ((i & 1) << 2))) * 16)), vb, (unsigned)(L_WUV + (32 * w + 8 * i) * 128));
    }
    unsigned ovo[5];
    const char* ob = wo + (size_t)(h * 128 + 64 * db) * 1280;
#pragma unroll
    for (int i = 0; i < 5; ++i) {
      const unsigned n = 64u * (unsigned)(w + 8 * i) + (unsigned)lane, row = (n * 52429u) >> 22, pos = n - row * 80u;
      ovo[i] = row * 1280u + ((pos ^ (((row & 3u) << 1) | (((row >> 3) & 1u) << 3))) * 16u);
    }
    RAWBAR();
#pragma unroll
    for (int i = 0; i < 5; ++i) dma16(ovo[i], ob + ca * 40960, (unsigned)(L_WO + ca * 40960 + (w + 8 * i) * 1024));
    if (w == 6) {
      VMWAIT(9);
      if (lane == 0) { v2u f = {(unsigned)epoch, (unsigned)(epoch >> 32)}; GST2(f, (unsigned)(OFF_FLAG2 + b * 8), ws, "sc1"); }
    }
    unsigned pvo = (unsigned)(OFF_PP + (h * 32 + n16) * 1024 + 512 * ca + 64 * w + 16 * g4), pvo2 = pvo + 16384u;
    unsigned sto = (unsigned)(STL ? OFF_ST + (h * 32 + (lane & 31)) * 8 : OFF_ST + ((h >> 3) * 32 + (lane & 31)) * 64 + (h & 7) * 8);
    unsigned cw_w = L_CRED + w * 160 + lane * 2, cw_r = L_CRED + n16 * 160 + g4 * 16;
    unsigned opo = (unsigned)(OFF_OP + (h * 4 + j) * 1280 + 160 * w + 32 * g4 + 2 * n16);
    unsigned bva[4], woa[5];
    {
      const int q = (lane & 15) >> 2, p = lane & 3;
#pragma unroll
      for (int dt = 0; dt < 4; ++dt)
        bva[dt] = L_WUV + (32 * w + 8 * g4 + q) * 128 + (((2 * dt + (p >> 1)) ^ (((q >> 1) << 1) | ((g4 & 1) << 2))) * 16) + 8 * (p & 1);
#pragma unroll
      for (int t = 0; t < 5; ++t) {
        woa[t] = L_WO + (8 * g4 + q) * 1280 + (((10 * w + 2 * t + (p >> 1)) ^ ((q << 1) | ((g4 & 1) << 3))) * 16) + 8 * (p & 1);
      }
    }
    TIE(pvo); TIE(pvo2); TIE(sto); TIE(cw_w); TIE(cw_r); TIE(opo);
#pragma unroll
    for (int t = 0; t < 5; ++t) TIE(woa[t]);
    if (w == 6) VMWAIT(6); else VMWAIT(5);
    STAMP(13);
    v4u bv[4];
#pragma unroll
    for (int dt = 0; dt < 4; ++dt) bv[dt] = ldstr(bva[dt], bva[dt] + 512);
    if (w == 7) { FLAG2_CHECK(1); }
    VMWAIT(0);
    STAMP(6);
    BARRIER();
    const unsigned wko = (unsigned)ca * 40960u, wkx = 40960u - wko;
    v4u wa0[5], wa1[5];
    v2u stw;
    if (w == 0) {
      v2u st;
      GLD2(st, sto, ws, 0, "sc1");
#pragma unroll
      for (int t = 0; t < 5; ++t) wa0[t] = ldstr(woa[t] + wko, woa[t] + wko + 5120);
      SCHED();
      VMWAIT(0); TIE(st);
      for (unsigned spin = 0; spin < SPIN_CAP && !allvalid(umax(st.x, st.y)); ++spin) {
        GLD2(st, sto, ws, 0, "sc1");
        VMWAIT(0); TIE(st);
      }
      STAMP(14);
      if (lane == 0) sts1(L_OK, 2u);
      stw = st;
    } else {
#pragma unroll
      for (int t = 0; t < 5; ++t) wa0[t] = ldstr(woa[t] + wko, woa[t] + wko + 5120);
      SCHED();
      for (unsigned spin = 0; *(volatile LAS unsigned*)(size_t)L_OK < 2u && spin < 64u * SPIN_CAP; ++spin) __builtin_amdgcn_s_sleep(1);
    }
    v4u pa[2];
    GLD4(pa[0], pvo, ws, 0, "sc1"); GLD4(pa[1], pvo2, ws, 0, "sc1");
    RAWBAR();
#pragma unroll
    for (int i = 0; i < 5; ++i) dma16(ovo[i], ob + wkx, (unsigned)(L_WO + wkx + (w + 8 * i) * 1024));
    STAMP(7);
    if (w == 0) {
      float m = asf(stw.x), l = asf(stw.y);
      float M = p16_max(row_max(m));
      float a = __builtin_amdgcn_exp2f((m - M) * SC);
      float Ls = p16_sum(row_sum(a * l));
      a = a * __builtin_amdgcn_rcpf(Ls);
      if (lane < 32) sts1(L_AW + lane * 4, asu(a));
      LGWAIT();
      if (lane == 0) sts1(L_OK, 3u);
    }
    for (unsigned spin = 0; *(volatile LAS unsigned*)(size_t)L_OK < 3u && spin < 64u * SPIN_CAP; ++spin) __builtin_amdgcn_s_sleep(1);
    f4 aw[2];
    aw[0] = ldsf4(L_AW + g4 * 16); aw[1] = ldsf4(L_AW + 64 + g4 * 16);
    VMWAIT(5); TIE(pa[0]); TIE(pa[1]);
    if (__builtin_expect(!allvalid(umax(mx4(pa[0]), mx4(pa[1]))), 0)) {
      v4u pb0, pb1;
      for (unsigned spin = 0;; ++spin) {
        GLD4(pb0, pvo, ws, 0, "sc1"); GLD4(pb1, pvo2, ws, 0, "sc1");
        VMWAIT(0); TIE(pb0); TIE(pb1);
        if (allvalid(umax(mx4(pb0), mx4(pb1))) || spin > SPIN_CAP) break;
      }
      pa[0] = pb0; pa[1] = pb1;
    }
    STAMP(8);
    {
      const f4 z = {0.f, 0.f, 0.f, 0.f};
      f4 t0_[4], t1_[4];
#pragma unroll
      for (int dt = 0; dt < 4; ++dt) { t0_[dt] = mfma(pa[0], bv[dt], z); t1_[dt] = mfma(pa[1], bv[dt], z); }
      SCHED();
      float u[4];
#pragma unroll
      for (int dt = 0; dt < 4; ++dt) { f4 c = aw[0] * t0_[dt] + aw[1] * t1_[dt]; u[dt] = (c.x + c.y) + (c.z + c.w); }
      auto t0 = __builtin_amdgcn_permlane16_swap(asu(u[0]), asu(u[1]), false, false); auto t1 = __builtin_amdgcn_permlane16_swap(asu(u[2]), asu(u[3]), false, false);
      unsigned x0 = t0[0], x1 = t0[1], y0 = t1[0], y1 = t1[1];
      auto t2 = __builtin_amdgcn_permlane32_swap(asu(asf(x0) + asf(x1)), asu(asf(y0) + asf(y1)), false, false);
      unsigned z0 = t2[0], z1 = t2[1];
      const float c = asf(z0) + asf(z1);
      const unsigned hi = cvtpk(c, c);
      const float rs = c - asf(hi << 16);
      const unsigned lo = cvtpk(rs, rs);
      *(LAS unsigned short*)(size_t)cw_w = (unsigned short)hi;
      *(LAS unsigned short*)(size_t)(cw_w + 8 * 160) = (unsigned short)lo;
    }
    VMWAIT(0);
    BARRIER();
    STAMP(9);
    {
      const v4u ca0 = lds4(cw_r + ca * 64), ca1 = lds4(cw_r + (1 - ca) * 64);
#pragma unroll
      for (int t = 0; t < 5; ++t) wa1[t] = ldstr(woa[t] + wkx, woa[t] + wkx + 5120);
      SCHED();
      const f4 z = {0.f, 0.f, 0.f, 0.f};
      f4 d[5];
#pragma unroll
      for (int t = 0; t < 5; ++t) d[t] = mfma(ca0, wa0[t], z);
#pragma unroll
      for (int t = 0; t < 5; ++t) d[t] = mfma(ca1, wa1[t], d[t]);
      SCHED();
      float v[5];
#pragma unroll
      for (int t = 0; t < 5; ++t) v[t] = (d[t].x + d[t].y) + (d[t].z + d[t].w);
      auto t0 = __builtin_amdgcn_permlane16_swap(asu(v[0]), asu(v[1]), false, false); auto t1 = __builtin_amdgcn_permlane16_swap(asu(v[2]), asu(v[3]), false, false);
      unsigned x0 = t0[0], x1 = t0[1], y0 = t1[0], y1 = t1[1];
      auto t2 = __builtin_amdgcn_permlane32_swap(asu(asf(x0) + asf(x1)), asu(asf(y0) + asf(y1)), false, false);
      unsigned z0 = t2[0], z1 = t2[1];
      const float va = asf(z0) + asf(z1), vb = p32_sum(p16_sum(v[4]));
      const unsigned pa_ = cvtpk(va, dpp<XOR1>(va)), pb_ = cvtpk(vb, dpp<XOR1>(vb));
      v4u oa = {guard(pa_), guard(dppu<0x102>(pa_)), guard(dppu<0x104>(pa_)), guard(dppu<0x106>(pa_))};
      v4u ob_ = {guard(pb_), guard(dppu<0x102>(pb_)), guard(dppu<0x104>(pb_)), guard(dppu<0x106>(pb_))};
      if ((n16 & 7) == 0) GST4(oa, opo, ws, "sc1");
      if ((n16 & 7) == 0 && g4 == 0) GST4(ob_, opo + 128u, ws, "sc1");
    }
    STAMP(10);
  } else {
    const int g = rr & 3, s = xcd * 4 + (rr >> 2), bi = xcd * 16 + rr;
    RAWBAR();
    if (w < 5) {
      CKV_ROWS(0, 16);
    } else {
      CKV_ROWS(NPRE2, 16);
      v2u f1;
      unsigned f1o;
      { const int hd = 8 * g + ((lane & 31) >> 2); f1o = (unsigned)(OFF_FLAG1 + ((hd >> 2) + 8 * ((hd & 3) * 4 + (lane & 3))) * 128); }
      if (w == 7) GLD2(f1, f1o, ws, 0, "sc1");
      if (w == 5) { dma16((unsigned)(lane * 16), x, L_QB); if (lane < 16) dma16((unsigned)(lane * 16), x + 1024, L_QB + 1024); }
      if (w == 5) VMWAIT(18); else if (w == 6) VMWAIT(16); else VMWAIT(17);
      unsigned old = 0;
      if (lane == 0) old = __hip_atomic_fetch_add((LAS unsigned*)(size_t)L_CNT, 1u, __ATOMIC_RELAXED, __HIP_MEMORY_SCOPE_WORKGROUP);
      if (lane == 0 && old == 2u) { v2u f = {(unsigned)epoch, (unsigned)(epoch >> 32)}; GST2(f, (unsigned)(OFF_FLAG2 + b * 8), ws, "sc1"); }
      if (w == 7) {
        VMWAIT(0); TIE(f1);
        for (unsigned spin = 0; spin < SPIN_CAP; ++spin) {
          const unsigned df = (f1.x ^ (unsigned)epoch) | (f1.y ^ (unsigned)(epoch >> 32));
          if (__builtin_amdgcn_ballot_w64(df != 0u) == 0ull) break;
          GLD2(f1, f1o, ws, 0, "sc1");
          VMWAIT(0); TIE(f1);
        }
        CODEPF_DONE();
        if (lane == 0) sts1(L_OK, 1u);
      }
    }
    unsigned qvo = (unsigned)(OFF_QA + (8 * g + w) * 1024 + lane * 16);
    unsigned qsw = L_QAS + w * 1056 + lane * 16, qsr = L_QAS + (n16 & 7) * 1056 + g4 * 16;
    unsigned mxw = L_MX + lane * 32 + w * 4, mxr = L_MX + (n16 & 7) * 32;
    unsigned pw = L_P + n16 * 288 + (16 * kt + 4 * g4) * 2, pr = L_P + (n16 & 7) * 288 + g4 * 16;
    unsigned ppo = (unsigned)(OFF_PP + ((8 * g + (n16 & 7)) * 32 + s) * 1024 + (64 * w + 32 * (g4 >> 1) + 8 * (2 * (n16 >> 3) + (g4 & 1))) * 2);
    unsigned ka[4], ta[4][2];
    {
      const unsigned kr = L_CKV + (16 * kt + n16) * 1024;
#pragma unroll
      for (int u = 0; u < 4; ++u) ka[u] = kr + (((4 * u + g4) ^ n16) * 16);
      const int q = (lane & 15) >> 2, p = lane & 3;
#pragma unroll
      for (int t = 0; t < 4; ++t) {
        const int ch = 8 * w + 4 * (p >> 1) + t;
        const int r0 = 8 * g4 + q, r1 = r0 + 4;
        ta[t][0] = L_CKV + r0 * 1024 + ((ch ^ (r0 & 15)) * 16) + 8 * (p & 1);
        ta[t][1] = L_CKV + r1 * 1024 + ((ch ^ (r1 & 15)) * 16) + 8 * (p & 1);
      }
    }
    TIE(qvo); TIE(qsw); TIE(qsr); TIE(mxw); TIE(mxr); TIE(pw); TIE(pr); TIE(ppo);
#pragma unroll
    for (int u = 0; u < 4; ++u) { TIE(ka[u]); TIE(ta[u][0]); TIE(ta[u][1]); }
    VMWAIT(0);
    STAMP(4);
    for (unsigned spin = 0; *(volatile LAS unsigned*)(size_t)L_OK == 0u && spin < SPIN_CAP; ++spin) __builtin_amdgcn_s_sleep(1);
    v4u qa;
    GLD4(qa, qvo, ws, 0, "sc1");
    v4u kA[16];
#pragma unroll
    for (int ks = 0; ks < 16; ++ks) kA[ks] = lds4(ka[ks & 3] + (ks >> 2) * 256);
    BARRIER();
    STAMP(5);
    v4u vA[4][4];
#pragma unroll
    for (int t = 0; t < 4; ++t) {
      vA[0][t] = ldstr(ta[t][0], ta[t][1]); vA[1][t] = ldstr(ta[t][0] + 32768, ta[t][1] + 32768);
      const unsigned b0 = ta[t][0] + 65536, b1 = ta[t][1] + 65536;
      vA[2][t] = ldstr(b0, b1); vA[3][t] = ldstr(b0 + 32768, b1 + 32768);
    }
    SCHED();
    VMWAIT(0); TIE(qa);
    if (__builtin_expect(!allvalid(mx4(qa)), 0)) {
      v4u qb;
      for (unsigned spin = 0;; ++spin) {
        GLD4(qb, qvo, ws, 0, "sc1");
        VMWAIT(0); TIE(qb);
        if (allvalid(mx4(qb)) || spin > SPIN_CAP) break;
      }
      qa = qb;
    }
    STAMP(6);
    sts4(qsw, qa);
    BARRIER();
    const int fb = bi < 108 ? bi : 107;
    const char* b1 = w1 + fb * 32;
    const unsigned vo1 = (unsigned)((lane >> 1) * 3456 + (lane & 1) * 16);
    const int pq = 5 * (bi & 3);
    f4 sc = {0.f, 0.f, 0.f, 0.f};
    {
      v4u bq[16];
#pragma unroll
      for (int ks = 0; ks < 16; ++ks) bq[ks] = lds4(qsr + ks * 64);
      const char* b2 = w2 + (size_t)(16 * fb) * 1280 + w * 1024;
      const unsigned vo2 = (unsigned)(lane * 16);
      const int p0 = pq + w;
      SCHED();
#pragma unroll
      for (int ks = 0; ks < 16; ++ks) {
        sc = mfma(kA[ks], bq[ks], sc);
        if (MLP1 && ks == 2) { SCHED(); if (w < 5) dma16(vo1, b1 + (size_t)(32 * p0) * 3456, L_W1 + p0 * 1024); SCHED(); }
        if (MLP1 && ks == 5) { SCHED(); dma16(vo2, b2, L_W2 + w * 1024); SCHED(); }
        if (MLP1 && ks == 8) { SCHED(); dma16(vo2, b2 + 8192, L_W2 + (w + 8) * 1024); SCHED(); }
        if (MLP1 && ks == 11) { SCHED(); if (w < 4) dma16(vo2, b2 + 16384, L_W2 + (w + 16) * 1024); SCHED(); }
      }
      float mw = p32_max(p16_max(fmx(fmx(sc.x, sc.y), fmx(sc.z, sc.w))));
      if (lane < 8) sts1(mxw, asu(mw));
    }
    BARRIER();
    STAMP(7);
    float Mraw;
    {
      Mraw = max8(ldsf4(mxr), ldsf4(mxr + 16));
      const float Ms = -Mraw * SC;
      float p0 = __builtin_amdgcn_exp2f(fmaf(sc.x, SC, Ms)), p1 = __builtin_amdgcn_exp2f(fmaf(sc.y, SC, Ms)), p2 = __builtin_amdgcn_exp2f(fmaf(sc.z, SC, Ms)), p3 = __builtin_amdgcn_exp2f(fmaf(sc.w, SC, Ms));
      v2u pk = {cvtpk(p0, p1), cvtpk(p2, p3)};
      if (n16 < 8) sts2(pw, pk);
      float ls = p32_sum(p16_sum((p0 + p1) + (p2 + p3)));
      if (lane < 8) sts1(L_LS + lane * 32 + w * 4, asu(ls));
    }
    BARRIER();
    STAMP(8);
    {
      v4u pb[4];
#pragma unroll
      for (int ks = 0; ks < 4; ++ks) pb[ks] = lds4(pr + ks * 64);
      if (w == 0) {
        const unsigned sa = L_LS + (lane & 7) * 32;
        v2u o = {guard(asu(Mraw)), guard(asu(sum8(ldsf4(sa), ldsf4(sa + 16))))};
        if (lane < 8) GST2(o, (unsigned)(STL ? OFF_ST + ((8 * g + lane) * 32 + s) * 8 : OFF_ST + (g * 32 + s) * 64 + lane * 8), ws, "sc1");
        STAMP(14);
      }
      SCHED();
      f4 ac[4];
#pragma unroll
      for (int t = 0; t < 4; ++t) ac[t] = mfma(vA[0][t], pb[0], f4{0.f, 0.f, 0.f, 0.f});
#pragma unroll
      for (int ks = 1; ks < 4; ++ks)
#pragma unroll
        for (int t = 0; t < 4; ++t) ac[t] = mfma(vA[ks][t], pb[ks], ac[t]);
      const bool up = n16 >= 8;
      f4 xa = up ? ac[2] : ac[0], ya = up ? ac[3] : ac[1];
      unsigned x0 = cvtpk(xa.x, xa.y), x1 = cvtpk(xa.z, xa.w), y0 = cvtpk(ya.x, ya.y), y1 = cvtpk(ya.z, ya.w);
      auto s0 = __builtin_amdgcn_permlane16_swap(x0, y0, false, false); auto s1 = __builtin_amdgcn_permlane16_swap(x1, y1, false, false);
      unsigned e0 = s0[0], e1 = s1[0], f0 = s0[1], f1 = s1[1];
      v4u o = {guard(e0), guard(e1), guard(f0), guard(f1)};
      GST4(o, ppo, ws, "sc1");
    }
    STAMP(9);
    const bool fin = bi < 80 && w == 5;
    v4u ff0, ff1; unsigned gg[5];
    if (fin) {
      GLD4(ff0, (unsigned)(OFF_FLAG2 + lane * 16), ws, 0, "sc1"); GLD4(ff1, (unsigned)(OFF_FLAG2 + 1024 + lane * 16), ws, 0, "sc1");
#pragma unroll
      for (int i = 0; i < 5; ++i) GLD1(gg[i], (unsigned)(lane * 4), gw, i * 256, "");
    }
    RAWBAR();
    if (MDELAY) __builtin_amdgcn_s_sleep(MDELAY);
    int pc0 = pq + 5 + w, pc1 = pq + 13 + w;
    if (pc0 >= 20) pc0 -= 20;
    if (pc1 >= 20) pc1 -= 20;
    if (!MLP1) {
      const char* b2 = w2 + (size_t)(16 * fb) * 1280 + w * 1024;
      const unsigned vo2 = (unsigned)(lane * 16);
      if (w < 5) dma16(vo1, b1 + (size_t)(32 * (pq + w)) * 3456, L_W1 + (pq + w) * 1024);
      dma16(vo2, b2, L_W2 + w * 1024); dma16(vo2, b2 + 8192, L_W2 + (w + 8) * 1024); if (w < 4) dma16(vo2, b2 + 16384, L_W2 + (w + 16) * 1024);
      RAWBAR();
    }
    dma16(vo1, b1 + (size_t)(32 * pc0) * 3456, L_W1 + pc0 * 1024);
    if (w < 7) dma16(vo1, b1 + (size_t)(32 * pc1) * 3456, L_W1 + pc1 * 1024);
    VMWAIT(0);
    STAMP(12);
    {
      const int q = (lane & 15) >> 2, p = lane & 3;
      const unsigned wa = L_W1 + (8 * g4 + q) * 32 + 8 * p, xa = L_QB + g4 * 16;
      f4 hacc = {0.f, 0.f, 0.f, 0.f};
      hacc = mfma(lds4(xa + pc0 * 64), ldstr(wa + pc0 * 1024, wa + pc0 * 1024 + 128), hacc);
      if (w < 7) hacc = mfma(lds4(xa + pc1 * 64), ldstr(wa + pc1 * 1024, wa + pc1 * 1024 + 128), hacc);
      if (w < 5) { const int p0 = pq + w; hacc = mfma(lds4(xa + p0 * 64), ldstr(wa + p0 * 1024, wa + p0 * 1024 + 128), hacc); }
      if (lane < 16) sts1(L_RED + lane * 32 + w * 4, asu(hacc.x));
    }
    BARRIER();
    STAMP(15);
    if (w < 5) {
      v4u w2r[4];
#pragma unroll
      for (int i = 0; i < 4; ++i) w2r[i] = lds4(L_W2 + (4 * g4 + i) * 1280 + w * 256 + n16 * 16);
      const int f = lane & 15;
      float hh = sum8(ldsf4(L_RED + f * 32), ldsf4(L_RED + f * 32 + 16));
      hh = hh * __builtin_amdgcn_rcpf(1.f + __expf(-hh));
      if (bi >= 108) hh = 0.f;
      unsigned hi = cvtpk(hh, hh);
      float rs = hh - asf(hi << 16);
      unsigned lo = cvtpk(rs, rs);
      if (lane < 32) sts1(L_TAB + w * 512 + lane * 4, lane < 16 ? hi : lo);
      LGWAIT();
      v4u t0 = lds4(L_TAB + w * 512 + ((n16 >> 3) * 16 + 4 * g4) * 4);
      unsigned dv[4] = {t0.x, t0.y, t0.z, t0.w};
      f4 acc = {0.f, 0.f, 0.f, 0.f};
#pragma unroll
      for (int i = 0; i < 4; ++i) { v4u a = {dv[i] & msk[0], dv[i] & msk[1], dv[i] & msk[2], dv[i] & msk[3]}; acc = mfma(a, w2r[i], acc); }
      float o0 = p32_sum(acc.x), o1 = p32_sum(acc.y), o2 = p32_sum(acc.z), o3 = p32_sum(acc.w);
      unsigned a0 = guard(cvtpk(o0, o1)), a1 = guard(cvtpk(o2, o3));
      auto s0 = __builtin_amdgcn_permlane16_swap(a0, a0, false, false); auto s1 = __builtin_amdgcn_permlane16_swap(a1, a1, false, false);
      unsigned b0 = s0[1], b1 = s1[1];
      v4u o = {a0, a1, b0, b1};
      if (lane < 16) GST4(o, (unsigned)(OFF_MP + bi * 1280 + w * 256 + lane * 16), ws, "sc1");
    }
    STAMP(10);
    if (fin) {
      TIE(ff0); TIE(ff1);
#pragma unroll
      for (int i = 0; i < 5; ++i) TIE(gg[i]);
      {
        const unsigned lo = (unsigned)epoch, hi = (unsigned)(epoch >> 32);
        const unsigned df = ((ff0.x ^ lo) | (ff0.y ^ hi)) | ((ff0.z ^ lo) | (ff0.w ^ hi)) | ((ff1.x ^ lo) | (ff1.y ^ hi)) | ((ff1.z ^ lo) | (ff1.w ^ hi));
        if (__builtin_amdgcn_ballot_w64(df != 0u) != 0ull) { FLAG2_CHECK(0); FLAG2_CHECK(1); }
      }
      float gate;
      {
        float a = 0.f;
#pragma unroll
        for (int i = 0; i < 5; ++i) { const unsigned gx = *(LAS unsigned*)(size_t)(L_QB + i * 256 + lane * 4); a += asf(gx << 16) * asf(gg[i] << 16) + asf(gx & 0xFFFF0000u) * asf(gg[i] & 0xFFFF0000u); }
        a = p32_sum(p16_sum(row_sum(a)));
        gate = __builtin_amdgcn_rcpf(1.f + __expf(-a));
      }
      const unsigned one = 0x3F803F80u;
      v4u ones = {one & msk[0], one & msk[1], one & msk[2], one & msk[3]};
      v4u m0, m1;
      const unsigned mvo = (unsigned)(OFF_MP + lane * 1280 + bi * 16), mvo2 = mvo + 64 * 1280;
      for (unsigned spin = 0;; ++spin) {
        GLD4(m0, mvo, ws, 0, "sc1"); GLD4(m1, mvo2, ws, 0, "sc1");
        VMWAIT(0); TIE(m0); TIE(m1);
        if (allvalid(umax(mx4(m0), mx4(m1))) || spin > SPIN_CAP) break;
      }
      f4 bb = {0.f, 0.f, 0.f, 0.f};
      bb = mfma(ones, m0, bb); bb = mfma(ones, m1, bb);
      bb *= (1.f - gate);
      STAMP(11);
      const unsigned ovo = (unsigned)(OFF_OP + lane * 1280 + bi * 16), ovo2 = ovo + 64 * 1280;
      for (unsigned spin = 0;; ++spin) {
        GLD4(m0, ovo, ws, 0, "sc1"); GLD4(m1, ovo2, ws, 0, "sc1");
        VMWAIT(0); TIE(m0); TIE(m1);
        if (allvalid(umax(mx4(m0), mx4(m1))) || spin > SPIN_CAP) break;
      }
      STAMP(12);
      f4 aa = {0.f, 0.f, 0.f, 0.f};
      aa = mfma(ones, m0, aa); aa = mfma(ones, m1, aa);
      float r0 = row_sum(fmaf(gate, aa.x, bb.x)), r1 = row_sum(fmaf(gate, aa.y, bb.y));
      float r2 = row_sum(fmaf(gate, aa.z, bb.z)), r3 = row_sum(fmaf(gate, aa.w, bb.w));
      v2u o = {cvtpk(r0, r1), cvtpk(r2, r3)};
      if (n16 == 0 && g4 < 2) GST2(o, (unsigned)(bi * 16 + g4 * 8), out, "");
      if (bi == 0 && lane == 0) {
        v2u f = {(unsigned)epoch, (unsigned)(epoch >> 32)};
        GST2(f, (unsigned)OFF_E, ws, "sc1");
      }
      STAMP(13);
    }
  }
  asm volatile(".Lgm_code_end:" ::: "memory");
}

extern "C" void kernel_launch(void* const* d_in, const int* in_sizes, int n_in,
                              void* d_out, int out_size, void* d_ws, size_t ws_size,
                              hipStream_t stream) {
  gm_kernel<<<256, 512, 0, stream>>>((const char*)d_in[0], (const char*)d_in[1], (const char*)d_in[2], (const char*)d_in[3],
                                      (const char*)d_in[4], (const char*)d_in[5], (const char*)d_in[7], (const char*)d_in[8],
                                      (const char*)d_in[9], (char*)d_out, (char*)d_ws);
}
```

```cpp
#include <hip/hip_runtime.h>
#include <stdint.h>
#pragma clang diagnostic ignored "-Winline-asm"

typedef unsigned v4u __attribute__((ext_vector_type(4)));
typedef unsigned v2u __attribute__((ext_vector_type(2)));
typedef float f4 __attribute__((ext_vector_type(4)));
typedef float f2 __attribute__((ext_vector_type(2)));
typedef short s4 __attribute__((ext_vector_type(4)));
typedef short s8 __attribute__((ext_vector_type(8)));
typedef __bf16 bf2 __attribute__((ext_vector_type(2)));
typedef unsigned long long u64;
#define LAS __attribute__((address_space(3)))
#define DI __device__ __forceinline__

constexpr unsigned OFF_E = 0;
constexpr unsigned OFF_FLAG2 = 4096;
constexpr unsigned OFF_FLAG1 = 8192;
constexpr unsigned OFF_QP = OFF_FLAG1 + 32768;
constexpr unsigned OFF_QA = OFF_QP + 65536;
constexpr unsigned OFF_ST = OFF_QA + 32768;
constexpr unsigned OFF_OP = OFF_ST + 8192;
constexpr unsigned OFF_MP = OFF_OP + 163840;
constexpr unsigned OFF_PP = OFF_MP + 163840;
constexpr unsigned OFF_END = OFF_PP + 1048576;
constexpr unsigned OFF_STAMP = 8u << 20;
constexpr unsigned SENT = 0xFFFFFFFFu;
constexpr unsigned SPIN_CAP = 1u << 15;
constexpr float SC = 0.08838834764831845f * 1.4426950408889634f;

constexpr unsigned L_CKV = 0;
constexpr unsigned L_W1 = 0, L_W2 = 32768;
constexpr unsigned L_WUV = 0, L_WO = 32768;
constexpr unsigned L_AW = 131072 + 2560;
constexpr unsigned L_CRED = 131072;
constexpr unsigned L_UK = 32768;
constexpr unsigned L_QAS = 131072;
constexpr unsigned L_QRED = L_QAS;
constexpr unsigned L_P = L_QAS + 8448;
constexpr unsigned L_MX = L_P + 2304;
constexpr unsigned L_LS = L_MX + 256;
constexpr unsigned L_RED = L_LS + 256;
constexpr unsigned L_TAB = L_RED + 1024;
constexpr unsigned L_QB = L_TAB + 4096;
constexpr unsigned L_EP = L_QB + 2304;
constexpr unsigned L_OK = L_EP + 8;
constexpr unsigned L_CNT = L_EP + 12;
constexpr unsigned L_END = L_EP + 16;

DI f4 mfma(v4u a, v4u b, f4 c) { return __builtin_amdgcn_mfma_f32_16x16x32_bf16(__builtin_bit_cast(s8, a), __builtin_bit_cast(s8, b), c, 0, 0, 0); }
DI unsigned cvtpk(float lo, float hi) { f2 x = {lo, hi}; bf2 y = __builtin_convertvector(x, bf2); return __builtin_bit_cast(unsigned, y); }
DI float asf(unsigned u) { return __uint_as_float(u); }
DI unsigned asu(float f) { return __float_as_uint(f); }
DI unsigned guard(unsigned u) { return u < 0xFFFFFFFEu ? u : 0xFFFFFFFEu; }
DI float fmx(float a, float b) { return __builtin_elementwise_maximum(a, b); }
template <int CTRL> DI float dpp(float x) { return asf((unsigned)__builtin_amdgcn_mov_dpp((int)asu(x), CTRL, 0xf, 0xf, true)); }
template <int CTRL> DI unsigned dppu(unsigned x) { return (unsigned)__builtin_amdgcn_mov_dpp((int)x, CTRL, 0xf, 0xf, true); }
constexpr int XOR1 = 0xB1, XOR2 = 0x4E, ROR4 = 0x124, ROR8 = 0x128, HMIR = 0x141;
DI float p16_max(float x) { auto t = __builtin_amdgcn_permlane16_swap(asu(x), asu(x), false, false); unsigned a = t[0], b = t[1]; return fmx(asf(a), asf(b)); }
DI float p32_max(float x) { auto t = __builtin_amdgcn_permlane32_swap(asu(x), asu(x), false, false); unsigned a = t[0], b = t[1]; return fmx(asf(a), asf(b)); }
DI float p16_sum(float x) { auto t = __builtin_amdgcn_permlane16_swap(asu(x), asu(x), false, false); unsigned a = t[0], b = t[1]; return asf(a) + asf(b); }
DI float p32_sum(float x) { auto t = __builtin_amdgcn_permlane32_swap(asu(x), asu(x), false, false); unsigned a = t[0], b = t[1]; return asf(a) + asf(b); }
DI float row_sum(float x) { x += dpp<XOR1>(x); x += dpp<XOR2>(x); x += dpp<ROR4>(x); x += dpp<ROR8>(x); return x; }
DI float row_max(float x) { x = fmx(x, dpp<XOR1>(x)); x = fmx(x, dpp<XOR2>(x)); x = fmx(x, dpp<ROR4>(x)); x = fmx(x, dpp<ROR8>(x)); return x; }
DI float max8(f4 u, f4 v) { return fmx(fmx(fmx(u.x, u.y), fmx(u.z, u.w)), fmx(fmx(v.x, v.y), fmx(v.z, v.w))); }
DI float sum8(f4 u, f4 v) { return ((u.x + u.y) + (u.z + u.w)) + ((v.x + v.y) + (v.z + v.w)); }
DI unsigned umax(unsigned a, unsigned b) { return a > b ? a : b; }
DI unsigned mx4(v4u v) { return umax(umax(v.x, v.y), umax(v.z, v.w)); }
DI bool allvalid(unsigned m) { return __builtin_amdgcn_ballot_w64(m == SENT) == 0ull; }
DI v4u lds4(unsigned a) { return *(LAS v4u*)(size_t)a; }
DI f4 ldsf4(unsigned a) { return *(LAS f4*)(size_t)a; }
DI void sts4(unsigned a, v4u v) { *(LAS v4u*)(size_t)a = v; }
DI void stsf4(unsigned a, f4 v) { *(LAS f4*)(size_t)a = v; }
DI void sts2(unsigned a, v2u v) { *(LAS v2u*)(size_t)a = v; }
DI void sts1(unsigned a, unsigned v) { *(LAS unsigned*)(size_t)a = v; }
DI v4u ldstr(unsigned a0, unsigned a1) {
  s4 lo = __builtin_amdgcn_ds_read_tr16_b64_v4i16((LAS s4*)(size_t)a0), hi = __builtin_amdgcn_ds_read_tr16_b64_v4i16((LAS s4*)(size_t)a1);
  s8 c = __builtin_shufflevector(lo, hi, 0, 1, 2, 3, 4, 5, 6, 7);
  return __builtin_bit_cast(v4u, c);
}

#define GLD4(dst, voff, sbase, IMM, MOD) asm volatile("global_load_dwordx4 %0, %1, %2 offset:%3 " MOD : "=v"(dst) : "v"(voff), "s"(sbase), "i"(IMM) : "memory")
#define GLD2(dst, voff, sbase, IMM, MOD) asm volatile("global_load_dwordx2 %0, %1, %2 offset:%3 " MOD : "=v"(dst) : "v"(voff), "s"(sbase), "i"(IMM) : "memory")
#define GLD1(dst, voff, sbase, IMM, MOD) asm volatile("global_load_dword %0, %1, %2 offset:%3 " MOD : "=v"(dst) : "v"(voff), "s"(sbase), "i"(IMM) : "memory")
#define GST4(src, voff, sbase, MOD) asm volatile("global_store_dwordx4 %0, %1, %2 " MOD "\n\ts_nop 1" :: "v"(voff), "v"(src), "s"(sbase) : "memory")
#define GST2(src, voff, sbase, MOD) asm volatile("global_store_dwordx2 %0, %1, %2 " MOD :: "v"(voff), "v"(src), "s"(sbase) : "memory")
#define SCHED() __builtin_amdgcn_sched_barrier(0)
#define VMWAIT(N) do { asm volatile("s_waitcnt vmcnt(%0)" :: "i"(N) : "memory"); SCHED(); } while (0)
#define LGWAIT() do { asm volatile("s_waitcnt lgkmcnt(0)" ::: "memory"); SCHED(); } while (0)
#define TIE(x) asm volatile("" : "+v"(x))
DI void dma16(unsigned voff, const void* sbase, unsigned ldsdst) {
  asm volatile("s_mov_b32 m0, %2\n\ts_nop 0\n\tglobal_load_lds_dwordx4 %0, %1" :: "v"(voff), "s"(sbase), "s"(ldsdst) : "memory", "m0");
}
#define BARRIER() do { asm volatile("s_waitcnt lgkmcnt(0)\n\ts_barrier" ::: "memory"); SCHED(); } while (0)
#define RAWBAR() do { asm volatile("s_barrier" ::: "memory"); SCHED(); } while (0)

#define CODEPF_DONE() asm volatile("" :: "v"(codepf))
#define STAMP(i) do {} while (0)
#define NPRE 4
#define OV3 1
#define LAY1 0
#define FINPF 0
#define STW 0
#define EDELAY 0
#define EXP_NOMLP 0
#define CANARY 0
#define MLP1 0
#define NPRE2 10

__global__ void __launch_bounds__(512, 2)
gm_kernel(const char* __restrict__ x, const char* __restrict__ ckv, const char* __restrict__ wq, const char* __restrict__ wuk,
          const char* __restrict__ wuv, const char* __restrict__ wo, const char* __restrict__ w1, const char* __restrict__ w2,
          const char* __restrict__ gw, char* __restrict__ out, char* __restrict__ ws) {
  __shared__ __attribute__((aligned(16))) char smem[L_END];
  asm volatile("" :: "v"((LAS char*)smem));
  STAMP(0);
  const int tid = threadIdx.x, lane = tid & 63;
  const int w = __builtin_amdgcn_readfirstlane(tid >> 6);
  const int b = blockIdx.x, xcd = b & 7, r = b >> 3;
  v4u codepf;
  {
    u64 pc; unsigned csz;
    asm volatile("s_getpc_b64 %0\n.Lgm_pc_here:\n\ts_mov_b32 %1, .Lgm_code_end-.Lgm_pc_here" : "=s"(pc), "=s"(csz));
    unsigned co = (unsigned)(r * 1024 + lane * 16);
    if (w == 7 && co + 16u <= csz) GLD4(codepf, co, (const char*)pc, 0, "");
  }
  const bool roleA = r < 16;
  const int g4 = lane >> 4, n16 = lane & 15;
  const int rr = r & 15;
  const int hq = xcd * 4 + (rr >> 2), j8 = (roleA ? 0 : 4) + (rr & 3);
  asm volatile("" :: "s"(x), "s"(ckv), "s"(wq), "s"(wuk), "s"(wuv), "s"(wo), "s"(w1), "s"(w2), "s"(gw), "s"(out), "s"(ws));
  STAMP(14);
  unsigned msk[4];
#pragma unroll
  for (int t = 0; t < 4; ++t) msk[t] = (((n16 & 7) >> 1) == t) ? ((n16 & 1) ? 0xFFFF0000u : 0x0000FFFFu) : 0u;

#define PP_SLOT(G_, S_, W_) ((unsigned)(LAY1 ? OFF_PP + (8 * (G_) + (n16 & 7)) * 32768 + (2 * (W_) + (g4 >> 1)) * 2048 + (S_) * 64 + (2 * (n16 >> 3) + (g4 & 1)) * 16 \
    : OFF_PP + ((8 * (G_) + (n16 & 7)) * 32 + (S_)) * 1024 + (64 * (W_) + 32 * (g4 >> 1) + 8 * (2 * (n16 >> 3) + (g4 & 1))) * 2))
#define B_CLEARS(G_, S_, BI_) \
    { v4u s_ = {SENT, SENT, SENT, SENT}; \
      _Pragma("unroll") for (int i = 0; i < 3; ++i) { const int hh = 3 * (w - 5) + i; if (hh < 8) GST4(s_, LAY1 ? PP_SLOT(G_, S_, hh) : (unsigned)(OFF_PP + ((8 * (G_) + hh) * 32 + (S_)) * 1024 + lane * 16), ws, "sc1"); } \
      if (w == 7) { \
        GST4(s_, (unsigned)(OFF_MP + (BI_) * 1280 + lane * 16), ws, "sc1"); \
        unsigned vo = (unsigned)(OFF_MP + (BI_) * 1280 + 1024 + lane * 16); \
        if (lane >= 16) vo = (unsigned)(OFF_ST + ((G_) * 32 + (S_)) * 64 + (lane - 16) * 16); \
        if (lane < 20) GST4(s_, vo, ws, "sc1"); } }
#define CLR_EARLY 2
  const int sB = xcd * 4 + (rr >> 2), gB = rr & 3;
  v4u wqr[4]; v2u xr, ew;
  STAMP(15);
  if (w == 0) {
    GLD2(ew, 0u, ws, OFF_E, "sc1");
    v4u s_ = {SENT, SENT, SENT, SENT};
    unsigned vo = (unsigned)(OFF_QP + (hq * 8 + j8) * 256 + n16 * 16);
    if (roleA && g4 == 1) vo = (unsigned)(OFF_QA + hq * 1024 + j8 * 256 + n16 * 16);
    if (lane < (roleA ? 32 : 16)) GST4(s_, vo, ws, "sc1");
  }
  if (w < 5) {
    const char* wb = wq + (size_t)(j8 * 80 + 16 * w) * 8192 + hq * 256;
    const unsigned vo = (unsigned)(4 * g4 * 8192 + n16 * 16);
    GLD2(xr, (unsigned)(8 * g4), x + (j8 * 80 + 16 * w) * 2, 0, "");
    GLD4(wqr[0], vo, wb, 0, ""); GLD4(wqr[1], vo, wb + 8192, 0, ""); GLD4(wqr[2], vo, wb + 16384, 0, ""); GLD4(wqr[3], vo, wb + 24576, 0, "");
  }
  if (CLR_EARLY == 2 && !roleA && w >= 5) B_CLEARS(gB, sB, xcd * 16 + rr);
  STAMP(11);
  RAWBAR();
  STAMP(1);
  const int kt = (w + 2 * gB) & 7;
  const char* kb = ckv + (size_t)(128 * sB + 16 * kt) * 1024;
  const unsigned kl = L_CKV + (16 * kt) * 1024;
#define CKV_ROWS(I0, I1) \
  _Pragma("unroll") for (int i = I0; i < I1; ++i) dma16((unsigned)((lane ^ i) * 16), kb + i * 1024, kl + i * 1024);
  if (roleA) {
    const char* ub = wuk + (size_t)(128 * j8 + 16 * w) * 8192 + hq * 256;
#pragma unroll
    for (int i = 0; i < 4; ++i)
      dma16((unsigned)((4 * i + g4) * 8192 + ((n16 ^ (4 * i + g4)) * 16)), ub, (unsigned)(L_UK + (16 * w + 4 * i) * 256));
  } else if (w >= 5) {
    if (EDELAY) __builtin_amdgcn_s_sleep(EDELAY);
    if (CLR_EARLY == 1) B_CLEARS(gB, sB, xcd * 16 + rr);
    CKV_ROWS(0, NPRE2);
  }
  if (w == 0) {
    if (roleA) VMWAIT(9); else VMWAIT(5);
    TIE(ew);
    const u64 e1 = (((u64)ew.y << 32) | ew.x) + 1;
    v2u f = {(unsigned)e1, (unsigned)(e1 >> 32)};
    if (lane == 0) GST2(f, (unsigned)(OFF_FLAG1 + b * 128), ws, "sc1");
    if (lane == 0) { sts2(L_EP, f); sts1(L_OK, 0u); sts1(L_CNT, 0u); }
  }
  if (w < 5) {
    if (w == 0) { if (roleA) VMWAIT(9); else VMWAIT(5); } else { if (roleA) VMWAIT(8); else VMWAIT(4); }
    TIE(xr);
    const unsigned d0 = __builtin_amdgcn_perm(xr.x, xr.x, 0x01000100u), d1 = __builtin_amdgcn_perm(xr.x, xr.x, 0x03020302u);
    const unsigned d2 = __builtin_amdgcn_perm(xr.y, xr.y, 0x01000100u), d3 = __builtin_amdgcn_perm(xr.y, xr.y, 0x03020302u);
    v4u a0 = {d0 & msk[0], d0 & msk[1], d0 & msk[2], d0 & msk[3]}, a1 = {d1 & msk[0], d1 & msk[1], d1 & msk[2], d1 & msk[3]};
    v4u a2 = {d2 & msk[0], d2 & msk[1], d2 & msk[2], d2 & msk[3]}, a3 = {d3 & msk[0], d3 & msk[1], d3 & msk[2], d3 & msk[3]};
    unsigned qr = L_QRED + w * 512 + (8 * n16 + 4 * g4) * 4;
    TIE(a0); TIE(a1); TIE(a2); TIE(a3); TIE(qr);
    if (w == 0) { if (roleA) VMWAIT(5); else VMWAIT(1); } else { if (roleA) VMWAIT(4); else VMWAIT(0); }
    TIE(wqr[0]); TIE(wqr[1]); TIE(wqr[2]); TIE(wqr[3]);
    STAMP(2);
    f4 acc = {0.f, 0.f, 0.f, 0.f};
    acc = mfma(a0, wqr[0], acc); acc = mfma(a1, wqr[1], acc); acc = mfma(a2, wqr[2], acc); acc = mfma(a3, wqr[3], acc);
    if (lane < 32) stsf4(qr, acc);
  }
  BARRIER();
  if (w == 0) {
    if (lane < 32) {
      const unsigned ra = L_QRED + lane * 16;
      f4 s0 = ldsf4(ra), s1 = ldsf4(ra + 512), s2 = ldsf4(ra + 1024), s3 = ldsf4(ra + 1536), s4_ = ldsf4(ra + 2048);
      f4 sm = (s0 + s1) + (s2 + s3) + s4_;
      v2u o = {guard(cvtpk(sm.x, sm.y)), guard(cvtpk(sm.z, sm.w))};
      GST2(o, (unsigned)(OFF_QP + (hq * 8 + j8) * 256 + lane * 8), ws, "sc1");
    }
    STAMP(3);
  }
#define EPOCH() ({ v2u e_ = *(LAS v2u*)(size_t)L_EP; (((u64)(unsigned)__builtin_amdgcn_readfirstlane(e_.y)) << 32) | (unsigned)__builtin_amdgcn_readfirstlane(e_.x); })
#define FLAG1_CHECK(BLK) \
  for (unsigned spin = 0;; ++spin) { \
    v2u f0; \
    GLD2(f0, (unsigned)(OFF_FLAG1 + (BLK) * 128), ws, 0, "sc1"); \
    VMWAIT(0); TIE(f0); \
    const unsigned df = (f0.x ^ (unsigned)epoch) | (f0.y ^ (unsigned)(epoch >> 32)); \
    if (__builtin_amdgcn_ballot_w64(df != 0u) == 0ull || spin > SPIN_CAP) break; \
  }
#define FLAG2_CHECK(HALF) \
  for (unsigned spin = 0;; ++spin) { \
    v4u f0; \
    GLD4(f0, (unsigned)(OFF_FLAG2 + (HALF) * 1024 + lane * 16), ws, 0, "sc1"); \
    VMWAIT(0); TIE(f0); \
    const unsigned lo = (unsigned)epoch, hi = (unsigned)(epoch >> 32); \
    const unsigned df = ((f0.x ^ lo) | (f0.y ^ hi)) | ((f0.z ^ lo) | (f0.w ^ hi)); \
    if (__builtin_amdgcn_ballot_w64(df != 0u) == 0ull || spin > SPIN_CAP) break; \
  }
  const u64 epoch = EPOCH();

  if (roleA) {
    const int h = hq, j = j8;
    if (w == 7) { const int j8l = lane & 7; const int blk = (h >> 2) + 8 * ((j8l >> 2) * 16 + (h & 3) * 4 + (j8l & 3)); FLAG1_CHECK(blk); CODEPF_DONE(); STAMP(3); }
    else VMWAIT(0);
    STAMP(12);
    v4u uk[4];
#pragma unroll
    for (int ks = 0; ks < 4; ++ks) uk[ks] = lds4(L_UK + (16 * w + n16) * 256 + (((4 * ks + g4) ^ n16) * 16));
    unsigned qb_a = L_QB + (n16 & 7) * 288 + g4 * 16;
    unsigned qa_o = (unsigned)(OFF_QA + h * 1024 + (128 * j + 16 * w + 4 * g4) * 2);
    TIE(qb_a); TIE(qa_o);
    if (w == 7) {
      v4u p0, p1;
      const unsigned vo = (unsigned)(OFF_QP + h * 2048 + lane * 16);
      unsigned wa = L_QB + g4 * 288 + n16 * 16; TIE(wa);
      for (unsigned spin = 0;; ++spin) {
        GLD4(p0, vo, ws, 0, "sc1"); GLD4(p1, vo, ws, 1024, "sc1");
        VMWAIT(0); TIE(p0); TIE(p1);
        if (allvalid(umax(mx4(p0), mx4(p1))) || spin > SPIN_CAP) break;
      }
      STAMP(4);
      sts4(wa, p0); sts4(wa + 4 * 288, p1);
    }
    BARRIER();
    {
      v4u bq[4];
#pragma unroll
      for (int ks = 0; ks < 4; ++ks) bq[ks] = lds4(qb_a + ks * 64);
      SCHED();
      f4 acc = {0.f, 0.f, 0.f, 0.f};
#pragma unroll
      for (int ks = 0; ks < 4; ++ks) acc = mfma(uk[ks], bq[ks], acc);
      float v0 = acc.x, v1 = acc.y, v2 = acc.z, v3 = acc.w;
      v0 += dpp<XOR1>(v0); v1 += dpp<XOR1>(v1); v2 += dpp<XOR1>(v2); v3 += dpp<XOR1>(v3);
      v0 += dpp<XOR2>(v0); v1 += dpp<XOR2>(v1); v2 += dpp<XOR2>(v2); v3 += dpp<XOR2>(v3);
      v0 += dpp<HMIR>(v0); v1 += dpp<HMIR>(v1); v2 += dpp<HMIR>(v2); v3 += dpp<HMIR>(v3);
      v2u o = {guard(cvtpk(v0, v1)), guard(cvtpk(v2, v3))};
      if (n16 == 0) GST2(o, qa_o, ws, "sc1");
    }
    STAMP(5);
    RAWBAR();
    const int ca = j & 1, db = j >> 1;
    {
      const char* vb = wuv + (size_t)(256 * ca + 32 * w) * 8192 + h * 256 + db * 128;
#pragma unroll
      for (int i = 0; i < 4; ++i)
        dma16((unsigned)((8 * i + (lane >> 3)) * 8192 + (((lane & 7) ^ ((((lane >> 4) & 1) << 1) | ((i & 1) << 2))) * 16)), vb, (unsigned)(L_WUV + (32 * w + 8 * i) * 128));
    }
    if (w == 6) {
      v4u s_ = {SENT, SENT, SENT, SENT};
      GST4(s_, (unsigned)(OFF_OP + (h * 4 + j) * 1280 + lane * 16), ws, "sc1");
      if (lane < 16) GST4(s_, (unsigned)(OFF_OP + (h * 4 + j) * 1280 + 1024 + lane * 16), ws, "sc1");
    }
    {
      unsigned ovo[5];
#pragma unroll
      for (int i = 0; i < 5; ++i) {
        const unsigned n = 64u * (unsigned)(w + 8 * i) + (unsigned)lane, row = (n * 52429u) >> 22, pos = n - row * 80u;
        ovo[i] = row * 1280u + ((pos ^ (((row & 3u) << 1) | (((row >> 3) & 1u) << 3))) * 16u);
      }
      const char* ob = wo + (size_t)(h * 128 + 64 * db) * 1280;
      RAWBAR();
#pragma unroll
      for (int i = 0; i < 5; ++i) dma16(ovo[i], ob + ca * 40960, (unsigned)(L_WO + ca * 40960 + (w + 8 * i) * 1024));
      RAWBAR();
#pragma unroll
      for (int i = 0; i < 5; ++i) dma16(ovo[i], ob + (1 - ca) * 40960, (unsigned)(L_WO + (1 - ca) * 40960 + (w + 8 * i) * 1024));
    }
    if (lane < 8) sts4(L_TAB + w * 512 + 256 + lane * 16, v4u{0u, 0u, 0u, 0u});
    unsigned pvo = (unsigned)(LAY1 ? OFF_PP + h * 32768 + (8 * ca + w) * 2048 + n16 * 64 + g4 * 16 : OFF_PP + (h * 32 + n16) * 1024 + 512 * ca + 64 * w + 16 * g4), pvo2 = pvo + (LAY1 ? 1024u : 16384u);
    unsigned sto = (unsigned)(OFF_ST + ((h >> 3) * 32 + (lane & 31)) * 64 + (h & 7) * 8);
    unsigned crw = L_CRED + lane * 32 + w * 4, crr = L_CRED + lane * 32;
    unsigned tabh = L_TAB + w * 512 + lane * 2;
    unsigned opo = (unsigned)(OFF_OP + (h * 4 + j) * 1280 + 160 * w + (OV3 ? 32 * g4 + 2 * n16 : 32 * n16 + 8 * g4));
    unsigned cw_w = L_CRED + w * 160 + lane * 2, cw_r = L_CRED + n16 * 160 + g4 * 16;
    TIE(cw_w); TIE(cw_r);
    unsigned bva[4], woa[5], bta[5];
    {
      const int q = (lane & 15) >> 2, p = lane & 3;
#pragma unroll
      for (int dt = 0; dt < 4; ++dt)
        bva[dt] = L_WUV + (32 * w + 8 * g4 + q) * 128 + (((2 * dt + (p >> 1)) ^ (((q >> 1) << 1) | ((g4 & 1) << 2))) * 16) + 8 * (p & 1);
#pragma unroll
      for (int t = 0; t < 5; ++t) {
        woa[t] = L_WO + (8 * g4 + q) * 1280 + (((10 * w + 2 * t + (p >> 1)) ^ ((q << 1) | ((g4 & 1) << 3))) * 16) + 8 * (p & 1);
        bta[t] = L_TAB + w * 512 + ((n16 & 7) == t ? (n16 >> 3) * 128 : 256) + g4 * 16;
      }
    }
    TIE(pvo); TIE(pvo2); TIE(sto); TIE(crw); TIE(crr); TIE(tabh); TIE(opo);
#pragma unroll
    for (int t = 0; t < 5; ++t) { TIE(woa[t]); TIE(bta[t]); }
    if (w == 6) VMWAIT(12); else VMWAIT(10);
    STAMP(13);
    v4u bv[4];
#pragma unroll
    for (int dt = 0; dt < 4; ++dt) bv[dt] = ldstr(bva[dt], bva[dt] + 512);
    if (w == 7) { FLAG2_CHECK(1); }
    VMWAIT(0);
    if (w == 6 && lane == 0) { v2u f = {(unsigned)epoch, (unsigned)(epoch >> 32)}; GST2(f, (unsigned)(OFF_FLAG2 + b * 8), ws, "sc1"); }
    STAMP(6);
    BARRIER();
    v4u wa[5][2];
#pragma unroll
    for (int t = 0; t < 5; ++t) { wa[t][0] = ldstr(woa[t], woa[t] + 5120); wa[t][1] = ldstr(woa[t] + 40960, woa[t] + 46080); }
    SCHED();
    if (w == 0) {
      v2u st;
      for (unsigned spin = 0;; ++spin) {
        GLD2(st, sto, ws, 0, "sc1");
        VMWAIT(0); TIE(st);
        if (allvalid(umax(st.x, st.y)) || spin > SPIN_CAP) break;
      }
      if (lane == 0) sts1(L_OK, 2u);
      float m = asf(st.x), l = asf(st.y);
      float M = p16_max(row_max(m));
      float a = __builtin_amdgcn_exp2f((m - M) * SC);
      float Ls = p16_sum(row_sum(a * l));
      a = a * __builtin_amdgcn_rcpf(Ls);
      if (lane < 32) sts1(L_AW + lane * 4, asu(a));
      LGWAIT();
      if (lane == 0) sts1(L_OK, 3u);
    } else {
      for (unsigned spin = 0; *(volatile LAS unsigned*)(size_t)L_OK < 2u && spin < 64u * SPIN_CAP; ++spin) __builtin_amdgcn_s_sleep(1);
    }
    v4u pa[2];
    GLD4(pa[0], pvo, ws, 0, "sc1"); GLD4(pa[1], pvo2, ws, 0, "sc1");
    STAMP(7);
    for (unsigned spin = 0; *(volatile LAS unsigned*)(size_t)L_OK < 3u && spin < 64u * SPIN_CAP; ++spin) __builtin_amdgcn_s_sleep(1);
    f4 aw[2];
    aw[0] = ldsf4(L_AW + g4 * 16); aw[1] = ldsf4(L_AW + 64 + g4 * 16);
    for (unsigned spin = 0;; ++spin) {
      VMWAIT(0); TIE(pa[0]); TIE(pa[1]);
      if (allvalid(umax(mx4(pa[0]), mx4(pa[1]))) || spin > SPIN_CAP) break;
      GLD4(pa[0], pvo, ws, 0, "sc1"); GLD4(pa[1], pvo2, ws, 0, "sc1");
    }
    STAMP(8);
    {
      const f4 z = {0.f, 0.f, 0.f, 0.f};
      f4 t0_[4], t1_[4];
#pragma unroll
      for (int dt = 0; dt < 4; ++dt) { t0_[dt] = mfma(pa[0], bv[dt], z); t1_[dt] = mfma(pa[1], bv[dt], z); }
      SCHED();
      float u[4];
#pragma unroll
      for (int dt = 0; dt < 4; ++dt) { f4 c = aw[0] * t0_[dt] + aw[1] * t1_[dt]; u[dt] = (c.x + c.y) + (c.z + c.w); }
      auto t0 = __builtin_amdgcn_permlane16_swap(asu(u[0]), asu(u[1]), false, false); auto t1 = __builtin_amdgcn_permlane16_swap(asu(u[2]), asu(u[3]), false, false);
      unsigned x0 = t0[0], x1 = t0[1], y0 = t1[0], y1 = t1[1];
      auto t2 = __builtin_amdgcn_permlane32_swap(asu(asf(x0) + asf(x1)), asu(asf(y0) + asf(y1)), false, false);
      unsigned z0 = t2[0], z1 = t2[1];
      const float c = asf(z0) + asf(z1);
      const unsigned hi = cvtpk(c, c);
      const float rs = c - asf(hi << 16);
      const unsigned lo = cvtpk(rs, rs);
      *(LAS unsigned short*)(size_t)cw_w = (unsigned short)hi;
      *(LAS unsigned short*)(size_t)(cw_w + 8 * 160) = (unsigned short)lo;
    }
    BARRIER();
    STAMP(9);
    {
      const v4u ca0 = lds4(cw_r), ca1 = lds4(cw_r + 64);
      SCHED();
      const f4 z = {0.f, 0.f, 0.f, 0.f};
      f4 d[5];
#pragma unroll
      for (int t = 0; t < 5; ++t) d[t] = mfma(ca0, wa[t][0], z);
#pragma unroll
      for (int t = 0; t < 5; ++t) d[t] = mfma(ca1, wa[t][1], d[t]);
      SCHED();
      float v[5];
#pragma unroll
      for (int t = 0; t < 5; ++t) v[t] = (d[t].x + d[t].y) + (d[t].z + d[t].w);
      auto t0 = __builtin_amdgcn_permlane16_swap(asu(v[0]), asu(v[1]), false, false); auto t1 = __builtin_amdgcn_permlane16_swap(asu(v[2]), asu(v[3]), false, false);
      unsigned x0 = t0[0], x1 = t0[1], y0 = t1[0], y1 = t1[1];
      auto t2 = __builtin_amdgcn_permlane32_swap(asu(asf(x0) + asf(x1)), asu(asf(y0) + asf(y1)), false, false);
      unsigned z0 = t2[0], z1 = t2[1];
      const float va = asf(z0) + asf(z1), vb = p32_sum(p16_sum(v[4]));
      const unsigned pa_ = cvtpk(va, dpp<XOR1>(va)), pb_ = cvtpk(vb, dpp<XOR1>(vb));
      v4u oa = {guard(pa_), guard(dppu<0x102>(pa_)), guard(dppu<0x104>(pa_)), guard(dppu<0x106>(pa_))};
      v4u ob_ = {guard(pb_), guard(dppu<0x102>(pb_)), guard(dppu<0x104>(pb_)), guard(dppu<0x106>(pb_))};
      if ((n16 & 7) == 0) GST4(oa, opo, ws, "sc1");
      if ((n16 & 7) == 0 && g4 == 0) GST4(ob_, opo + 128u, ws, "sc1");
    }
    STAMP(10);
  } else {
    const int g = rr & 3, s = xcd * 4 + (rr >> 2), bi = xcd * 16 + rr;
    RAWBAR();
    if (w < 5) {
      CKV_ROWS(0, 16);
    } else {
      CKV_ROWS(NPRE2, 16);
      v2u f1;
      unsigned f1o;
      { const int hd = 8 * g + ((lane & 31) >> 2); f1o = (unsigned)(OFF_FLAG1 + ((hd >> 2) + 8 * ((hd & 3) * 4 + (lane & 3))) * 128); }
      if (w == 7) GLD2(f1, f1o, ws, 0, "sc1");
      if (w == 5) { dma16((unsigned)(lane * 16), x, L_QB); if (lane < 16) dma16((unsigned)(lane * 16), x + 1024, L_QB + 1024); }
      if (w == 5) VMWAIT(18); else if (w == 6) VMWAIT(16); else VMWAIT(17);
      unsigned old = 0;
      if (lane == 0) old = __hip_atomic_fetch_add((LAS unsigned*)(size_t)L_CNT, 1u, __ATOMIC_RELAXED, __HIP_MEMORY_SCOPE_WORKGROUP);
      if (lane == 0 && old == 2u) { v2u f = {(unsigned)epoch, (unsigned)(epoch >> 32)}; GST2(f, (unsigned)(OFF_FLAG2 + b * 8), ws, "sc1"); }
      if (w == 7) {
        VMWAIT(0); TIE(f1);
        for (unsigned spin = 0; spin < SPIN_CAP; ++spin) {
          const unsigned df = (f1.x ^ (unsigned)epoch) | (f1.y ^ (unsigned)(epoch >> 32));
          if (__builtin_amdgcn_ballot_w64(df != 0u) == 0ull) break;
          GLD2(f1, f1o, ws, 0, "sc1");
          VMWAIT(0); TIE(f1);
        }
        CODEPF_DONE();
        if (lane == 0) sts1(L_OK, 1u);
      }
    }
    unsigned qvo = (unsigned)(OFF_QA + (8 * g + w) * 1024 + lane * 16);
    unsigned qsw = L_QAS + w * 1056 + lane * 16, qsr = L_QAS + (n16 & 7) * 1056 + g4 * 16;
    unsigned mxw = L_MX + lane * 32 + w * 4, mxr = L_MX + (n16 & 7) * 32;
    unsigned pw = L_P + n16 * 288 + (16 * kt + 4 * g4) * 2, pr = L_P + (n16 & 7) * 288 + g4 * 16;
    unsigned lsw = L_LS + lane * 32 + w * 4;
    unsigned ppo = PP_SLOT(g, s, w);
    unsigned ka[4], ta[4][2];
    {
      const unsigned kr = L_CKV + (16 * kt + n16) * 1024;
#pragma unroll
      for (int u = 0; u < 4; ++u) ka[u] = kr + (((4 * u + g4) ^ n16) * 16);
      const int q = (lane & 15) >> 2, p = lane & 3;
#pragma unroll
      for (int t = 0; t < 4; ++t) {
        const int ch = 8 * w + 4 * (p >> 1) + t;
        const int r0 = 8 * g4 + q, r1 = r0 + 4;
        ta[t][0] = L_CKV + r0 * 1024 + ((ch ^ (r0 & 15)) * 16) + 8 * (p & 1);
        ta[t][1] = L_CKV + r1 * 1024 + ((ch ^ (r1 & 15)) * 16) + 8 * (p & 1);
      }
    }
    TIE(qvo); TIE(qsw); TIE(qsr); TIE(mxw); TIE(mxr); TIE(pw); TIE(pr); TIE(lsw); TIE(ppo);
#pragma unroll
    for (int u = 0; u < 4; ++u) { TIE(ka[u]); TIE(ta[u][0]); TIE(ta[u][1]); }
    VMWAIT(0);
    STAMP(4);
    for (unsigned spin = 0; *(volatile LAS unsigned*)(size_t)L_OK == 0u && spin < SPIN_CAP; ++spin) __builtin_amdgcn_s_sleep(1);
    v4u qa;
    GLD4(qa, qvo, ws, 0, "sc1");
    v4u kA[16];
#pragma unroll
    for (int ks = 0; ks < 16; ++ks) kA[ks] = lds4(ka[ks & 3] + (ks >> 2) * 256);
    BARRIER();
    STAMP(5);
    v4u vA[4][4];
#pragma unroll
    for (int t = 0; t < 4; ++t) {
      vA[0][t] = ldstr(ta[t][0], ta[t][1]); vA[1][t] = ldstr(ta[t][0] + 32768, ta[t][1] + 32768);
      const unsigned b0 = ta[t][0] + 65536, b1 = ta[t][1] + 65536;
      vA[2][t] = ldstr(b0, b1); vA[3][t] = ldstr(b0 + 32768, b1 + 32768);
    }
    SCHED();
    VMWAIT(0); TIE(qa);
    if (__builtin_expect(!allvalid(mx4(qa)), 0)) {
      v4u qb;
      for (unsigned spin = 0;; ++spin) {
        GLD4(qb, qvo, ws, 0, "sc1");
        VMWAIT(0); TIE(qb);
        if (allvalid(mx4(qb)) || spin > SPIN_CAP) break;
      }
      qa = qb;
    }
    STAMP(6);
    sts4(qsw, qa);
    BARRIER();
    const int fb = bi < 108 ? bi : 107;
    const char* b1 = w1 + fb * 32;
    const unsigned vo1 = (unsigned)((lane >> 1) * 3456 + (lane & 1) * 16);
    const int pq = 5 * (bi & 3);
    f4 sc = {0.f, 0.f, 0.f, 0.f};
    {
      v4u bq[16];
#pragma unroll
      for (int ks = 0; ks < 16; ++ks) bq[ks] = lds4(qsr + ks * 64);
      const char* b2 = w2 + (size_t)(16 * fb) * 1280 + w * 1024;
      const unsigned vo2 = (unsigned)(lane * 16);
      const int p0 = pq + w;
      SCHED();
#pragma unroll
      for (int ks = 0; ks < 16; ++ks) {
        sc = mfma(kA[ks], bq[ks], sc);
        if (MLP1 && ks == 2) { SCHED(); if (w < 5) dma16(vo1, b1 + (size_t)(32 * p0) * 3456, L_W1 + p0 * 1024); SCHED(); }
        if (MLP1 && ks == 5) { SCHED(); dma16(vo2, b2, L_W2 + w * 1024); SCHED(); }
        if (MLP1 && ks == 8) { SCHED(); dma16(vo2, b2 + 8192, L_W2 + (w + 8) * 1024); SCHED(); }
        if (MLP1 && ks == 11) { SCHED(); if (w < 4) dma16(vo2, b2 + 16384, L_W2 + (w + 16) * 1024); SCHED(); }
      }
      float mw = p32_max(p16_max(fmx(fmx(sc.x, sc.y), fmx(sc.z, sc.w))));
      if (lane < 8) sts1(mxw, asu(mw));
    }
    BARRIER();
    STAMP(7);
    {
      const float Ms = -max8(ldsf4(mxr), ldsf4(mxr + 16)) * SC;
      float p0 = __builtin_amdgcn_exp2f(fmaf(sc.x, SC, Ms)), p1 = __builtin_amdgcn_exp2f(fmaf(sc.y, SC, Ms)), p2 = __builtin_amdgcn_exp2f(fmaf(sc.z, SC, Ms)), p3 = __builtin_amdgcn_exp2f(fmaf(sc.w, SC, Ms));
      v2u pk = {cvtpk(p0, p1), cvtpk(p2, p3)};
      if (n16 < 8) sts2(pw, pk);
      float ls = p32_sum(p16_sum((p0 + p1) + (p2 + p3)));
      if (lane < 8) sts1(lsw, asu(ls));
    }
    BARRIER();
    STAMP(8);
    {
      v4u pb[4];
#pragma unroll
      for (int ks = 0; ks < 4; ++ks) pb[ks] = lds4(pr + ks * 64);
      f4 lu, lv, mu, mv;
      if (w == STW) { const unsigned sa = (lane & 7) * 32; lu = ldsf4(L_LS + sa); lv = ldsf4(L_LS + sa + 16); mu = ldsf4(L_MX + sa); mv = ldsf4(L_MX + sa + 16); }
      SCHED();
      f4 ac[4];
#pragma unroll
      for (int t = 0; t < 4; ++t) ac[t] = mfma(vA[0][t], pb[0], f4{0.f, 0.f, 0.f, 0.f});
      if (w == STW) {
        v2u o = {guard(asu(max8(mu, mv))), guard(asu(sum8(lu, lv)))};
        if (lane < 8) GST2(o, (unsigned)(OFF_ST + (g * 32 + s) * 64 + lane * 8), ws, "sc1");
      }
#pragma unroll
      for (int ks = 1; ks < 4; ++ks)
#pragma unroll
        for (int t = 0; t < 4; ++t) ac[t] = mfma(vA[ks][t], pb[ks], ac[t]);
      const bool up = n16 >= 8;
      f4 xa = up ? ac[2] : ac[0], ya = up ? ac[3] : ac[1];
      unsigned x0 = cvtpk(xa.x, xa.y), x1 = cvtpk(xa.z, xa.w), y0 = cvtpk(ya.x, ya.y), y1 = cvtpk(ya.z, ya.w);
      auto s0 = __builtin_amdgcn_permlane16_swap(x0, y0, false, false); auto s1 = __builtin_amdgcn_permlane16_swap(x1, y1, false, false);
      unsigned e0 = s0[0], e1 = s1[0], f0 = s0[1], f1 = s1[1];
      v4u o = {guard(e0), guard(e1), guard(f0), guard(f1)};
      GST4(o, ppo, ws, "sc1");
    }
    STAMP(9);
    const bool fin = bi < 80 && w == 5;
    v4u ff0, ff1; unsigned gpre[5];
    if (FINPF && fin) {
      GLD4(ff0, (unsigned)(OFF_FLAG2 + lane * 16), ws, 0, "sc1"); GLD4(ff1, (unsigned)(OFF_FLAG2 + 1024 + lane * 16), ws, 0, "sc1");
#pragma unroll
      for (int i = 0; i < 5; ++i) GLD1(gpre[i], (unsigned)(lane * 4), gw, i * 256, "");
    }
    RAWBAR();
    int pc0 = pq + 5 + w, pc1 = pq + 13 + w;
    if (pc0 >= 20) pc0 -= 20;
    if (pc1 >= 20) pc1 -= 20;
    if (!MLP1 && !EXP_NOMLP) {
      const char* b2 = w2 + (size_t)(16 * fb) * 1280 + w * 1024;
      const unsigned vo2 = (unsigned)(lane * 16);
      if (w < 5) dma16(vo1, b1 + (size_t)(32 * (pq + w)) * 3456, L_W1 + (pq + w) * 1024);
      dma16(vo2, b2, L_W2 + w * 1024); dma16(vo2, b2 + 8192, L_W2 + (w + 8) * 1024); if (w < 4) dma16(vo2, b2 + 16384, L_W2 + (w + 16) * 1024);
      RAWBAR();
    }
    if (!EXP_NOMLP) {
    dma16(vo1, b1 + (size_t)(32 * pc0) * 3456, L_W1 + pc0 * 1024);
    if (w < 7) dma16(vo1, b1 + (size_t)(32 * pc1) * 3456, L_W1 + pc1 * 1024); }
    VMWAIT(0);
    STAMP(12);
    {
      const int q = (lane & 15) >> 2, p = lane & 3;
      const unsigned wa = L_W1 + (8 * g4 + q) * 32 + 8 * p, xa = L_QB + g4 * 16;
      f4 hacc = {0.f, 0.f, 0.f, 0.f};
      hacc = mfma(lds4(xa + pc0 * 64), ldstr(wa + pc0 * 1024, wa + pc0 * 1024 + 128), hacc);
      if (w < 7) hacc = mfma(lds4(xa + pc1 * 64), ldstr(wa + pc1 * 1024, wa + pc1 * 1024 + 128), hacc);
      if (w < 5) { const int p0 = pq + w; hacc = mfma(lds4(xa + p0 * 64), ldstr(wa + p0 * 1024, wa + p0 * 1024 + 128), hacc); }
      if (lane < 16) sts1(L_RED + lane * 32 + w * 4, asu(hacc.x));
    }
    BARRIER();
    STAMP(15);
    if (w < 5) {
      v4u w2r[4];
#pragma unroll
      for (int i = 0; i < 4; ++i) w2r[i] = lds4(L_W2 + (4 * g4 + i) * 1280 + w * 256 + n16 * 16);
      const int f = lane & 15;
      float hh = sum8(ldsf4(L_RED + f * 32), ldsf4(L_RED + f * 32 + 16));
      hh = hh * __builtin_amdgcn_rcpf(1.f + __expf(-hh));
      if (bi >= 108) hh = 0.f;
      unsigned hi = cvtpk(hh, hh);
      float rs = hh - asf(hi << 16);
      unsigned lo = cvtpk(rs, rs);
      if (lane < 32) sts1(L_TAB + w * 512 + lane * 4, lane < 16 ? hi : lo);
      LGWAIT();
      v4u t0 = lds4(L_TAB + w * 512 + ((n16 >> 3) * 16 + 4 * g4) * 4);
      unsigned dv[4] = {t0.x, t0.y, t0.z, t0.w};
      f4 acc = {0.f, 0.f, 0.f, 0.f};
#pragma unroll
      for (int i = 0; i < 4; ++i) { v4u a = {dv[i] & msk[0], dv[i] & msk[1], dv[i] & msk[2], dv[i] & msk[3]}; acc = mfma(a, w2r[i], acc); }
      float o0 = p32_sum(acc.x), o1 = p32_sum(acc.y), o2 = p32_sum(acc.z), o3 = p32_sum(acc.w);
      unsigned a0 = guard(cvtpk(o0, o1)), a1 = guard(cvtpk(o2, o3));
      auto s0 = __builtin_amdgcn_permlane16_swap(a0, a0, false, false); auto s1 = __builtin_amdgcn_permlane16_swap(a1, a1, false, false);
      unsigned b0 = s0[1], b1 = s1[1];
      v4u o = {a0, a1, b0, b1};
      if (lane < 16) GST4(o, (unsigned)(OFF_MP + bi * 1280 + w * 256 + lane * 16), ws, "sc1");
    }
    STAMP(10);
    if (bi < 80 && w == 5) {
      unsigned gg[5];
      if (FINPF) {
        TIE(ff0); TIE(ff1);
#pragma unroll
        for (int i = 0; i < 5; ++i) { TIE(gpre[i]); gg[i] = gpre[i]; }
        const unsigned lo = (unsigned)epoch, hi = (unsigned)(epoch >> 32);
        const unsigned df = ((ff0.x ^ lo) | (ff0.y ^ hi)) | ((ff0.z ^ lo) | (ff0.w ^ hi)) | ((ff1.x ^ lo) | (ff1.y ^ hi)) | ((ff1.z ^ lo) | (ff1.w ^ hi));
        if (__builtin_amdgcn_ballot_w64(df != 0u) != 0ull) { FLAG2_CHECK(0); FLAG2_CHECK(1); }
      } else {
        FLAG2_CHECK(0);
        FLAG2_CHECK(1);
#pragma unroll
        for (int i = 0; i < 5; ++i) GLD1(gg[i], (unsigned)(lane * 4), gw, i * 256, "");
        VMWAIT(0);
#pragma unroll
        for (int i = 0; i < 5; ++i) TIE(gg[i]);
      }
      float gate;
      {
        float a = 0.f;
#pragma unroll
        for (int i = 0; i < 5; ++i) { const unsigned gx = *(LAS unsigned*)(size_t)(L_QB + i * 256 + lane * 4); a += asf(gx << 16) * asf(gg[i] << 16) + asf(gx & 0xFFFF0000u) * asf(gg[i] & 0xFFFF0000u); }
        a = p32_sum(p16_sum(row_sum(a)));
        gate = __builtin_amdgcn_rcpf(1.f + __expf(-a));
      }
      const unsigned one = 0x3F803F80u;
      v4u ones = {one & msk[0], one & msk[1], one & msk[2], one & msk[3]};
      v4u m0, m1;
      const unsigned mvo = (unsigned)(OFF_MP + lane * 1280 + bi * 16), mvo2 = mvo + 64 * 1280;
      for (unsigned spin = 0;; ++spin) {
        GLD4(m0, mvo, ws, 0, "sc1"); GLD4(m1, mvo2, ws, 0, "sc1");
        VMWAIT(0); TIE(m0); TIE(m1);
        if (allvalid(umax(mx4(m0), mx4(m1))) || spin > SPIN_CAP) break;
      }
      f4 bb = {0.f, 0.f, 0.f, 0.f};
      bb = mfma(ones, m0, bb); bb = mfma(ones, m1, bb);
      bb *= (1.f - gate);
      STAMP(11);
      const unsigned ovo = (unsigned)(OFF_OP + lane * 1280 + bi * 16), ovo2 = ovo + 64 * 1280;
      if (CANARY) {
        const unsigned cvo = (unsigned)(OFF_OP + ((bi * 13 + 5) & 127) * 1280 + bi * 16);
        for (unsigned spin = 0; spin < SPIN_CAP; ++spin) { unsigned cv; GLD1(cv, cvo, ws, 0, "sc1"); VMWAIT(0); TIE(cv); if (allvalid(cv)) break; }
      }
      for (unsigned spin = 0;; ++spin) {
        GLD4(m0, ovo, ws, 0, "sc1"); GLD4(m1, ovo2, ws, 0, "sc1");
        VMWAIT(0); TIE(m0); TIE(m1);
        if (allvalid(umax(mx4(m0), mx4(m1))) || spin > SPIN_CAP) break;
      }
      STAMP(12);
      f4 aa = {0.f, 0.f, 0.f, 0.f};
      aa = mfma(ones, m0, aa); aa = mfma(ones, m1, aa);
      float r0 = row_sum(fmaf(gate, aa.x, bb.x)), r1 = row_sum(fmaf(gate, aa.y, bb.y));
      float r2 = row_sum(fmaf(gate, aa.z, bb.z)), r3 = row_sum(fmaf(gate, aa.w, bb.w));
      v2u o = {cvtpk(r0, r1), cvtpk(r2, r3)};
      if (n16 == 0 && g4 < 2) GST2(o, (unsigned)(bi * 16 + g4 * 8), out, "");
      if (bi == 0 && lane == 0) {
        v2u f = {(unsigned)epoch, (unsigned)(epoch >> 32)};
        GST2(f, (unsigned)OFF_E, ws, "sc1");
      }
      STAMP(13);
    }
  }
  asm volatile(".Lgm_code_end:" ::: "memory");
}

extern "C" void kernel_launch(void* const* d_in, const int* in_sizes, int n_in,
                              void* d_out, int out_size, void* d_ws, size_t ws_size,
                              hipStream_t stream) {
  gm_kernel<<<256, 512, 0, stream>>>((const char*)d_in[0], (const char*)d_in[1], (const char*)d_in[2], (const char*)d_in[3],
                                      (const char*)d_in[4], (const char*)d_in[5], (const char*)d_in[7], (const char*)d_in[8],
                                      (const char*)d_in[9], (char*)d_out, (char*)d_ws);
}
```

```cpp
#include <hip/hip_runtime.h>
#include <stdint.h>
#pragma clang diagnostic ignored "-Winline-asm"

typedef unsigned v4u __attribute__((ext_vector_type(4)));
typedef unsigned v2u __attribute__((ext_vector_type(2)));
typedef float f4 __attribute__((ext_vector_type(4)));
typedef float f2 __attribute__((ext_vector_type(2)));
typedef short s4 __attribute__((ext_vector_type(4)));
typedef short s8 __attribute__((ext_vector_type(8)));
typedef __bf16 bf2 __attribute__((ext_vector_type(2)));
typedef unsigned long long u64;
#define LAS __attribute__((address_space(3)))
#define DI __device__ __forceinline__

constexpr unsigned OFF_E = 0;
constexpr unsigned OFF_FLAG2 = 4096;
constexpr unsigned OFF_FLAG1 = 8192;
constexpr unsigned OFF_QP = OFF_FLAG1 + 32768;
constexpr unsigned OFF_QA = OFF_QP + 65536;
constexpr unsigned OFF_ST = OFF_QA + 32768;
constexpr unsigned OFF_OP = OFF_ST + 8192;
constexpr unsigned OFF_MP = OFF_OP + 163840;
constexpr unsigned OFF_PP = OFF_MP + 163840;
constexpr unsigned OFF_END = OFF_PP + 1048576;
constexpr unsigned OFF_NOTE = (OFF_END + 127) / 128 * 128;
constexpr unsigned OFF_STAMP = 8u << 20;
constexpr unsigned SENT = 0xFFFFFFFFu;
constexpr unsigned SPIN_CAP = 1u << 15;
constexpr float SC = 0.08838834764831845f * 1.4426950408889634f;

constexpr unsigned L_CKV = 0;
constexpr unsigned L_W1 = 0, L_W2 = 32768;
constexpr unsigned L_WUV = 0, L_WO = 32768;
constexpr unsigned L_AW = 131072 + 2560;
constexpr unsigned L_CRED = 131072;
constexpr unsigned L_UK = 32768;
constexpr unsigned L_QAS = 131072;
constexpr unsigned L_QRED = L_QAS;
constexpr unsigned L_P = L_QAS + 8448;
constexpr unsigned L_MX = L_P + 2304;
constexpr unsigned L_LS = L_MX + 256;
constexpr unsigned L_RED = L_LS + 256;
constexpr unsigned L_TAB = L_RED + 1024;
constexpr unsigned L_QB = L_TAB + 4096;
constexpr unsigned L_EP = L_QB + 2304;
constexpr unsigned L_OK = L_EP + 8;
constexpr unsigned L_CNT = L_EP + 12;
constexpr unsigned L_END = L_EP + 16;

DI f4 mfma(v4u a, v4u b, f4 c) { return __builtin_amdgcn_mfma_f32_16x16x32_bf16(__builtin_bit_cast(s8, a), __builtin_bit_cast(s8, b), c, 0, 0, 0); }
DI unsigned cvtpk(float lo, float hi) { f2 x = {lo, hi}; bf2 y = __builtin_convertvector(x, bf2); return __builtin_bit_cast(unsigned, y); }
DI float asf(unsigned u) { return __uint_as_float(u); }
DI unsigned asu(float f) { return __float_as_uint(f); }
DI unsigned guard(unsigned u) { return u < 0xFFFFFFFEu ? u : 0xFFFFFFFEu; }
DI float fmx(float a, float b) { return __builtin_elementwise_maximum(a, b); }
template <int CTRL> DI float dpp(float x) { return asf((unsigned)__builtin_amdgcn_mov_dpp((int)asu(x), CTRL, 0xf, 0xf, true)); }
template <int CTRL> DI unsigned dppu(unsigned x) { return (unsigned)__builtin_amdgcn_mov_dpp((int)x, CTRL, 0xf, 0xf, true); }
constexpr int XOR1 = 0xB1, XOR2 = 0x4E, ROR4 = 0x124, ROR8 = 0x128, HMIR = 0x141;
DI float p16_max(float x) { auto t = __builtin_amdgcn_permlane16_swap(asu(x), asu(x), false, false); unsigned a = t[0], b = t[1]; return fmx(asf(a), asf(b)); }
DI float p32_max(float x) { auto t = __builtin_amdgcn_permlane32_swap(asu(x), asu(x), false, false); unsigned a = t[0], b = t[1]; return fmx(asf(a), asf(b)); }
DI float p16_sum(float x) { auto t = __builtin_amdgcn_permlane16_swap(asu(x), asu(x), false, false); unsigned a = t[0], b = t[1]; return asf(a) + asf(b); }
DI float p32_sum(float x) { auto t = __builtin_amdgcn_permlane32_swap(asu(x), asu(x), false, false); unsigned a = t[0], b = t[1]; return asf(a) + asf(b); }
DI float row_sum(float x) { x += dpp<XOR1>(x); x += dpp<XOR2>(x); x += dpp<ROR4>(x); x += dpp<ROR8>(x); return x; }
DI float row_max(float x) { x = fmx(x, dpp<XOR1>(x)); x = fmx(x, dpp<XOR2>(x)); x = fmx(x, dpp<ROR4>(x)); x = fmx(x, dpp<ROR8>(x)); return x; }
DI float max8(f4 u, f4 v) { return fmx(fmx(fmx(u.x, u.y), fmx(u.z, u.w)), fmx(fmx(v.x, v.y), fmx(v.z, v.w))); }
DI float sum8(f4 u, f4 v) { return ((u.x + u.y) + (u.z + u.w)) + ((v.x + v.y) + (v.z + v.w)); }
DI unsigned umax(unsigned a, unsigned b) { return a > b ? a : b; }
DI unsigned mx4(v4u v) { return umax(umax(v.x, v.y), umax(v.z, v.w)); }
DI bool allvalid(unsigned m) { return __builtin_amdgcn_ballot_w64(m == SENT) == 0ull; }
DI v4u lds4(unsigned a) { return *(LAS v4u*)(size_t)a; }
DI f4 ldsf4(unsigned a) { return *(LAS f4*)(size_t)a; }
DI void sts4(unsigned a, v4u v) { *(LAS v4u*)(size_t)a = v; }
DI void stsf4(unsigned a, f4 v) { *(LAS f4*)(size_t)a = v; }
DI void sts2(unsigned a, v2u v) { *(LAS v2u*)(size_t)a = v; }
DI void sts1(unsigned a, unsigned v) { *(LAS unsigned*)(size_t)a = v; }
DI v4u ldstr(unsigned a0, unsigned a1) {
  s4 lo = __builtin_amdgcn_ds_read_tr16_b64_v4i16((LAS s4*)(size_t)a0), hi = __builtin_amdgcn_ds_read_tr16_b64_v4i16((LAS s4*)(size_t)a1);
  s8 c = __builtin_shufflevector(lo, hi, 0, 1, 2, 3, 4, 5, 6, 7);
  return __builtin_bit_cast(v4u, c);
}

#define GLD4(dst, voff, sbase, IMM, MOD) asm volatile("global_load_dwordx4 %0, %1, %2 offset:%3 " MOD : "=v"(dst) : "v"(voff), "s"(sbase), "i"(IMM) : "memory")
#define GLD2(dst, voff, sbase, IMM, MOD) asm volatile("global_load_dwordx2 %0, %1, %2 offset:%3 " MOD : "=v"(dst) : "v"(voff), "s"(sbase), "i"(IMM) : "memory")
#define GLD1(dst, voff, sbase, IMM, MOD) asm volatile("global_load_dword %0, %1, %2 offset:%3 " MOD : "=v"(dst) : "v"(voff), "s"(sbase), "i"(IMM) : "memory")
#define GST4(src, voff, sbase, MOD) asm volatile("global_store_dwordx4 %0, %1, %2 " MOD "\n\ts_nop 1" :: "v"(voff), "v"(src), "s"(sbase) : "memory")
#define GST2(src, voff, sbase, MOD) asm volatile("global_store_dwordx2 %0, %1, %2 " MOD :: "v"(voff), "v"(src), "s"(sbase) : "memory")
#define SCHED() __builtin_amdgcn_sched_barrier(0)
#define VMWAIT(N) do { asm volatile("s_waitcnt vmcnt(%0)" :: "i"(N) : "memory"); SCHED(); } while (0)
#define LGWAIT() do { asm volatile("s_waitcnt lgkmcnt(0)" ::: "memory"); SCHED(); } while (0)
#define TIE(x) asm volatile("" : "+v"(x))
DI unsigned now32() { u64 t; asm volatile("s_memrealtime %0\n\ts_waitcnt lgkmcnt(0)" : "=s"(t) :: "memory"); return (unsigned)t; }
DI void dma16(unsigned voff, const void* sbase, unsigned ldsdst) {
  asm volatile("s_mov_b32 m0, %2\n\ts_nop 0\n\tglobal_load_lds_dwordx4 %0, %1" :: "v"(voff), "s"(sbase), "s"(ldsdst) : "memory", "m0");
}
#define BARRIER() do { asm volatile("s_waitcnt lgkmcnt(0)\n\ts_barrier" ::: "memory"); SCHED(); } while (0)
#define RAWBAR() do { asm volatile("s_barrier" ::: "memory"); SCHED(); } while (0)

#define CODEPF_DONE() asm volatile("" :: "v"(codepf))
#define STAMP(i) do {} while (0)
#define NPRE 4
#define OV3 1
#define LAY1 1
#define FINPF 0
#define STW 0
#define EDELAY 0
#define EXP_NOMLP 0
#define CANARY 0
#define NOTICE 1
#define NDELAY 16
#define NDELTA 125
#define NSKIP 290
#define LATEH 0
#define FINT 1
#define FDELTA 45
#define ODELTA 185
#define MLP1 0
#define NPRE2 6

__global__ void __launch_bounds__(512, 2)
gm_kernel(const char* __restrict__ x, const char* __restrict__ ckv, const char* __restrict__ wq, const char* __restrict__ wuk,
          const char* __restrict__ wuv, const char* __restrict__ wo, const char* __restrict__ w1, const char* __restrict__ w2,
          const char* __restrict__ gw, char* __restrict__ out, char* __restrict__ ws) {
  __shared__ __attribute__((aligned(16))) char smem[L_END];
  asm volatile("" :: "v"((LAS char*)smem));
  STAMP(0);
  const int tid = threadIdx.x, lane = tid & 63;
  const int w = __builtin_amdgcn_readfirstlane(tid >> 6);
  const int b = blockIdx.x, xcd = b & 7, r = b >> 3;
  v4u codepf;
  {
    u64 pc; unsigned csz;
    asm volatile("s_getpc_b64 %0\n.Lgm_pc_here:\n\ts_mov_b32 %1, .Lgm_code_end-.Lgm_pc_here" : "=s"(pc), "=s"(csz));
    unsigned co = (unsigned)(r * 1024 + lane * 16);
    if (w == 7 && co + 16u <= csz) GLD4(codepf, co, (const char*)pc, 0, "");
  }
  const bool roleA = r < 16;
  const int g4 = lane >> 4, n16 = lane & 15;
  const int rr = r & 15;
  const int hq = xcd * 4 + (rr >> 2), j8 = (roleA ? 0 : 4) + (rr & 3);
  asm volatile("" :: "s"(x), "s"(ckv), "s"(wq), "s"(wuk), "s"(wuv), "s"(wo), "s"(w1), "s"(w2), "s"(gw), "s"(out), "s"(ws));
  STAMP(14);
  unsigned msk[4];
#pragma unroll
  for (int t = 0; t < 4; ++t) msk[t] = (((n16 & 7) >> 1) == t) ? ((n16 & 1) ? 0xFFFF0000u : 0x0000FFFFu) : 0u;

#define PP_SLOT(G_, S_, W_) ((unsigned)(LAY1 ? OFF_PP + (8 * (G_) + (n16 & 7)) * 32768 + (2 * (W_) + (g4 >> 1)) * 2048 + (S_) * 64 + (2 * (n16 >> 3) + (g4 & 1)) * 16 \
    : OFF_PP + ((8 * (G_) + (n16 & 7)) * 32 + (S_)) * 1024 + (64 * (W_) + 32 * (g4 >> 1) + 8 * (2 * (n16 >> 3) + (g4 & 1))) * 2))
#define B_CLEARS(G_, S_, BI_) \
    { v4u s_ = {SENT, SENT, SENT, SENT}; \
      _Pragma("unroll") for (int i = 0; i < 3; ++i) { const int hh = 3 * (w - 5) + i; if (hh < 8) GST4(s_, LAY1 ? PP_SLOT(G_, S_, hh) : (unsigned)(OFF_PP + ((8 * (G_) + hh) * 32 + (S_)) * 1024 + lane * 16), ws, "sc1"); } \
      if (w == 7) { \
        GST4(s_, (unsigned)(OFF_MP + (BI_) * 1280 + lane * 16), ws, "sc1"); \
        unsigned vo = (unsigned)(OFF_MP + (BI_) * 1280 + 1024 + lane * 16); \
        if (lane >= 16) vo = (unsigned)(OFF_ST + ((G_) * 32 + (S_)) * 64 + (lane - 16) * 16); \
        if (lane < 20) GST4(s_, vo, ws, "sc1"); } }
#define CLR_EARLY 2
  const int sB = xcd * 4 + (rr >> 2), gB = rr & 3;
  v4u wqr[4]; v2u xr, ew;
  STAMP(15);
  if (w == 0) {
    GLD2(ew, 0u, ws, OFF_E, "sc1");
    v4u s_ = {SENT, SENT, SENT, SENT};
    unsigned vo = (unsigned)(OFF_QP + (hq * 8 + j8) * 256 + n16 * 16);
    if (roleA && g4 == 1) vo = (unsigned)(OFF_QA + hq * 1024 + j8 * 256 + n16 * 16);
    if (lane < (roleA ? 32 : 16)) GST4(s_, vo, ws, "sc1");
  }
  if (w < 5) {
    const char* wb = wq + (size_t)(j8 * 80 + 16 * w) * 8192 + hq * 256;
    const unsigned vo = (unsigned)(4 * g4 * 8192 + n16 * 16);
    GLD2(xr, (unsigned)(8 * g4), x + (j8 * 80 + 16 * w) * 2, 0, "");
    GLD4(wqr[0], vo, wb, 0, ""); GLD4(wqr[1], vo, wb + 8192, 0, ""); GLD4(wqr[2], vo, wb + 16384, 0, ""); GLD4(wqr[3], vo, wb + 24576, 0, "");
  }
  if (CLR_EARLY == 2 && !roleA && w >= 5) B_CLEARS(gB, sB, xcd * 16 + rr);
  STAMP(11);
  RAWBAR();
  STAMP(1);
  const int kt = (w + 2 * gB) & 7;
  const char* kb = ckv + (size_t)(128 * sB + 16 * kt) * 1024;
  const unsigned kl = L_CKV + (16 * kt) * 1024;
#define CKV_ROWS(I0, I1) \
  _Pragma("unroll") for (int i = I0; i < I1; ++i) dma16((unsigned)((lane ^ i) * 16), kb + i * 1024, kl + i * 1024);
  if (roleA) {
    const char* ub = wuk + (size_t)(128 * j8 + 16 * w) * 8192 + hq * 256;
#pragma unroll
    for (int i = 0; i < 4; ++i)
      dma16((unsigned)((4 * i + g4) * 8192 + ((n16 ^ (4 * i + g4)) * 16)), ub, (unsigned)(L_UK + (16 * w + 4 * i) * 256));
  } else if (w >= 5) {
    if (EDELAY) __builtin_amdgcn_s_sleep(EDELAY);
    if (CLR_EARLY == 1) B_CLEARS(gB, sB, xcd * 16 + rr);
    CKV_ROWS(0, NPRE2);
  }
  if (w == 0) {
    if (roleA) VMWAIT(9); else VMWAIT(5);
    TIE(ew);
    const u64 e1 = (((u64)ew.y << 32) | ew.x) + 1;
    v2u f = {(unsigned)e1, (unsigned)(e1 >> 32)};
    if (lane == 0) GST2(f, (unsigned)(OFF_FLAG1 + b * 128), ws, "sc1");
    if (lane == 0) { sts2(L_EP, f); sts1(L_OK, 0u); sts1(L_CNT, 0u); }
  }
  if (w < 5) {
    if (w == 0) { if (roleA) VMWAIT(9); else VMWAIT(5); } else { if (roleA) VMWAIT(8); else VMWAIT(4); }
    TIE(xr);
    const unsigned d0 = __builtin_amdgcn_perm(xr.x, xr.x, 0x01000100u), d1 = __builtin_amdgcn_perm(xr.x, xr.x, 0x03020302u);
    const unsigned d2 = __builtin_amdgcn_perm(xr.y, xr.y, 0x01000100u), d3 = __builtin_amdgcn_perm(xr.y, xr.y, 0x03020302u);
    v4u a0 = {d0 & msk[0], d0 & msk[1], d0 & msk[2], d0 & msk[3]}, a1 = {d1 & msk[0], d1 & msk[1], d1 & msk[2], d1 & msk[3]};
    v4u a2 = {d2 & msk[0], d2 & msk[1], d2 & msk[2], d2 & msk[3]}, a3 = {d3 & msk[0], d3 & msk[1], d3 & msk[2], d3 & msk[3]};
    unsigned qr = L_QRED + w * 512 + (8 * n16 + 4 * g4) * 4;
    TIE(a0); TIE(a1); TIE(a2); TIE(a3); TIE(qr);
    if (w == 0) { if (roleA) VMWAIT(5); else VMWAIT(1); } else { if (roleA) VMWAIT(4); else VMWAIT(0); }
    TIE(wqr[0]); TIE(wqr[1]); TIE(wqr[2]); TIE(wqr[3]);
    STAMP(2);
    f4 acc = {0.f, 0.f, 0.f, 0.f};
    acc = mfma(a0, wqr[0], acc); acc = mfma(a1, wqr[1], acc); acc = mfma(a2, wqr[2], acc); acc = mfma(a3, wqr[3], acc);
    if (lane < 32) stsf4(qr, acc);
  }
  BARRIER();
  if (w == 0) {
    if (lane < 32) {
      const unsigned ra = L_QRED + lane * 16;
      f4 s0 = ldsf4(ra), s1 = ldsf4(ra + 512), s2 = ldsf4(ra + 1024), s3 = ldsf4(ra + 1536), s4_ = ldsf4(ra + 2048);
      f4 sm = (s0 + s1) + (s2 + s3) + s4_;
      v2u o = {guard(cvtpk(sm.x, sm.y)), guard(cvtpk(sm.z, sm.w))};
      GST2(o, (unsigned)(OFF_QP + (hq * 8 + j8) * 256 + lane * 8), ws, "sc1");
    }
    STAMP(3);
  }
#define EPOCH() ({ v2u e_ = *(LAS v2u*)(size_t)L_EP; (((u64)(unsigned)__builtin_amdgcn_readfirstlane(e_.y)) << 32) | (unsigned)__builtin_amdgcn_readfirstlane(e_.x); })
#define FLAG1_CHECK(BLK) \
  for (unsigned spin = 0;; ++spin) { \
    v2u f0; \
    GLD2(f0, (unsigned)(OFF_FLAG1 + (BLK) * 128), ws, 0, "sc1"); \
    VMWAIT(0); TIE(f0); \
    const unsigned df = (f0.x ^ (unsigned)epoch) | (f0.y ^ (unsigned)(epoch >> 32)); \
    if (__builtin_amdgcn_ballot_w64(df != 0u) == 0ull || spin > SPIN_CAP) break; \
  }
#define FLAG2_CHECK(HALF) \
  for (unsigned spin = 0;; ++spin) { \
    v4u f0; \
    GLD4(f0, (unsigned)(OFF_FLAG2 + (HALF) * 1024 + lane * 16), ws, 0, "sc1"); \
    VMWAIT(0); TIE(f0); \
    const unsigned lo = (unsigned)epoch, hi = (unsigned)(epoch >> 32); \
    const unsigned df = ((f0.x ^ lo) | (f0.y ^ hi)) | ((f0.z ^ lo) | (f0.w ^ hi)); \
    if (__builtin_amdgcn_ballot_w64(df != 0u) == 0ull || spin > SPIN_CAP) break; \
  }
  const u64 epoch = EPOCH();

  if (roleA) {
    const int h = hq, j = j8;
    if (w == 7) { const int j8l = lane & 7; const int blk = (h >> 2) + 8 * ((j8l >> 2) * 16 + (h & 3) * 4 + (j8l & 3)); FLAG1_CHECK(blk); CODEPF_DONE(); STAMP(3); }
    else VMWAIT(0);
    STAMP(12);
    v4u uk[4];
#pragma unroll
    for (int ks = 0; ks < 4; ++ks) uk[ks] = lds4(L_UK + (16 * w + n16) * 256 + (((4 * ks + g4) ^ n16) * 16));
    unsigned qb_a = L_QB + (n16 & 7) * 288 + g4 * 16;
    unsigned qa_o = (unsigned)(OFF_QA + h * 1024 + (128 * j + 16 * w + 4 * g4) * 2);
    TIE(qb_a); TIE(qa_o);
    if (w == 7) {
      v4u p0, p1;
      const unsigned vo = (unsigned)(OFF_QP + h * 2048 + lane * 16);
      unsigned wa = L_QB + g4 * 288 + n16 * 16; TIE(wa);
      for (unsigned spin = 0;; ++spin) {
        GLD4(p0, vo, ws, 0, "sc1"); GLD4(p1, vo, ws, 1024, "sc1");
        VMWAIT(0); TIE(p0); TIE(p1);
        if (allvalid(umax(mx4(p0), mx4(p1))) || spin > SPIN_CAP) break;
      }
      STAMP(4);
      sts4(wa, p0); sts4(wa + 4 * 288, p1);
    }
    BARRIER();
    {
      v4u bq[4];
#pragma unroll
      for (int ks = 0; ks < 4; ++ks) bq[ks] = lds4(qb_a + ks * 64);
      SCHED();
      f4 acc = {0.f, 0.f, 0.f, 0.f};
#pragma unroll
      for (int ks = 0; ks < 4; ++ks) acc = mfma(uk[ks], bq[ks], acc);
      float v0 = acc.x, v1 = acc.y, v2 = acc.z, v3 = acc.w;
      v0 += dpp<XOR1>(v0); v1 += dpp<XOR1>(v1); v2 += dpp<XOR1>(v2); v3 += dpp<XOR1>(v3);
      v0 += dpp<XOR2>(v0); v1 += dpp<XOR2>(v1); v2 += dpp<XOR2>(v2); v3 += dpp<XOR2>(v3);
      v0 += dpp<HMIR>(v0); v1 += dpp<HMIR>(v1); v2 += dpp<HMIR>(v2); v3 += dpp<HMIR>(v3);
      v2u o = {guard(cvtpk(v0, v1)), guard(cvtpk(v2, v3))};
      if (n16 == 0) GST2(o, qa_o, ws, "sc1");
    }
    STAMP(5);
    const unsigned tpub = now32();
    RAWBAR();
    const int ca = j & 1, db = j >> 1;
    {
      const char* vb = wuv + (size_t)(256 * ca + 32 * w) * 8192 + h * 256 + db * 128;
#pragma unroll
      for (int i = 0; i < 4; ++i)
        dma16((unsigned)((8 * i + (lane >> 3)) * 8192 + (((lane & 7) ^ ((((lane >> 4) & 1) << 1) | ((i & 1) << 2))) * 16)), vb, (unsigned)(L_WUV + (32 * w + 8 * i) * 128));
    }
    if (w == 6) {
      v4u s_ = {SENT, SENT, SENT, SENT};
      GST4(s_, (unsigned)(OFF_OP + (h * 4 + j) * 1280 + lane * 16), ws, "sc1");
      if (lane < 16) GST4(s_, (unsigned)(OFF_OP + (h * 4 + j) * 1280 + 1024 + lane * 16), ws, "sc1");
    }
    unsigned ovo[5];
    const char* ob = wo + (size_t)(h * 128 + 64 * db) * 1280;
    {
#pragma unroll
      for (int i = 0; i < 5; ++i) {
        const unsigned n = 64u * (unsigned)(w + 8 * i) + (unsigned)lane, row = (n * 52429u) >> 22, pos = n - row * 80u;
        ovo[i] = row * 1280u + ((pos ^ (((row & 3u) << 1) | (((row >> 3) & 1u) << 3))) * 16u);
      }
      RAWBAR();
#pragma unroll
      for (int i = 0; i < 5; ++i) dma16(ovo[i], ob + ca * 40960, (unsigned)(L_WO + ca * 40960 + (w + 8 * i) * 1024));
      if (!LATEH) {
        RAWBAR();
#pragma unroll
        for (int i = 0; i < 5; ++i) dma16(ovo[i], ob + (1 - ca) * 40960, (unsigned)(L_WO + (1 - ca) * 40960 + (w + 8 * i) * 1024));
      }
    }
    if (lane < 8) sts4(L_TAB + w * 512 + 256 + lane * 16, v4u{0u, 0u, 0u, 0u});
    unsigned pvo = (unsigned)(LAY1 ? OFF_PP + h * 32768 + (8 * ca + w) * 2048 + n16 * 64 + g4 * 16 : OFF_PP + (h * 32 + n16) * 1024 + 512 * ca + 64 * w + 16 * g4), pvo2 = pvo + (LAY1 ? 1024u : 16384u);
    unsigned sto = (unsigned)(OFF_ST + ((h >> 3) * 32 + (lane & 31)) * 64 + (h & 7) * 8);
    unsigned crw = L_CRED + lane * 32 + w * 4, crr = L_CRED + lane * 32;
    unsigned tabh = L_TAB + w * 512 + lane * 2;
    unsigned opo = (unsigned)(OFF_OP + (h * 4 + j) * 1280 + 160 * w + (OV3 ? 32 * g4 + 2 * n16 : 32 * n16 + 8 * g4));
    unsigned cw_w = L_CRED + w * 160 + lane * 2, cw_r = L_CRED + n16 * 160 + g4 * 16;
    TIE(cw_w); TIE(cw_r);
    unsigned bva[4], woa[5], bta[5];
    {
      const int q = (lane & 15) >> 2, p = lane & 3;
#pragma unroll
      for (int dt = 0; dt < 4; ++dt)
        bva[dt] = L_WUV + (32 * w + 8 * g4 + q) * 128 + (((2 * dt + (p >> 1)) ^ (((q >> 1) << 1) | ((g4 & 1) << 2))) * 16) + 8 * (p & 1);
#pragma unroll
      for (int t = 0; t < 5; ++t) {
        woa[t] = L_WO + (8 * g4 + q) * 1280 + (((10 * w + 2 * t + (p >> 1)) ^ ((q << 1) | ((g4 & 1) << 3))) * 16) + 8 * (p & 1);
        bta[t] = L_TAB + w * 512 + ((n16 & 7) == t ? (n16 >> 3) * 128 : 256) + g4 * 16;
      }
    }
    TIE(pvo); TIE(pvo2); TIE(sto); TIE(crw); TIE(crr); TIE(tabh); TIE(opo);
#pragma unroll
    for (int t = 0; t < 5; ++t) { TIE(woa[t]); TIE(bta[t]); }
    if (LATEH) { if (w == 6) VMWAIT(7); else VMWAIT(5); } else { if (w == 6) VMWAIT(12); else VMWAIT(10); }
    STAMP(13);
    v4u bv[4];
#pragma unroll
    for (int dt = 0; dt < 4; ++dt) bv[dt] = ldstr(bva[dt], bva[dt] + 512);
    if (w == 7) { FLAG2_CHECK(1); }
    VMWAIT(0);
    if (w == 6 && lane == 0) { v2u f = {(unsigned)epoch, (unsigned)(epoch >> 32)}; GST2(f, (unsigned)(OFF_FLAG2 + b * 8), ws, "sc1"); }
    STAMP(6);
    BARRIER();
    const unsigned wko = (unsigned)ca * 40960u, wkx = 40960u - wko;
    v4u wa[5][2];
#pragma unroll
    for (int t = 0; t < 5; ++t) { wa[t][0] = ldstr(woa[t] + wko, woa[t] + wko + 5120); if (!LATEH) wa[t][1] = ldstr(woa[t] + wkx, woa[t] + wkx + 5120); }
    SCHED();
    v2u st;
    v4u pa[2];
    if (w == 0) {
      if (now32() - tpub < (unsigned)NSKIP) {
        const unsigned nvo = (unsigned)(OFF_NOTE + ((h >> 3) * 32 + (lane & 31)) * 128);
        unsigned tread = 0;
        for (unsigned spin = 0; spin < SPIN_CAP; ++spin) {
          v2u nf;
          GLD2(nf, nvo, ws, 0, "sc1");
          VMWAIT(0); TIE(nf);
          if (__builtin_amdgcn_ballot_w64(nf.x != (unsigned)epoch) == 0ull) {
            float d = (float)(int)(nf.y - tpub);
            d = p16_max(row_max(d));
            tread = tpub + (unsigned)__builtin_amdgcn_readfirstlane((int)d) + (unsigned)NDELTA;
            for (unsigned k = 0; k < 4096u && (int)(tread - now32()) > 0; ++k) __builtin_amdgcn_s_sleep(1);
            break;
          }
        }
      }
      if (lane == 0) sts1(L_OK, 2u);
      GLD2(st, sto, ws, 0, "sc1");
    } else {
      for (unsigned spin = 0; *(volatile LAS unsigned*)(size_t)L_OK < 2u && spin < 64u * SPIN_CAP; ++spin) __builtin_amdgcn_s_sleep(1);
    }
    GLD4(pa[0], pvo, ws, 0, "sc1"); GLD4(pa[1], pvo2, ws, 0, "sc1");
    if (LATEH) {
      RAWBAR();
#pragma unroll
      for (int i = 0; i < 5; ++i) dma16(ovo[i], ob + wkx, (unsigned)(L_WO + wkx + (w + 8 * i) * 1024));
    }
    STAMP(7);
    if (w == 0) {
      if (LATEH) VMWAIT(7); else VMWAIT(2);
      TIE(st);
      unsigned again = 0;
      if (__builtin_expect(!allvalid(umax(st.x, st.y)), 0)) {
        again = 1;
        v2u s2;
        for (unsigned spin = 0;; ++spin) {
          GLD2(s2, sto, ws, 0, "sc1");
          VMWAIT(0); TIE(s2);
          if (allvalid(umax(s2.x, s2.y)) || spin > SPIN_CAP) break;
        }
        st = s2;
      }
      float m = asf(st.x), l = asf(st.y);
      float M = p16_max(row_max(m));
      float a = __builtin_amdgcn_exp2f((m - M) * SC);
      float Ls = p16_sum(row_sum(a * l));
      a = a * __builtin_amdgcn_rcpf(Ls);
      if (lane < 32) sts1(L_AW + lane * 4, asu(a));
      LGWAIT();
      if (lane == 0) sts1(L_OK, 3u + again);
    }
    for (unsigned spin = 0; *(volatile LAS unsigned*)(size_t)L_OK < 3u && spin < 64u * SPIN_CAP; ++spin) __builtin_amdgcn_s_sleep(1);
    f4 aw[2];
    aw[0] = ldsf4(L_AW + g4 * 16); aw[1] = ldsf4(L_AW + 64 + g4 * 16);
    if (LATEH) VMWAIT(5); else VMWAIT(0);
    TIE(pa[0]); TIE(pa[1]);
    if (__builtin_expect(!allvalid(umax(mx4(pa[0]), mx4(pa[1]))), 0)) {
      v4u pb0, pb1;
      for (unsigned spin = 0;; ++spin) {
        GLD4(pb0, pvo, ws, 0, "sc1"); GLD4(pb1, pvo2, ws, 0, "sc1");
        VMWAIT(0); TIE(pb0); TIE(pb1);
        if (allvalid(umax(mx4(pb0), mx4(pb1))) || spin > SPIN_CAP) break;
      }
      pa[0] = pb0; pa[1] = pb1;
    }
    STAMP(8);
    {
      const f4 z = {0.f, 0.f, 0.f, 0.f};
      f4 t0_[4], t1_[4];
#pragma unroll
      for (int dt = 0; dt < 4; ++dt) { t0_[dt] = mfma(pa[0], bv[dt], z); t1_[dt] = mfma(pa[1], bv[dt], z); }
      SCHED();
      float u[4];
#pragma unroll
      for (int dt = 0; dt < 4; ++dt) { f4 c = aw[0] * t0_[dt] + aw[1] * t1_[dt]; u[dt] = (c.x + c.y) + (c.z + c.w); }
      auto t0 = __builtin_amdgcn_permlane16_swap(asu(u[0]), asu(u[1]), false, false); auto t1 = __builtin_amdgcn_permlane16_swap(asu(u[2]), asu(u[3]), false, false);
      unsigned x0 = t0[0], x1 = t0[1], y0 = t1[0], y1 = t1[1];
      auto t2 = __builtin_amdgcn_permlane32_swap(asu(asf(x0) + asf(x1)), asu(asf(y0) + asf(y1)), false, false);
      unsigned z0 = t2[0], z1 = t2[1];
      const float c = asf(z0) + asf(z1);
      const unsigned hi = cvtpk(c, c);
      const float rs = c - asf(hi << 16);
      const unsigned lo = cvtpk(rs, rs);
      *(LAS unsigned short*)(size_t)cw_w = (unsigned short)hi;
      *(LAS unsigned short*)(size_t)(cw_w + 8 * 160) = (unsigned short)lo;
    }
    if (LATEH) VMWAIT(0);
    BARRIER();
    STAMP(9);
    {
      const v4u ca0 = lds4(cw_r + ca * 64), ca1 = lds4(cw_r + (1 - ca) * 64);
      if (LATEH) {
#pragma unroll
        for (int t = 0; t < 5; ++t) wa[t][1] = ldstr(woa[t] + wkx, woa[t] + wkx + 5120);
      }
      SCHED();
      const f4 z = {0.f, 0.f, 0.f, 0.f};
      f4 d[5];
#pragma unroll
      for (int t = 0; t < 5; ++t) d[t] = mfma(ca0, wa[t][0], z);
#pragma unroll
      for (int t = 0; t < 5; ++t) d[t] = mfma(ca1, wa[t][1], d[t]);
      SCHED();
      float v[5];
#pragma unroll
      for (int t = 0; t < 5; ++t) v[t] = (d[t].x + d[t].y) + (d[t].z + d[t].w);
      auto t0 = __builtin_amdgcn_permlane16_swap(asu(v[0]), asu(v[1]), false, false); auto t1 = __builtin_amdgcn_permlane16_swap(asu(v[2]), asu(v[3]), false, false);
      unsigned x0 = t0[0], x1 = t0[1], y0 = t1[0], y1 = t1[1];
      auto t2 = __builtin_amdgcn_permlane32_swap(asu(asf(x0) + asf(x1)), asu(asf(y0) + asf(y1)), false, false);
      unsigned z0 = t2[0], z1 = t2[1];
      const float va = asf(z0) + asf(z1), vb = p32_sum(p16_sum(v[4]));
      const unsigned pa_ = cvtpk(va, dpp<XOR1>(va)), pb_ = cvtpk(vb, dpp<XOR1>(vb));
      v4u oa = {guard(pa_), guard(dppu<0x102>(pa_)), guard(dppu<0x104>(pa_)), guard(dppu<0x106>(pa_))};
      v4u ob_ = {guard(pb_), guard(dppu<0x102>(pb_)), guard(dppu<0x104>(pb_)), guard(dppu<0x106>(pb_))};
      if ((n16 & 7) == 0) GST4(oa, opo, ws, "sc1");
      if ((n16 & 7) == 0 && g4 == 0) GST4(ob_, opo + 128u, ws, "sc1");
    }
    STAMP(10);
  } else {
    const int g = rr & 3, s = xcd * 4 + (rr >> 2), bi = xcd * 16 + rr;
    RAWBAR();
    if (w < 5) {
      CKV_ROWS(0, 16);
    } else {
      CKV_ROWS(NPRE2, 16);
      v2u f1;
      unsigned f1o;
      { const int hd = 8 * g + ((lane & 31) >> 2); f1o = (unsigned)(OFF_FLAG1 + ((hd >> 2) + 8 * ((hd & 3) * 4 + (lane & 3))) * 128); }
      if (w == 7) GLD2(f1, f1o, ws, 0, "sc1");
      if (w == 5) { dma16((unsigned)(lane * 16), x, L_QB); if (lane < 16) dma16((unsigned)(lane * 16), x + 1024, L_QB + 1024); }
      if (w == 5) VMWAIT(18); else if (w == 6) VMWAIT(16); else VMWAIT(17);
      unsigned old = 0;
      if (lane == 0) old = __hip_atomic_fetch_add((LAS unsigned*)(size_t)L_CNT, 1u, __ATOMIC_RELAXED, __HIP_MEMORY_SCOPE_WORKGROUP);
      if (lane == 0 && old == 2u) { v2u f = {(unsigned)epoch, (unsigned)(epoch >> 32)}; GST2(f, (unsigned)(OFF_FLAG2 + b * 8), ws, "sc1"); }
      if (w == 7) {
        VMWAIT(0); TIE(f1);
        for (unsigned spin = 0; spin < SPIN_CAP; ++spin) {
          const unsigned df = (f1.x ^ (unsigned)epoch) | (f1.y ^ (unsigned)(epoch >> 32));
          if (__builtin_amdgcn_ballot_w64(df != 0u) == 0ull) break;
          GLD2(f1, f1o, ws, 0, "sc1");
          VMWAIT(0); TIE(f1);
        }
        CODEPF_DONE();
        if (lane == 0) sts1(L_OK, 1u);
      }
    }
    unsigned qvo = (unsigned)(OFF_QA + (8 * g + w) * 1024 + lane * 16);
    unsigned qsw = L_QAS + w * 1056 + lane * 16, qsr = L_QAS + (n16 & 7) * 1056 + g4 * 16;
    unsigned mxw = L_MX + lane * 32 + w * 4, mxr = L_MX + (n16 & 7) * 32;
    unsigned pw = L_P + n16 * 288 + (16 * kt + 4 * g4) * 2, pr = L_P + (n16 & 7) * 288 + g4 * 16;
    unsigned lsw = L_LS + lane * 32 + w * 4;
    unsigned ppo = PP_SLOT(g, s, w);
    unsigned ka[4], ta[4][2];
    {
      const unsigned kr = L_CKV + (16 * kt + n16) * 1024;
#pragma unroll
      for (int u = 0; u < 4; ++u) ka[u] = kr + (((4 * u + g4) ^ n16) * 16);
      const int q = (lane & 15) >> 2, p = lane & 3;
#pragma unroll
      for (int t = 0; t < 4; ++t) {
        const int ch = 8 * w + 4 * (p >> 1) + t;
        const int r0 = 8 * g4 + q, r1 = r0 + 4;
        ta[t][0] = L_CKV + r0 * 1024 + ((ch ^ (r0 & 15)) * 16) + 8 * (p & 1);
        ta[t][1] = L_CKV + r1 * 1024 + ((ch ^ (r1 & 15)) * 16) + 8 * (p & 1);
      }
    }
    TIE(qvo); TIE(qsw); TIE(qsr); TIE(mxw); TIE(mxr); TIE(pw); TIE(pr); TIE(lsw); TIE(ppo);
#pragma unroll
    for (int u = 0; u < 4; ++u) { TIE(ka[u]); TIE(ta[u][0]); TIE(ta[u][1]); }
    VMWAIT(0);
    STAMP(4);
    for (unsigned spin = 0; *(volatile LAS unsigned*)(size_t)L_OK == 0u && spin < SPIN_CAP; ++spin) __builtin_amdgcn_s_sleep(1);
    v4u qa;
    GLD4(qa, qvo, ws, 0, "sc1");
    v4u kA[16];
#pragma unroll
    for (int ks = 0; ks < 16; ++ks) kA[ks] = lds4(ka[ks & 3] + (ks >> 2) * 256);
    BARRIER();
    STAMP(5);
    v4u vA[4][4];
#pragma unroll
    for (int t = 0; t < 4; ++t) {
      vA[0][t] = ldstr(ta[t][0], ta[t][1]); vA[1][t] = ldstr(ta[t][0] + 32768, ta[t][1] + 32768);
      const unsigned b0 = ta[t][0] + 65536, b1 = ta[t][1] + 65536;
      vA[2][t] = ldstr(b0, b1); vA[3][t] = ldstr(b0 + 32768, b1 + 32768);
    }
    SCHED();
    VMWAIT(0); TIE(qa);
    if (__builtin_expect(!allvalid(mx4(qa)), 0)) {
      v4u qb;
      for (unsigned spin = 0;; ++spin) {
        GLD4(qb, qvo, ws, 0, "sc1");
        VMWAIT(0); TIE(qb);
        if (allvalid(mx4(qb)) || spin > SPIN_CAP) break;
      }
      qa = qb;
    }
    STAMP(6);
    sts4(qsw, qa);
    BARRIER();
    if (NOTICE && w == 7) { const unsigned tn = now32(); if (lane == 0) { v2u f = {(unsigned)epoch, tn}; GST2(f, (unsigned)(OFF_NOTE + (g * 32 + s) * 128), ws, "sc1"); } }
    const int fb = bi < 108 ? bi : 107;
    const char* b1 = w1 + fb * 32;
    const unsigned vo1 = (unsigned)((lane >> 1) * 3456 + (lane & 1) * 16);
    const int pq = 5 * (bi & 3);
    f4 sc = {0.f, 0.f, 0.f, 0.f};
    {
      v4u bq[16];
#pragma unroll
      for (int ks = 0; ks < 16; ++ks) bq[ks] = lds4(qsr + ks * 64);
      const char* b2 = w2 + (size_t)(16 * fb) * 1280 + w * 1024;
      const unsigned vo2 = (unsigned)(lane * 16);
      const int p0 = pq + w;
      SCHED();
#pragma unroll
      for (int ks = 0; ks < 16; ++ks) {
        sc = mfma(kA[ks], bq[ks], sc);
        if (MLP1 == 1 && ks == 2) { SCHED(); if (w < 5) dma16(vo1, b1 + (size_t)(32 * p0) * 3456, L_W1 + p0 * 1024); SCHED(); }
        if (MLP1 == 1 && ks == 5) { SCHED(); dma16(vo2, b2, L_W2 + w * 1024); SCHED(); }
        if (MLP1 == 1 && ks == 8) { SCHED(); dma16(vo2, b2 + 8192, L_W2 + (w + 8) * 1024); SCHED(); }
        if (MLP1 == 1 && ks == 11) { SCHED(); if (w < 4) dma16(vo2, b2 + 16384, L_W2 + (w + 16) * 1024); SCHED(); }
      }
      float mw = p32_max(p16_max(fmx(fmx(sc.x, sc.y), fmx(sc.z, sc.w))));
      if (lane < 8) sts1(mxw, asu(mw));
      if (MLP1 == 2 && w < 4) {
        const char* b2w = w2 + (size_t)(16 * fb) * 1280;
#pragma unroll
        for (int i = 0; i < 5; ++i) dma16((unsigned)(lane * 16), b2w + (5 * w + i) * 1024, L_W2 + (5 * w + i) * 1024);
        dma16(vo1, b1 + (size_t)(32 * (pq + w)) * 3456, L_W1 + (pq + w) * 1024);
        if (w == 0) dma16(vo1, b1 + (size_t)(32 * (pq + 4)) * 3456, L_W1 + (pq + 4) * 1024);
      }
    }
    BARRIER();
    STAMP(7);
    {
      const float Ms = -max8(ldsf4(mxr), ldsf4(mxr + 16)) * SC;
      float p0 = __builtin_amdgcn_exp2f(fmaf(sc.x, SC, Ms)), p1 = __builtin_amdgcn_exp2f(fmaf(sc.y, SC, Ms)), p2 = __builtin_amdgcn_exp2f(fmaf(sc.z, SC, Ms)), p3 = __builtin_amdgcn_exp2f(fmaf(sc.w, SC, Ms));
      v2u pk = {cvtpk(p0, p1), cvtpk(p2, p3)};
      if (n16 < 8) sts2(pw, pk);
      float ls = p32_sum(p16_sum((p0 + p1) + (p2 + p3)));
      if (lane < 8) sts1(lsw, asu(ls));
    }
    BARRIER();
    STAMP(8);
    {
      v4u pb[4];
#pragma unroll
      for (int ks = 0; ks < 4; ++ks) pb[ks] = lds4(pr + ks * 64);
      f4 lu, lv, mu, mv;
      if (w == STW) { const unsigned sa = (lane & 7) * 32; lu = ldsf4(L_LS + sa); lv = ldsf4(L_LS + sa + 16); mu = ldsf4(L_MX + sa); mv = ldsf4(L_MX + sa + 16); }
      SCHED();
      f4 ac[4];
#pragma unroll
      for (int t = 0; t < 4; ++t) ac[t] = mfma(vA[0][t], pb[0], f4{0.f, 0.f, 0.f, 0.f});
      if (w == STW) {
        v2u o = {guard(asu(max8(mu, mv))), guard(asu(sum8(lu, lv)))};
        if (lane < 8) GST2(o, (unsigned)(OFF_ST + (g * 32 + s) * 64 + lane * 8), ws, "sc1");
      }
#pragma unroll
      for (int ks = 1; ks < 4; ++ks)
#pragma unroll
        for (int t = 0; t < 4; ++t) ac[t] = mfma(vA[ks][t], pb[ks], ac[t]);
      const bool up = n16 >= 8;
      f4 xa = up ? ac[2] : ac[0], ya = up ? ac[3] : ac[1];
      unsigned x0 = cvtpk(xa.x, xa.y), x1 = cvtpk(xa.z, xa.w), y0 = cvtpk(ya.x, ya.y), y1 = cvtpk(ya.z, ya.w);
      auto s0 = __builtin_amdgcn_permlane16_swap(x0, y0, false, false); auto s1 = __builtin_amdgcn_permlane16_swap(x1, y1, false, false);
      unsigned e0 = s0[0], e1 = s1[0], f0 = s0[1], f1 = s1[1];
      v4u o = {guard(e0), guard(e1), guard(f0), guard(f1)};
      GST4(o, ppo, ws, "sc1");
    }
    STAMP(9);
    const unsigned tpv_ = now32();
    const bool fin = bi < 80 && w == 5;
    v4u ff0, ff1; unsigned gpre[5];
    if (FINPF && fin) {
      GLD4(ff0, (unsigned)(OFF_FLAG2 + lane * 16), ws, 0, "sc1"); GLD4(ff1, (unsigned)(OFF_FLAG2 + 1024 + lane * 16), ws, 0, "sc1");
#pragma unroll
      for (int i = 0; i < 5; ++i) GLD1(gpre[i], (unsigned)(lane * 4), gw, i * 256, "");
    }
    RAWBAR();
    int pc0 = pq + 5 + w, pc1 = pq + 13 + w;
    if (pc0 >= 20) pc0 -= 20;
    if (pc1 >= 20) pc1 -= 20;
    if (!MLP1 && !EXP_NOMLP) {
      const char* b2 = w2 + (size_t)(16 * fb) * 1280 + w * 1024;
      const unsigned vo2 = (unsigned)(lane * 16);
      if (w < 5) dma16(vo1, b1 + (size_t)(32 * (pq + w)) * 3456, L_W1 + (pq + w) * 1024);
      dma16(vo2, b2, L_W2 + w * 1024); dma16(vo2, b2 + 8192, L_W2 + (w + 8) * 1024); if (w < 4) dma16(vo2, b2 + 16384, L_W2 + (w + 16) * 1024);
      RAWBAR();
    }
    if (!EXP_NOMLP) {
    dma16(vo1, b1 + (size_t)(32 * pc0) * 3456, L_W1 + pc0 * 1024);
    if (w < 7) dma16(vo1, b1 + (size_t)(32 * pc1) * 3456, L_W1 + pc1 * 1024); }
    VMWAIT(0);
    if (MLP1 == 2) BARRIER();
    STAMP(12);
    {
      const int q = (lane & 15) >> 2, p = lane & 3;
      const unsigned wa = L_W1 + (8 * g4 + q) * 32 + 8 * p, xa = L_QB + g4 * 16;
      f4 hacc = {0.f, 0.f, 0.f, 0.f};
      hacc = mfma(lds4(xa + pc0 * 64), ldstr(wa + pc0 * 1024, wa + pc0 * 1024 + 128), hacc);
      if (w < 7) hacc = mfma(lds4(xa + pc1 * 64), ldstr(wa + pc1 * 1024, wa + pc1 * 1024 + 128), hacc);
      if (w < 5) { const int p0 = pq + w; hacc = mfma(lds4(xa + p0 * 64), ldstr(wa + p0 * 1024, wa + p0 * 1024 + 128), hacc); }
      if (lane < 16) sts1(L_RED + lane * 32 + w * 4, asu(hacc.x));
    }
    BARRIER();
    const unsigned tmp_ = now32() + 40u;
    STAMP(15);
    if (w < 5) {
      v4u w2r[4];
#pragma unroll
      for (int i = 0; i < 4; ++i) w2r[i] = lds4(L_W2 + (4 * g4 + i) * 1280 + w * 256 + n16 * 16);
      const int f = lane & 15;
      float hh = sum8(ldsf4(L_RED + f * 32), ldsf4(L_RED + f * 32 + 16));
      hh = hh * __builtin_amdgcn_rcpf(1.f + __expf(-hh));
      if (bi >= 108) hh = 0.f;
      unsigned hi = cvtpk(hh, hh);
      float rs = hh - asf(hi << 16);
      unsigned lo = cvtpk(rs, rs);
      if (lane < 32) sts1(L_TAB + w * 512 + lane * 4, lane < 16 ? hi : lo);
      LGWAIT();
      v4u t0 = lds4(L_TAB + w * 512 + ((n16 >> 3) * 16 + 4 * g4) * 4);
      unsigned dv[4] = {t0.x, t0.y, t0.z, t0.w};
      f4 acc = {0.f, 0.f, 0.f, 0.f};
#pragma unroll
      for (int i = 0; i < 4; ++i) { v4u a = {dv[i] & msk[0], dv[i] & msk[1], dv[i] & msk[2], dv[i] & msk[3]}; acc = mfma(a, w2r[i], acc); }
      float o0 = p32_sum(acc.x), o1 = p32_sum(acc.y), o2 = p32_sum(acc.z), o3 = p32_sum(acc.w);
      unsigned a0 = guard(cvtpk(o0, o1)), a1 = guard(cvtpk(o2, o3));
      auto s0 = __builtin_amdgcn_permlane16_swap(a0, a0, false, false); auto s1 = __builtin_amdgcn_permlane16_swap(a1, a1, false, false);
      unsigned b0 = s0[1], b1 = s1[1];
      v4u o = {a0, a1, b0, b1};
      if (lane < 16) GST4(o, (unsigned)(OFF_MP + bi * 1280 + w * 256 + lane * 16), ws, "sc1");
    }
    STAMP(10);
    if (bi < 80 && w == 5) {
      unsigned gg[5];
      if (FINPF) {
        TIE(ff0); TIE(ff1);
#pragma unroll
        for (int i = 0; i < 5; ++i) { TIE(gpre[i]); gg[i] = gpre[i]; }
        const unsigned lo = (unsigned)epoch, hi = (unsigned)(epoch >> 32);
        const unsigned df = ((ff0.x ^ lo) | (ff0.y ^ hi)) | ((ff0.z ^ lo) | (ff0.w ^ hi)) | ((ff1.x ^ lo) | (ff1.y ^ hi)) | ((ff1.z ^ lo) | (ff1.w ^ hi));
        if (__builtin_amdgcn_ballot_w64(df != 0u) != 0ull) { FLAG2_CHECK(0); FLAG2_CHECK(1); }
      } else {
        FLAG2_CHECK(0);
        FLAG2_CHECK(1);
#pragma unroll
        for (int i = 0; i < 5; ++i) GLD1(gg[i], (unsigned)(lane * 4), gw, i * 256, "");
        VMWAIT(0);
#pragma unroll
        for (int i = 0; i < 5; ++i) TIE(gg[i]);
      }
      float gate;
      {
        float a = 0.f;
#pragma unroll
        for (int i = 0; i < 5; ++i) { const unsigned gx = *(LAS unsigned*)(size_t)(L_QB + i * 256 + lane * 4); a += asf(gx << 16) * asf(gg[i] << 16) + asf(gx & 0xFFFF0000u) * asf(gg[i] & 0xFFFF0000u); }
        a = p32_sum(p16_sum(row_sum(a)));
        gate = __builtin_amdgcn_rcpf(1.f + __expf(-a));
      }
      const unsigned one = 0x3F803F80u;
      v4u ones = {one & msk[0], one & msk[1], one & msk[2], one & msk[3]};
      {
        const unsigned ta_ = tmp_ + (unsigned)FDELTA, tb_ = tpv_ + (unsigned)ODELTA, tr_ = (int)(ta_ - tb_) > 0 ? ta_ : tb_;
        for (unsigned k = 0; k < 4096u && (int)(tr_ - now32()) > 0; ++k) __builtin_amdgcn_s_sleep(1);
      }
      v4u m0, m1, o0_, o1_;
      const unsigned mvo = (unsigned)(OFF_MP + lane * 1280 + bi * 16), mvo2 = mvo + 64 * 1280;
      const unsigned ovo = (unsigned)(OFF_OP + lane * 1280 + bi * 16), ovo2 = ovo + 64 * 1280;
      GLD4(m0, mvo, ws, 0, "sc1"); GLD4(m1, mvo2, ws, 0, "sc1"); GLD4(o0_, ovo, ws, 0, "sc1"); GLD4(o1_, ovo2, ws, 0, "sc1");
      VMWAIT(0); TIE(m0); TIE(m1); TIE(o0_); TIE(o1_);
      if (!allvalid(umax(mx4(m0), mx4(m1)))) {
        v4u a0_, a1_;
        for (unsigned spin = 0;; ++spin) {
          GLD4(a0_, mvo, ws, 0, "sc1"); GLD4(a1_, mvo2, ws, 0, "sc1");
          VMWAIT(0); TIE(a0_); TIE(a1_);
          if (allvalid(umax(mx4(a0_), mx4(a1_))) || spin > SPIN_CAP) break;
        }
        m0 = a0_; m1 = a1_;
      }
      STAMP(11);
      if (!allvalid(umax(mx4(o0_), mx4(o1_)))) {
        v4u a0_, a1_;
        for (unsigned spin = 0;; ++spin) {
          GLD4(a0_, ovo, ws, 0, "sc1"); GLD4(a1_, ovo2, ws, 0, "sc1");
          VMWAIT(0); TIE(a0_); TIE(a1_);
          if (allvalid(umax(mx4(a0_), mx4(a1_))) || spin > SPIN_CAP) break;
        }
        o0_ = a0_; o1_ = a1_;
      }
      STAMP(12);
      f4 bb = {0.f, 0.f, 0.f, 0.f}, aa = {0.f, 0.f, 0.f, 0.f};
      bb = mfma(ones, m0, bb); aa = mfma(ones, o0_, aa); bb = mfma(ones, m1, bb); aa = mfma(ones, o1_, aa);
      bb *= (1.f - gate);
      float r0 = row_sum(fmaf(gate, aa.x, bb.x)), r1 = row_sum(fmaf(gate, aa.y, bb.y));
      float r2 = row_sum(fmaf(gate, aa.z, bb.z)), r3 = row_sum(fmaf(gate, aa.w, bb.w));
      v2u o = {cvtpk(r0, r1), cvtpk(r2, r3)};
      if (n16 == 0 && g4 < 2) GST2(o, (unsigned)(bi * 16 + g4 * 8), out, "");
      if (bi == 0 && lane == 0) {
        v2u f = {(unsigned)epoch, (unsigned)(epoch >> 32)};
        GST2(f, (unsigned)OFF_E, ws, "sc1");
      }
      STAMP(13);
    }
  }
  asm volatile(".Lgm_code_end:" ::: "memory");
}

extern "C" void kernel_launch(void* const* d_in, const int* in_sizes, int n_in,
                              void* d_out, int out_size, void* d_ws, size_t ws_size,
                              hipStream_t stream) {
  gm_kernel<<<256, 512, 0, stream>>>((const char*)d_in[0], (const char*)d_in[1], (const char*)d_in[2], (const char*)d_in[3],
                                      (const char*)d_in[4], (const char*)d_in[5], (const char*)d_in[7], (const char*)d_in[8],
                                      (const char*)d_in[9], (char*)d_out, (char*)d_ws);
}
```

```cpp
#include <hip/hip_runtime.h>
#include <stdint.h>
#pragma clang diagnostic ignored "-Winline-asm"

typedef unsigned v4u __attribute__((ext_vector_type(4)));
typedef unsigned v2u __attribute__((ext_vector_type(2)));
typedef float f4 __attribute__((ext_vector_type(4)));
typedef float f2 __attribute__((ext_vector_type(2)));
typedef short s4 __attribute__((ext_vector_type(4)));
typedef short s8 __attribute__((ext_vector_type(8)));
typedef __bf16 bf2 __attribute__((ext_vector_type(2)));
typedef unsigned long long u64;
#define LAS __attribute__((address_space(3)))
#define DI __device__ __forceinline__

constexpr unsigned OFF_E = 0;
constexpr unsigned OFF_FLAG2 = 4096;
constexpr unsigned OFF_FLAG1 = 8192;
constexpr unsigned OFF_QP = OFF_FLAG1 + 32768;
constexpr unsigned OFF_QA = OFF_QP + 65536;
constexpr unsigned OFF_ST = OFF_QA + 32768;
constexpr unsigned OFF_OP = OFF_ST + 8192;
constexpr unsigned OFF_MP = OFF_OP + 163840;
constexpr unsigned OFF_PP = OFF_MP + 163840;
constexpr unsigned OFF_NOTE = OFF_PP + 1048576;
constexpr unsigned SENT = 0xFFFFFFFFu;
constexpr unsigned SPIN_CAP = 1u << 15;
constexpr float SC = 0.08838834764831845f * 1.4426950408889634f;
constexpr unsigned T_PART = 125;
constexpr unsigned T_LATE = 290;
constexpr unsigned T_OUT = 185;
constexpr unsigned T_MLP = 100;
constexpr int EARLY_ROWS = 6;

constexpr unsigned L_CKV = 0;
constexpr unsigned L_W1 = 0, L_W2 = 32768;
constexpr unsigned L_UK = 32768;
constexpr unsigned L_WUV = 0, L_WO = 32768;
constexpr unsigned L_QRED = 131072;
constexpr unsigned L_QAS = 131072;
constexpr unsigned L_CTX = 131072;
constexpr unsigned L_AW = 131072 + 2560;
constexpr unsigned L_P = L_QAS + 8448;
constexpr unsigned L_MX = L_P + 2304;
constexpr unsigned L_LS = L_MX + 256;
constexpr unsigned L_RED = L_LS + 256;
constexpr unsigned L_TAB = L_RED + 1024;
constexpr unsigned L_QB = L_TAB + 4096;
constexpr unsigned L_EP = L_QB + 2304;
constexpr unsigned L_OK = L_EP + 8;
constexpr unsigned L_CNT = L_EP + 12;
constexpr unsigned L_END = L_EP + 16;

DI f4 mfma(v4u a, v4u b, f4 c) { return __builtin_amdgcn_mfma_f32_16x16x32_bf16(__builtin_bit_cast(s8, a), __builtin_bit_cast(s8, b), c, 0, 0, 0); }
DI unsigned cvtpk(float lo, float hi) { f2 x = {lo, hi}; bf2 y = __builtin_convertvector(x, bf2); return __builtin_bit_cast(unsigned, y); }
DI float asf(unsigned u) { return __uint_as_float(u); }
DI unsigned asu(float f) { return __float_as_uint(f); }
DI unsigned guard(unsigned u) { return u < 0xFFFFFFFEu ? u : 0xFFFFFFFEu; }
DI float fmx(float a, float b) { return __builtin_elementwise_maximum(a, b); }
template <int CTRL> DI float dpp(float x) { return asf((unsigned)__builtin_amdgcn_mov_dpp((int)asu(x), CTRL, 0xf, 0xf, true)); }
template <int CTRL> DI unsigned dppu(unsigned x) { return (unsigned)__builtin_amdgcn_mov_dpp((int)x, CTRL, 0xf, 0xf, true); }
constexpr int XOR1 = 0xB1, XOR2 = 0x4E, ROR4 = 0x124, ROR8 = 0x128, HMIR = 0x141, SHL2 = 0x102, SHL4 = 0x104, SHL6 = 0x106;
DI float p16_max(float x) { auto t = __builtin_amdgcn_permlane16_swap(asu(x), asu(x), false, false); unsigned a = t[0], b = t[1]; return fmx(asf(a), asf(b)); }
DI float p32_max(float x) { auto t = __builtin_amdgcn_permlane32_swap(asu(x), asu(x), false, false); unsigned a = t[0], b = t[1]; return fmx(asf(a), asf(b)); }
DI float p16_sum(float x) { auto t = __builtin_amdgcn_permlane16_swap(asu(x), asu(x), false, false); unsigned a = t[0], b = t[1]; return asf(a) + asf(b); }
DI float p32_sum(float x) { auto t = __builtin_amdgcn_permlane32_swap(asu(x), asu(x), false, false); unsigned a = t[0], b = t[1]; return asf(a) + asf(b); }
DI float row_sum(float x) { x += dpp<XOR1>(x); x += dpp<XOR2>(x); x += dpp<ROR4>(x); x += dpp<ROR8>(x); return x; }
DI float row_max(float x) { x = fmx(x, dpp<XOR1>(x)); x = fmx(x, dpp<XOR2>(x)); x = fmx(x, dpp<ROR4>(x)); x = fmx(x, dpp<ROR8>(x)); return x; }
DI float max8(f4 u, f4 v) { return fmx(fmx(fmx(u.x, u.y), fmx(u.z, u.w)), fmx(fmx(v.x, v.y), fmx(v.z, v.w))); }
DI float sum8(f4 u, f4 v) { return ((u.x + u.y) + (u.z + u.w)) + ((v.x + v.y) + (v.z + v.w)); }
DI float sum4_scatter(float v0, float v1, float v2, float v3) {
  auto t0 = __builtin_amdgcn_permlane16_swap(asu(v0), asu(v1), false, false); auto t1 = __builtin_amdgcn_permlane16_swap(asu(v2), asu(v3), false, false);
  unsigned x0 = t0[0], x1 = t0[1], y0 = t1[0], y1 = t1[1];
  auto t2 = __builtin_amdgcn_permlane32_swap(asu(asf(x0) + asf(x1)), asu(asf(y0) + asf(y1)), false, false);
  unsigned z0 = t2[0], z1 = t2[1];
  return asf(z0) + asf(z1);
}
DI unsigned umax(unsigned a, unsigned b) { return a > b ? a : b; }
DI unsigned mx4(v4u v) { return umax(umax(v.x, v.y), umax(v.z, v.w)); }
DI bool allvalid(unsigned m) { return __builtin_amdgcn_ballot_w64(m == SENT) == 0ull; }
DI v4u lds4(unsigned a) { return *(LAS v4u*)(size_t)a; }
DI f4 ldsf4(unsigned a) { return *(LAS f4*)(size_t)a; }
DI unsigned lds1v(unsigned a) { return *(volatile LAS unsigned*)(size_t)a; }
DI void sts4(unsigned a, v4u v) { *(LAS v4u*)(size_t)a = v; }
DI void stsf4(unsigned a, f4 v) { *(LAS f4*)(size_t)a = v; }
DI void sts2(unsigned a, v2u v) { *(LAS v2u*)(size_t)a = v; }
DI void sts1(unsigned a, unsigned v) { *(LAS unsigned*)(size_t)a = v; }
DI void stsh(unsigned a, unsigned v) { *(LAS unsigned short*)(size_t)a = (unsigned short)v; }
DI v4u ldstr(unsigned a0, unsigned a1) {
  s4 lo = __builtin_amdgcn_ds_read_tr16_b64_v4i16((LAS s4*)(size_t)a0), hi = __builtin_amdgcn_ds_read_tr16_b64_v4i16((LAS s4*)(size_t)a1);
  s8 c = __builtin_shufflevector(lo, hi, 0, 1, 2, 3, 4, 5, 6, 7);
  return __builtin_bit_cast(v4u, c);
}

#define GLD4(dst, voff, sbase, IMM, MOD) asm volatile("global_load_dwordx4 %0, %1, %2 offset:%3 " MOD : "=v"(dst) : "v"(voff), "s"(sbase), "i"(IMM) : "memory")
#define GLD2(dst, voff, sbase, IMM, MOD) asm volatile("global_load_dwordx2 %0, %1, %2 offset:%3 " MOD : "=v"(dst) : "v"(voff), "s"(sbase), "i"(IMM) : "memory")
#define GLD1(dst, voff, sbase, IMM, MOD) asm volatile("global_load_dword %0, %1, %2 offset:%3 " MOD : "=v"(dst) : "v"(voff), "s"(sbase), "i"(IMM) : "memory")
#define GST4(src, voff, sbase, MOD) asm volatile("global_store_dwordx4 %0, %1, %2 " MOD "\n\ts_nop 1" :: "v"(voff), "v"(src), "s"(sbase) : "memory")
#define GST2(src, voff, sbase, MOD) asm volatile("global_store_dwordx2 %0, %1, %2 " MOD :: "v"(voff), "v"(src), "s"(sbase) : "memory")
#define SCHED() __builtin_amdgcn_sched_barrier(0)
#define VMWAIT(N) do { asm volatile("s_waitcnt vmcnt(%0)" :: "i"(N) : "memory"); SCHED(); } while (0)
#define LGWAIT() do { asm volatile("s_waitcnt lgkmcnt(0)" ::: "memory"); SCHED(); } while (0)
#define TIE(x) asm volatile("" : "+v"(x))
#define BARRIER() do { asm volatile("s_waitcnt lgkmcnt(0)\n\ts_barrier" ::: "memory"); SCHED(); } while (0)
#define RAWBAR() do { asm volatile("s_barrier" ::: "memory"); SCHED(); } while (0)
DI void dma16(unsigned voff, const void* sbase, unsigned ldsdst) {
  asm volatile("s_mov_b32 m0, %2\n\ts_nop 0\n\tglobal_load_lds_dwordx4 %0, %1" :: "v"(voff), "s"(sbase), "s"(ldsdst) : "memory", "m0");
}
DI unsigned now32() { u64 t; asm volatile("s_memrealtime %0\n\ts_waitcnt lgkmcnt(0)" : "=s"(t) :: "memory"); return (unsigned)t; }
DI void wait_until(unsigned t) { for (unsigned k = 0; k < 4096u && (int)(t - now32()) > 0; ++k) __builtin_amdgcn_s_sleep(1); }

__global__ void __launch_bounds__(512, 2)
gm_kernel(const char* __restrict__ x, const char* __restrict__ ckv, const char* __restrict__ wq, const char* __restrict__ wuk,
          const char* __restrict__ wuv, const char* __restrict__ wo, const char* __restrict__ w1, const char* __restrict__ w2,
          const char* __restrict__ gw, char* __restrict__ out, char* __restrict__ ws) {
  __shared__ __attribute__((aligned(16))) char smem[L_END];
  asm volatile("" :: "v"((LAS char*)smem));
  const int tid = threadIdx.x, lane = tid & 63;
  const int w = __builtin_amdgcn_readfirstlane(tid >> 6);
  const int b = blockIdx.x, xcd = b & 7, r = b >> 3;
  v4u codepf;
  {
    u64 pc; unsigned csz;
    asm volatile("s_getpc_b64 %0\n.Lgm_pc_here:\n\ts_mov_b32 %1, .Lgm_code_end-.Lgm_pc_here" : "=s"(pc), "=s"(csz));
    unsigned co = (unsigned)(r * 1024 + lane * 16);
    if (w == 7 && co + 16u <= csz) GLD4(codepf, co, (const char*)pc, 0, "");
  }
#define CODEPF_DONE() asm volatile("" :: "v"(codepf))
  const bool roleA = r < 16;
  const int g4 = lane >> 4, n16 = lane & 15;
  const int rr = r & 15;
  const int hq = xcd * 4 + (rr >> 2), j8 = (roleA ? 0 : 4) + (rr & 3);
  asm volatile("" :: "s"(x), "s"(ckv), "s"(wq), "s"(wuk), "s"(wuv), "s"(wo), "s"(w1), "s"(w2), "s"(gw), "s"(out), "s"(ws));
  unsigned msk[4];
#pragma unroll
  for (int t = 0; t < 4; ++t) msk[t] = (((n16 & 7) >> 1) == t) ? ((n16 & 1) ? 0xFFFF0000u : 0x0000FFFFu) : 0u;

  const int sB = xcd * 4 + (rr >> 2), gB = rr & 3;
#define PP_SLOT(W_) ((unsigned)(OFF_PP + (8 * gB + (n16 & 7)) * 32768 + (2 * (W_) + (g4 >> 1)) * 2048 + sB * 64 + (2 * (n16 >> 3) + (g4 & 1)) * 16))

  v4u wqr[4]; v2u xr, ew;
  if (w == 0) {
    GLD2(ew, 0u, ws, OFF_E, "sc1");
    v4u s_ = {SENT, SENT, SENT, SENT};
    unsigned vo = (unsigned)(OFF_QP + (hq * 8 + j8) * 256 + n16 * 16);
    if (roleA && g4 == 1) vo = (unsigned)(OFF_QA + hq * 1024 + j8 * 256 + n16 * 16);
    if (lane < (roleA ? 32 : 16)) GST4(s_, vo, ws, "sc1");
  }
  if (w < 5) {
    const char* wb = wq + (size_t)(j8 * 80 + 16 * w) * 8192 + hq * 256;
    const unsigned vo = (unsigned)(4 * g4 * 8192 + n16 * 16);
    GLD2(xr, (unsigned)(8 * g4), x + (j8 * 80 + 16 * w) * 2, 0, "");
    GLD4(wqr[0], vo, wb, 0, ""); GLD4(wqr[1], vo, wb + 8192, 0, ""); GLD4(wqr[2], vo, wb + 16384, 0, ""); GLD4(wqr[3], vo, wb + 24576, 0, "");
  }
  if (!roleA && w >= 5) {
    v4u s_ = {SENT, SENT, SENT, SENT};
#pragma unroll
    for (int i = 0; i < 3; ++i) { const int ww = 3 * (w - 5) + i; if (ww < 8) GST4(s_, PP_SLOT(ww), ws, "sc1"); }
    if (w == 7) {
      const int bi_ = xcd * 16 + rr;
      GST4(s_, (unsigned)(OFF_MP + bi_ * 1280 + lane * 16), ws, "sc1");
      unsigned vo = (unsigned)(OFF_MP + bi_ * 1280 + 1024 + lane * 16);
      if (lane >= 16) vo = (unsigned)(OFF_ST + (gB * 32 + sB) * 64 + (lane - 16) * 16);
      if (lane < 20) GST4(s_, vo, ws, "sc1");
    }
  }
  RAWBAR();
  const int kt = (w + 2 * gB) & 7;
  const char* kb = ckv + (size_t)(128 * sB + 16 * kt) * 1024;
  const unsigned kl = L_CKV + (16 * kt) * 1024;
#define CKV_ROWS(I0, I1) \
  _Pragma("unroll") for (int i = I0; i < I1; ++i) dma16((unsigned)((lane ^ i) * 16), kb + i * 1024, kl + i * 1024);
  if (roleA) {
    const char* ub = wuk + (size_t)(128 * j8 + 16 * w) * 8192 + hq * 256;
#pragma unroll
    for (int i = 0; i < 4; ++i)
      dma16((unsigned)((4 * i + g4) * 8192 + ((n16 ^ (4 * i + g4)) * 16)), ub, (unsigned)(L_UK + (16 * w + 4 * i) * 256));
  } else if (w >= 5) {
    CKV_ROWS(0, EARLY_ROWS);
  }
  if (w == 0) {
    if (roleA) VMWAIT(9); else VMWAIT(5);
    TIE(ew);
    const u64 e1 = (((u64)ew.y << 32) | ew.x) + 1;
    v2u f = {(unsigned)e1, (unsigned)(e1 >> 32)};
    if (lane == 0) GST2(f, (unsigned)(OFF_FLAG1 + b * 128), ws, "sc1");
    if (lane == 0) { sts2(L_EP, f); sts1(L_OK, 0u); sts1(L_CNT, 0u); }
  }
  if (w < 5) {
    if (w == 0) { if (roleA) VMWAIT(9); else VMWAIT(5); } else { if (roleA) VMWAIT(8); else VMWAIT(4); }
    TIE(xr);
    const unsigned d0 = __builtin_amdgcn_perm(xr.x, xr.x, 0x01000100u), d1 = __builtin_amdgcn_perm(xr.x, xr.x, 0x03020302u);
    const unsigned d2 = __builtin_amdgcn_perm(xr.y, xr.y, 0x01000100u), d3 = __builtin_amdgcn_perm(xr.y, xr.y, 0x03020302u);
    v4u a0 = {d0 & msk[0], d0 & msk[1], d0 & msk[2], d0 & msk[3]}, a1 = {d1 & msk[0], d1 & msk[1], d1 & msk[2], d1 & msk[3]};
    v4u a2 = {d2 & msk[0], d2 & msk[1], d2 & msk[2], d2 & msk[3]}, a3 = {d3 & msk[0], d3 & msk[1], d3 & msk[2], d3 & msk[3]};
    unsigned qr = L_QRED + w * 512 + (8 * n16 + 4 * g4) * 4;
    TIE(a0); TIE(a1); TIE(a2); TIE(a3); TIE(qr);
    if (w == 0) { if (roleA) VMWAIT(5); else VMWAIT(1); } else { if (roleA) VMWAIT(4); else VMWAIT(0); }
    TIE(wqr[0]); TIE(wqr[1]); TIE(wqr[2]); TIE(wqr[3]);
    f4 acc = {0.f, 0.f, 0.f, 0.f};
    acc = mfma(a0, wqr[0], acc); acc = mfma(a1, wqr[1], acc); acc = mfma(a2, wqr[2], acc); acc = mfma(a3, wqr[3], acc);
    if (lane < 32) stsf4(qr, acc);
  }
  BARRIER();
  if (w == 0) {
    if (lane < 32) {
      const unsigned ra = L_QRED + lane * 16;
      f4 s0 = ldsf4(ra), s1 = ldsf4(ra + 512), s2 = ldsf4(ra + 1024), s3 = ldsf4(ra + 1536), s4_ = ldsf4(ra + 2048);
      f4 sm = (s0 + s1) + (s2 + s3) + s4_;
      v2u o = {guard(cvtpk(sm.x, sm.y)), guard(cvtpk(sm.z, sm.w))};
      GST2(o, (unsigned)(OFF_QP + (hq * 8 + j8) * 256 + lane * 8), ws, "sc1");
    }
  }
#define FLAG1_CHECK(BLK) \
  for (unsigned spin = 0;; ++spin) { \
    v2u f0; \
    GLD2(f0, (unsigned)(OFF_FLAG1 + (BLK) * 128), ws, 0, "sc1"); \
    VMWAIT(0); TIE(f0); \
    const unsigned df = (f0.x ^ (unsigned)epoch) | (f0.y ^ (unsigned)(epoch >> 32)); \
    if (__builtin_amdgcn_ballot_w64(df != 0u) == 0ull || spin > SPIN_CAP) break; \
  }
  u64 epoch;
  { v2u e_ = *(LAS v2u*)(size_t)L_EP; epoch = (((u64)(unsigned)__builtin_amdgcn_readfirstlane(e_.y)) << 32) | (unsigned)__builtin_amdgcn_readfirstlane(e_.x); }

  if (roleA) {
    const int h = hq, j = j8;
    if (w == 7) { const int j8l = lane & 7; const int blk = (h >> 2) + 8 * ((j8l >> 2) * 16 + (h & 3) * 4 + (j8l & 3)); FLAG1_CHECK(blk); CODEPF_DONE(); }
    else VMWAIT(0);
    v4u uk[4];
#pragma unroll
    for (int ks = 0; ks < 4; ++ks) uk[ks] = lds4(L_UK + (16 * w + n16) * 256 + (((4 * ks + g4) ^ n16) * 16));
    unsigned qb_a = L_QB + (n16 & 7) * 288 + g4 * 16;
    unsigned qa_o = (unsigned)(OFF_QA + h * 1024 + (128 * j + 16 * w + 4 * g4) * 2);
    TIE(qb_a); TIE(qa_o);
    if (w == 7) {
      v4u p0, p1;
      const unsigned vo = (unsigned)(OFF_QP + h * 2048 + lane * 16);
      unsigned wa = L_QB + g4 * 288 + n16 * 16; TIE(wa);
      for (unsigned spin = 0;; ++spin) {
        GLD4(p0, vo, ws, 0, "sc1"); GLD4(p1, vo, ws, 1024, "sc1");
        VMWAIT(0); TIE(p0); TIE(p1);
        if (allvalid(umax(mx4(p0), mx4(p1))) || spin > SPIN_CAP) break;
      }
      sts4(wa, p0); sts4(wa + 4 * 288, p1);
    }
    BARRIER();
    {
      v4u bq[4];
#pragma unroll
      for (int ks = 0; ks < 4; ++ks) bq[ks] = lds4(qb_a + ks * 64);
      SCHED();
      f4 acc = {0.f, 0.f, 0.f, 0.f};
#pragma unroll
      for (int ks = 0; ks < 4; ++ks) acc = mfma(uk[ks], bq[ks], acc);
      float v0 = acc.x, v1 = acc.y, v2 = acc.z, v3 = acc.w;
      v0 += dpp<XOR1>(v0); v1 += dpp<XOR1>(v1); v2 += dpp<XOR1>(v2); v3 += dpp<XOR1>(v3);
      v0 += dpp<XOR2>(v0); v1 += dpp<XOR2>(v1); v2 += dpp<XOR2>(v2); v3 += dpp<XOR2>(v3);
      v0 += dpp<HMIR>(v0); v1 += dpp<HMIR>(v1); v2 += dpp<HMIR>(v2); v3 += dpp<HMIR>(v3);
      v2u o = {guard(cvtpk(v0, v1)), guard(cvtpk(v2, v3))};
      if (n16 == 0) GST2(o, qa_o, ws, "sc1");
    }
    const unsigned tpub = now32();
    RAWBAR();
    const int ca = j & 1, db = j >> 1;
    {
      const char* vb = wuv + (size_t)(256 * ca + 32 * w) * 8192 + h * 256 + db * 128;
#pragma unroll
      for (int i = 0; i < 4; ++i)
        dma16((unsigned)((8 * i + (lane >> 3)) * 8192 + (((lane & 7) ^ ((((lane >> 4) & 1) << 1) | ((i & 1) << 2))) * 16)), vb, (unsigned)(L_WUV + (32 * w + 8 * i) * 128));
    }
    if (w == 6) {
      v4u s_ = {SENT, SENT, SENT, SENT};
      GST4(s_, (unsigned)(OFF_OP + (h * 4 + j) * 1280 + lane * 16), ws, "sc1");
      if (lane < 16) GST4(s_, (unsigned)(OFF_OP + (h * 4 + j) * 1280 + 1024 + lane * 16), ws, "sc1");
    }
    {
      unsigned ovo[5];
#pragma unroll
      for (int i = 0; i < 5; ++i) {
        const unsigned n = 64u * (unsigned)(w + 8 * i) + (unsigned)lane, row = (n * 52429u) >> 22, pos = n - row * 80u;
        ovo[i] = row * 1280u + ((pos ^ (((row & 3u) << 1) | (((row >> 3) & 1u) << 3))) * 16u);
      }
      const char* ob = wo + (size_t)(h * 128 + 64 * db) * 1280;
      RAWBAR();
#pragma unroll
      for (int i = 0; i < 5; ++i) dma16(ovo[i], ob + ca * 40960, (unsigned)(L_WO + ca * 40960 + (w + 8 * i) * 1024));
      RAWBAR();
#pragma unroll
      for (int i = 0; i < 5; ++i) dma16(ovo[i], ob + (1 - ca) * 40960, (unsigned)(L_WO + (1 - ca) * 40960 + (w + 8 * i) * 1024));
    }
    unsigned pvo = (unsigned)(OFF_PP + h * 32768 + (8 * ca + w) * 2048 + n16 * 64 + g4 * 16), pvo2 = pvo + 1024u;
    unsigned sto = (unsigned)(OFF_ST + ((h >> 3) * 32 + (lane & 31)) * 64 + (h & 7) * 8);
    unsigned opo = (unsigned)(OFF_OP + (h * 4 + j) * 1280 + 160 * w + 32 * g4 + 2 * n16);
    unsigned cw_w = L_CTX + w * 160 + lane * 2, cw_r = L_CTX + n16 * 160 + g4 * 16;
    unsigned bva[4], woa[5];
    {
      const int q = (lane & 15) >> 2, p = lane & 3;
#pragma unroll
      for (int dt = 0; dt < 4; ++dt)
        bva[dt] = L_WUV + (32 * w + 8 * g4 + q) * 128 + (((2 * dt + (p >> 1)) ^ (((q >> 1) << 1) | ((g4 & 1) << 2))) * 16) + 8 * (p & 1);
#pragma unroll
      for (int t = 0; t < 5; ++t)
        woa[t] = L_WO + (8 * g4 + q) * 1280 + (((10 * w + 2 * t + (p >> 1)) ^ ((q << 1) | ((g4 & 1) << 3))) * 16) + 8 * (p & 1);
    }
    TIE(pvo); TIE(pvo2); TIE(sto); TIE(opo); TIE(cw_w); TIE(cw_r);
#pragma unroll
    for (int t = 0; t < 5; ++t) TIE(woa[t]);
    if (w == 6) VMWAIT(12); else VMWAIT(10);
    v4u bv[4];
#pragma unroll
    for (int dt = 0; dt < 4; ++dt) bv[dt] = ldstr(bva[dt], bva[dt] + 512);
    if (w == 7) {
      for (unsigned spin = 0;; ++spin) {
        v4u f0;
        GLD4(f0, (unsigned)(OFF_FLAG2 + 1024 + lane * 16), ws, 0, "sc1");
        VMWAIT(0); TIE(f0);
        const unsigned lo = (unsigned)epoch, hi = (unsigned)(epoch >> 32);
        const unsigned df = ((f0.x ^ lo) | (f0.y ^ hi)) | ((f0.z ^ lo) | (f0.w ^ hi));
        if (__builtin_amdgcn_ballot_w64(df != 0u) == 0ull || spin > SPIN_CAP) break;
      }
    }
    VMWAIT(0);
    if (w == 6 && lane == 0) { v2u f = {(unsigned)epoch, (unsigned)(epoch >> 32)}; GST2(f, (unsigned)(OFF_FLAG2 + b * 8), ws, "sc1"); }
    BARRIER();
    v4u wa[5][2];
#pragma unroll
    for (int t = 0; t < 5; ++t) { wa[t][0] = ldstr(woa[t], woa[t] + 5120); wa[t][1] = ldstr(woa[t] + 40960, woa[t] + 46080); }
    SCHED();
    v2u st;
    v4u pa[2];
    if (w == 0) {
      if (now32() - tpub < T_LATE) {
        const unsigned nvo = (unsigned)(OFF_NOTE + ((h >> 3) * 32 + (lane & 31)) * 128);
        for (unsigned spin = 0; spin < SPIN_CAP; ++spin) {
          v2u nf;
          GLD2(nf, nvo, ws, 0, "sc1");
          VMWAIT(0); TIE(nf);
          if (__builtin_amdgcn_ballot_w64(nf.x != (unsigned)epoch) == 0ull) {
            float d = (float)(int)(nf.y - tpub);
            d = p16_max(row_max(d));
            wait_until(tpub + (unsigned)__builtin_amdgcn_readfirstlane((int)d) + T_PART);
            break;
          }
        }
      }
      if (lane == 0) sts1(L_OK, 2u);
      GLD2(st, sto, ws, 0, "sc1");
    } else {
      for (unsigned spin = 0; lds1v(L_OK) < 2u && spin < 64u * SPIN_CAP; ++spin) __builtin_amdgcn_s_sleep(1);
    }
    GLD4(pa[0], pvo, ws, 0, "sc1"); GLD4(pa[1], pvo2, ws, 0, "sc1");
    if (w == 0) {
      VMWAIT(2); TIE(st);
      if (__builtin_expect(!allvalid(umax(st.x, st.y)), 0)) {
        v2u s2;
        for (unsigned spin = 0;; ++spin) {
          GLD2(s2, sto, ws, 0, "sc1");
          VMWAIT(0); TIE(s2);
          if (allvalid(umax(s2.x, s2.y)) || spin > SPIN_CAP) break;
        }
        st = s2;
      }
      float m = asf(st.x), l = asf(st.y);
      float M = p16_max(row_max(m));
      float a = __builtin_amdgcn_exp2f((m - M) * SC);
      float Ls = p16_sum(row_sum(a * l));
      a = a * __builtin_amdgcn_rcpf(Ls);
      if (lane < 32) sts1(L_AW + lane * 4, asu(a));
      LGWAIT();
      if (lane == 0) sts1(L_OK, 3u);
    }
    for (unsigned spin = 0; lds1v(L_OK) < 3u && spin < 64u * SPIN_CAP; ++spin) __builtin_amdgcn_s_sleep(1);
    f4 aw[2];
    aw[0] = ldsf4(L_AW + g4 * 16); aw[1] = ldsf4(L_AW + 64 + g4 * 16);
    VMWAIT(0); TIE(pa[0]); TIE(pa[1]);
    if (__builtin_expect(!allvalid(umax(mx4(pa[0]), mx4(pa[1]))), 0)) {
      v4u pb0, pb1;
      for (unsigned spin = 0;; ++spin) {
        GLD4(pb0, pvo, ws, 0, "sc1"); GLD4(pb1, pvo2, ws, 0, "sc1");
        VMWAIT(0); TIE(pb0); TIE(pb1);
        if (allvalid(umax(mx4(pb0), mx4(pb1))) || spin > SPIN_CAP) break;
      }
      pa[0] = pb0; pa[1] = pb1;
    }
    {
      const f4 z = {0.f, 0.f, 0.f, 0.f};
      f4 t0_[4], t1_[4];
#pragma unroll
      for (int dt = 0; dt < 4; ++dt) { t0_[dt] = mfma(pa[0], bv[dt], z); t1_[dt] = mfma(pa[1], bv[dt], z); }
      SCHED();
      float u[4];
#pragma unroll
      for (int dt = 0; dt < 4; ++dt) { f4 c = aw[0] * t0_[dt] + aw[1] * t1_[dt]; u[dt] = (c.x + c.y) + (c.z + c.w); }
      const float c = sum4_scatter(u[0], u[1], u[2], u[3]);
      const unsigned hi = cvtpk(c, c);
      const float rs = c - asf(hi << 16);
      const unsigned lo = cvtpk(rs, rs);
      stsh(cw_w, hi); stsh(cw_w + 8 * 160, lo);
    }
    BARRIER();
    {
      const v4u ca0 = lds4(cw_r), ca1 = lds4(cw_r + 64);
      SCHED();
      const f4 z = {0.f, 0.f, 0.f, 0.f};
      f4 d[5];
#pragma unroll
      for (int t = 0; t < 5; ++t) d[t] = mfma(ca0, wa[t][0], z);
#pragma unroll
      for (int t = 0; t < 5; ++t) d[t] = mfma(ca1, wa[t][1], d[t]);
      SCHED();
      float v[5];
#pragma unroll
      for (int t = 0; t < 5; ++t) v[t] = (d[t].x + d[t].y) + (d[t].z + d[t].w);
      const float va = sum4_scatter(v[0], v[1], v[2], v[3]), vb = p32_sum(p16_sum(v[4]));
      const unsigned pa_ = cvtpk(va, dpp<XOR1>(va)), pb_ = cvtpk(vb, dpp<XOR1>(vb));
      v4u oa = {guard(pa_), guard(dppu<SHL2>(pa_)), guard(dppu<SHL4>(pa_)), guard(dppu<SHL6>(pa_))};
      v4u ob_ = {guard(pb_), guard(dppu<SHL2>(pb_)), guard(dppu<SHL4>(pb_)), guard(dppu<SHL6>(pb_))};
      if ((n16 & 7) == 0) GST4(oa, opo, ws, "sc1");
      if ((n16 & 7) == 0 && g4 == 0) GST4(ob_, opo + 128u, ws, "sc1");
    }
  } else {
    const int g = gB, s = sB, bi = xcd * 16 + rr;
    RAWBAR();
    if (w < 5) {
      CKV_ROWS(0, 16);
    } else {
      CKV_ROWS(EARLY_ROWS, 16);
      v2u f1;
      unsigned f1o;
      { const int hd = 8 * g + ((lane & 31) >> 2); f1o = (unsigned)(OFF_FLAG1 + ((hd >> 2) + 8 * ((hd & 3) * 4 + (lane & 3))) * 128); }
      if (w == 7) GLD2(f1, f1o, ws, 0, "sc1");
      if (w == 5) { dma16((unsigned)(lane * 16), x, L_QB); if (lane < 16) dma16((unsigned)(lane * 16), x + 1024, L_QB + 1024); }
      if (w == 5) VMWAIT(18); else if (w == 6) VMWAIT(16); else VMWAIT(17);
      unsigned old = 0;
      if (lane == 0) old = __hip_atomic_fetch_add((LAS unsigned*)(size_t)L_CNT, 1u, __ATOMIC_RELAXED, __HIP_MEMORY_SCOPE_WORKGROUP);
      if (lane == 0 && old == 2u) { v2u f = {(unsigned)epoch, (unsigned)(epoch >> 32)}; GST2(f, (unsigned)(OFF_FLAG2 + b * 8), ws, "sc1"); }
      if (w == 7) {
        VMWAIT(0); TIE(f1);
        for (unsigned spin = 0; spin < SPIN_CAP; ++spin) {
          const unsigned df = (f1.x ^ (unsigned)epoch) | (f1.y ^ (unsigned)(epoch >> 32));
          if (__builtin_amdgcn_ballot_w64(df != 0u) == 0ull) break;
          GLD2(f1, f1o, ws, 0, "sc1");
          VMWAIT(0); TIE(f1);
        }
        CODEPF_DONE();
        if (lane == 0) sts1(L_OK, 1u);
      }
    }
    unsigned qvo = (unsigned)(OFF_QA + (8 * g + w) * 1024 + lane * 16);
    unsigned qsw = L_QAS + w * 1056 + lane * 16, qsr = L_QAS + (n16 & 7) * 1056 + g4 * 16;
    unsigned mxw = L_MX + lane * 32 + w * 4, mxr = L_MX + (n16 & 7) * 32;
    unsigned pw = L_P + n16 * 288 + (16 * kt + 4 * g4) * 2, pr = L_P + (n16 & 7) * 288 + g4 * 16;
    unsigned lsw = L_LS + lane * 32 + w * 4;
    unsigned ppo = PP_SLOT(w);
    unsigned ka[4], ta[4][2];
    {
      const unsigned kr = L_CKV + (16 * kt + n16) * 1024;
#pragma unroll
      for (int u = 0; u < 4; ++u) ka[u] = kr + (((4 * u + g4) ^ n16) * 16);
      const int q = (lane & 15) >> 2, p = lane & 3;
#pragma unroll
      for (int t = 0; t < 4; ++t) {
        const int ch = 8 * w + 4 * (p >> 1) + t;
        const int r0 = 8 * g4 + q, r1 = r0 + 4;
        ta[t][0] = L_CKV + r0 * 1024 + ((ch ^ (r0 & 15)) * 16) + 8 * (p & 1);
        ta[t][1] = L_CKV + r1 * 1024 + ((ch ^ (r1 & 15)) * 16) + 8 * (p & 1);
      }
    }
    TIE(qvo); TIE(qsw); TIE(qsr); TIE(mxw); TIE(mxr); TIE(pw); TIE(pr); TIE(lsw); TIE(ppo);
#pragma unroll
    for (int u = 0; u < 4; ++u) { TIE(ka[u]); TIE(ta[u][0]); TIE(ta[u][1]); }
    VMWAIT(0);
    for (unsigned spin = 0; lds1v(L_OK) == 0u && spin < SPIN_CAP; ++spin) __builtin_amdgcn_s_sleep(1);
    v4u qa;
    GLD4(qa, qvo, ws, 0, "sc1");
    v4u kA[16];
#pragma unroll
    for (int ks = 0; ks < 16; ++ks) kA[ks] = lds4(ka[ks & 3] + (ks >> 2) * 256);
    BARRIER();
    v4u vA[4][4];
#pragma unroll
    for (int t = 0; t < 4; ++t) {
      vA[0][t] = ldstr(ta[t][0], ta[t][1]); vA[1][t] = ldstr(ta[t][0] + 32768, ta[t][1] + 32768);
      const unsigned b0 = ta[t][0] + 65536, b1 = ta[t][1] + 65536;
      vA[2][t] = ldstr(b0, b1); vA[3][t] = ldstr(b0 + 32768, b1 + 32768);
    }
    SCHED();
    VMWAIT(0); TIE(qa);
    if (__builtin_expect(!allvalid(mx4(qa)), 0)) {
      v4u qb;
      for (unsigned spin = 0;; ++spin) {
        GLD4(qb, qvo, ws, 0, "sc1");
        VMWAIT(0); TIE(qb);
        if (allvalid(mx4(qb)) || spin > SPIN_CAP) break;
      }
      qa = qb;
    }
    sts4(qsw, qa);
    BARRIER();
    if (w == 7) { const unsigned tn = now32(); if (lane == 0) { v2u f = {(unsigned)epoch, tn}; GST2(f, (unsigned)(OFF_NOTE + (g * 32 + s) * 128), ws, "sc1"); } }
    f4 sc = {0.f, 0.f, 0.f, 0.f};
    {
      v4u bq[16];
#pragma unroll
      for (int ks = 0; ks < 16; ++ks) bq[ks] = lds4(qsr + ks * 64);
      SCHED();
#pragma unroll
      for (int ks = 0; ks < 16; ++ks) sc = mfma(kA[ks], bq[ks], sc);
      float mw = p32_max(p16_max(fmx(fmx(sc.x, sc.y), fmx(sc.z, sc.w))));
      if (lane < 8) sts1(mxw, asu(mw));
    }
    BARRIER();
    {
      const float Ms = -max8(ldsf4(mxr), ldsf4(mxr + 16)) * SC;
      float p0 = __builtin_amdgcn_exp2f(fmaf(sc.x, SC, Ms)), p1 = __builtin_amdgcn_exp2f(fmaf(sc.y, SC, Ms)), p2 = __builtin_amdgcn_exp2f(fmaf(sc.z, SC, Ms)), p3 = __builtin_amdgcn_exp2f(fmaf(sc.w, SC, Ms));
      v2u pk = {cvtpk(p0, p1), cvtpk(p2, p3)};
      if (n16 < 8) sts2(pw, pk);
      float ls = p32_sum(p16_sum((p0 + p1) + (p2 + p3)));
      if (lane < 8) sts1(lsw, asu(ls));
    }
    BARRIER();
    {
      v4u pb[4];
#pragma unroll
      for (int ks = 0; ks < 4; ++ks) pb[ks] = lds4(pr + ks * 64);
      f4 lu, lv, mu, mv;
      if (w == 0) { const unsigned sa = (lane & 7) * 32; lu = ldsf4(L_LS + sa); lv = ldsf4(L_LS + sa + 16); mu = ldsf4(L_MX + sa); mv = ldsf4(L_MX + sa + 16); }
      SCHED();
      f4 ac[4];
#pragma unroll
      for (int t = 0; t < 4; ++t) ac[t] = mfma(vA[0][t], pb[0], f4{0.f, 0.f, 0.f, 0.f});
      if (w == 0) {
        v2u o = {guard(asu(max8(mu, mv))), guard(asu(sum8(lu, lv)))};
        if (lane < 8) GST2(o, (unsigned)(OFF_ST + (g * 32 + s) * 64 + lane * 8), ws, "sc1");
      }
#pragma unroll
      for (int ks = 1; ks < 4; ++ks)
#pragma unroll
        for (int t = 0; t < 4; ++t) ac[t] = mfma(vA[ks][t], pb[ks], ac[t]);
      const bool up = n16 >= 8;
      f4 xa = up ? ac[2] : ac[0], ya = up ? ac[3] : ac[1];
      unsigned x0 = cvtpk(xa.x, xa.y), x1 = cvtpk(xa.z, xa.w), y0 = cvtpk(ya.x, ya.y), y1 = cvtpk(ya.z, ya.w);
      auto s0 = __builtin_amdgcn_permlane16_swap(x0, y0, false, false); auto s1 = __builtin_amdgcn_permlane16_swap(x1, y1, false, false);
      unsigned e0 = s0[0], e1 = s1[0], f0 = s0[1], f1 = s1[1];
      v4u o = {guard(e0), guard(e1), guard(f0), guard(f1)};
      GST4(o, ppo, ws, "sc1");
    }
    const unsigned tpv = now32();
    const bool fin = bi < 80 && w == 5;
    unsigned gg[5];
    if (fin) {
#pragma unroll
      for (int i = 0; i < 5; ++i) GLD1(gg[i], (unsigned)(lane * 4), gw, i * 256, "");
    }
    RAWBAR();
    const int fb = bi < 108 ? bi : 107;
    const char* b1 = w1 + fb * 32;
    const unsigned vo1 = (unsigned)((lane >> 1) * 3456 + (lane & 1) * 16);
    const int pq = 5 * (bi & 3);
    int pc0 = pq + 5 + w, pc1 = pq + 13 + w;
    if (pc0 >= 20) pc0 -= 20;
    if (pc1 >= 20) pc1 -= 20;
    if (w < 5) dma16(vo1, b1 + (size_t)(32 * (pq + w)) * 3456, L_W1 + (pq + w) * 1024);
    RAWBAR();
    dma16(vo1, b1 + (size_t)(32 * pc0) * 3456, L_W1 + pc0 * 1024);
    if (w < 7) dma16(vo1, b1 + (size_t)(32 * pc1) * 3456, L_W1 + pc1 * 1024);
    RAWBAR();
    {
      const char* b2 = w2 + (size_t)(16 * fb) * 1280 + w * 1024;
      const unsigned vo2 = (unsigned)(lane * 16);
      dma16(vo2, b2, L_W2 + w * 1024); dma16(vo2, b2 + 8192, L_W2 + (w + 8) * 1024);
      if (w < 4) { dma16(vo2, b2 + 16384, L_W2 + (w + 16) * 1024); VMWAIT(3); } else VMWAIT(2);
    }
    {
      const int q = (lane & 15) >> 2, p = lane & 3;
      const unsigned wa = L_W1 + (8 * g4 + q) * 32 + 8 * p, xa = L_QB + g4 * 16;
      f4 hacc = {0.f, 0.f, 0.f, 0.f};
      hacc = mfma(lds4(xa + pc0 * 64), ldstr(wa + pc0 * 1024, wa + pc0 * 1024 + 128), hacc);
      if (w < 7) hacc = mfma(lds4(xa + pc1 * 64), ldstr(wa + pc1 * 1024, wa + pc1 * 1024 + 128), hacc);
      if (w < 5) { const int p0 = pq + w; hacc = mfma(lds4(xa + p0 * 64), ldstr(wa + p0 * 1024, wa + p0 * 1024 + 128), hacc); }
      if (lane < 16) sts1(L_RED + lane * 32 + w * 4, asu(hacc.x));
    }
    VMWAIT(0);
    BARRIER();
    const unsigned thid = now32();
    if (w < 5) {
      v4u w2r[4];
#pragma unroll
      for (int i = 0; i < 4; ++i) w2r[i] = lds4(L_W2 + (4 * g4 + i) * 1280 + w * 256 + n16 * 16);
      const int f = lane & 15;
      float hh = sum8(ldsf4(L_RED + f * 32), ldsf4(L_RED + f * 32 + 16));
      hh = hh * __builtin_amdgcn_rcpf(1.f + __expf(-hh));
      if (bi >= 108) hh = 0.f;
      unsigned hi = cvtpk(hh, hh);
      float rs = hh - asf(hi << 16);
      unsigned lo = cvtpk(rs, rs);
      if (lane < 32) sts1(L_TAB + w * 512 + lane * 4, lane < 16 ? hi : lo);
      LGWAIT();
      v4u t0 = lds4(L_TAB + w * 512 + ((n16 >> 3) * 16 + 4 * g4) * 4);
      unsigned dv[4] = {t0.x, t0.y, t0.z, t0.w};
      f4 acc = {0.f, 0.f, 0.f, 0.f};
#pragma unroll
      for (int i = 0; i < 4; ++i) { v4u a = {dv[i] & msk[0], dv[i] & msk[1], dv[i] & msk[2], dv[i] & msk[3]}; acc = mfma(a, w2r[i], acc); }
      float o0 = p32_sum(acc.x), o1 = p32_sum(acc.y), o2 = p32_sum(acc.z), o3 = p32_sum(acc.w);
      unsigned a0 = guard(cvtpk(o0, o1)), a1 = guard(cvtpk(o2, o3));
      auto s0 = __builtin_amdgcn_permlane16_swap(a0, a0, false, false); auto s1 = __builtin_amdgcn_permlane16_swap(a1, a1, false, false);
      unsigned b0 = s0[1], b1_ = s1[1];
      v4u o = {a0, a1, b0, b1_};
      if (lane < 16) GST4(o, (unsigned)(OFF_MP + bi * 1280 + w * 256 + lane * 16), ws, "sc1");
    }
    if (fin) {
#pragma unroll
      for (int i = 0; i < 5; ++i) TIE(gg[i]);
      for (unsigned spin = 0;; ++spin) {
        v4u f0, f1;
        GLD4(f0, (unsigned)(OFF_FLAG2 + lane * 16), ws, 0, "sc1"); GLD4(f1, (unsigned)(OFF_FLAG2 + 1024 + lane * 16), ws, 0, "sc1");
        VMWAIT(0); TIE(f0); TIE(f1);
        const unsigned lo = (unsigned)epoch, hi = (unsigned)(epoch >> 32);
        const unsigned df = ((f0.x ^ lo) | (f0.y ^ hi)) | ((f0.z ^ lo) | (f0.w ^ hi)) | ((f1.x ^ lo) | (f1.y ^ hi)) | ((f1.z ^ lo) | (f1.w ^ hi));
        if (__builtin_amdgcn_ballot_w64(df != 0u) == 0ull || spin > SPIN_CAP) break;
      }
      float gate;
      {
        float a = 0.f;
#pragma unroll
        for (int i = 0; i < 5; ++i) { const unsigned gx = *(LAS unsigned*)(size_t)(L_QB + i * 256 + lane * 4); a += asf(gx << 16) * asf(gg[i] << 16) + asf(gx & 0xFFFF0000u) * asf(gg[i] & 0xFFFF0000u); }
        a = p32_sum(p16_sum(row_sum(a)));
        gate = __builtin_amdgcn_rcpf(1.f + __expf(-a));
      }
      const unsigned one = 0x3F803F80u;
      v4u ones = {one & msk[0], one & msk[1], one & msk[2], one & msk[3]};
      {
        const unsigned ta_ = thid + T_MLP, tb_ = tpv + T_OUT;
        wait_until((int)(ta_ - tb_) > 0 ? ta_ : tb_);
      }
      v4u m0, m1, o0_, o1_;
      const unsigned mvo = (unsigned)(OFF_MP + lane * 1280 + bi * 16), mvo2 = mvo + 64 * 1280;
      const unsigned ovo = (unsigned)(OFF_OP + lane * 1280 + bi * 16), ovo2 = ovo + 64 * 1280;
      GLD4(m0, mvo, ws, 0, "sc1"); GLD4(m1, mvo2, ws, 0, "sc1"); GLD4(o0_, ovo, ws, 0, "sc1"); GLD4(o1_, ovo2, ws, 0, "sc1");
      VMWAIT(0); TIE(m0); TIE(m1); TIE(o0_); TIE(o1_);
      if (!allvalid(umax(mx4(m0), mx4(m1)))) {
        v4u a0_, a1_;
        for (unsigned spin = 0;; ++spin) {
          GLD4(a0_, mvo, ws, 0, "sc1"); GLD4(a1_, mvo2, ws, 0, "sc1");
          VMWAIT(0); TIE(a0_); TIE(a1_);
          if (allvalid(umax(mx4(a0_), mx4(a1_))) || spin > SPIN_CAP) break;
        }
        m0 = a0_; m1 = a1_;
      }
      if (!allvalid(umax(mx4(o0_), mx4(o1_)))) {
        v4u a0_, a1_;
        for (unsigned spin = 0;; ++spin) {
          GLD4(a0_, ovo, ws, 0, "sc1"); GLD4(a1_, ovo2, ws, 0, "sc1");
          VMWAIT(0); TIE(a0_); TIE(a1_);
          if (allvalid(umax(mx4(a0_), mx4(a1_))) || spin > SPIN_CAP) break;
        }
        o0_ = a0_; o1_ = a1_;
      }
      f4 bb = {0.f, 0.f, 0.f, 0.f}, aa = {0.f, 0.f, 0.f, 0.f};
      bb = mfma(ones, m0, bb); aa = mfma(ones, o0_, aa); bb = mfma(ones, m1, bb); aa = mfma(ones, o1_, aa);
      bb *= (1.f - gate);
      float r0 = row_sum(fmaf(gate, aa.x, bb.x)), r1 = row_sum(fmaf(gate, aa.y, bb.y));
      float r2 = row_sum(fmaf(gate, aa.z, bb.z)), r3 = row_sum(fmaf(gate, aa.w, bb.w));
      v2u o = {cvtpk(r0, r1), cvtpk(r2, r3)};
      if (n16 == 0 && g4 < 2) GST2(o, (unsigned)(bi * 16 + g4 * 8), out, "");
      if (bi == 0 && lane == 0) {
        v2u f = {(unsigned)epoch, (unsigned)(epoch >> 32)};
        GST2(f, (unsigned)OFF_E, ws, "sc1");
      }
    }
  }
  asm volatile(".Lgm_code_end:" ::: "memory");
}

extern "C" void kernel_launch(void* const* d_in, const int* in_sizes, int n_in,
                              void* d_out, int out_size, void* d_ws, size_t ws_size,
                              hipStream_t stream) {
  gm_kernel<<<256, 512, 0, stream>>>((const char*)d_in[0], (const char*)d_in[1], (const char*)d_in[2], (const char*)d_in[3],
                                      (const char*)d_in[4], (const char*)d_in[5], (const char*)d_in[7], (const char*)d_in[8],
                                      (const char*)d_in[9], (char*)d_out, (char*)d_ws);
}
```
